```python
import torch

def _softmax(x, axis=-1):
    return torch.softmax(x, dim=axis)
def _gelu(x):
    return torch.nn.functional.gelu(x, approximate="tanh")
def _silu(x):
    return torch.nn.functional.silu(x)
def _sigmoid(x):
    return torch.sigmoid(x)
def _layernorm(x, axis=-1, eps=1e-5):
    m = x.mean(dim=axis, keepdim=True)
    v = x.var(dim=axis, keepdim=True, unbiased=False)
    return (x - m) / torch.sqrt(v + eps)
def _scatter_add(tgt, idx, src, axis=0):
    return tgt.clone().index_add_(axis, idx, src)
def _segment_sum(src, seg, n):
    out = torch.zeros((n,) + src.shape[1:], dtype=src.dtype)
    return out.index_add_(0, seg, src)
def _pad(x, pw):
    flat = tuple(v for pair in reversed(pw) for v in pair)
    return torch.nn.functional.pad(x, flat)

kernel_name = 'recurrent_scan__sliding_local_dilated__geglu_ffn__prompt'

def setup_inputs():

    _w = (lambda a: (lambda t: t.to(torch.bfloat16) if t.is_floating_point() else t)(torch.as_tensor(a)))
    def _A_setup():
        B, S, D = 1, 2048, 576
        return {
            "x": _w((lambda *s: torch.randn(*s, dtype=torch.bfloat16) * 0.05)(B, S, D)),
            "wqkv": _w((lambda *s: torch.randn(*s, dtype=torch.bfloat16) * 0.05)(D, 3 * D)),
            "wo": _w((lambda *s: torch.randn(*s, dtype=torch.bfloat16) * 0.05)(D, D)),
        }
    _A_in = _A_setup()

    def _B_setup():
        B, S, D, F = 1, 2048, 576, 4608
        return {
            "x": _w((lambda *s: torch.randn(*s, dtype=torch.bfloat16) * 0.05)(B, S, D)),
            "w_gate": _w((lambda *s: torch.randn(*s, dtype=torch.bfloat16) * 0.05)(D, F)),
            "w_up": _w((lambda *s: torch.randn(*s, dtype=torch.bfloat16) * 0.05)(D, F)),
            "w_down": _w((lambda *s: torch.randn(*s, dtype=torch.bfloat16) * 0.05)(F, D)),
        }
    _B_in = _B_setup()

    return {
        "A_x": _A_in["x"],
        "A_wqkv": _A_in["wqkv"],
        "A_wo": _A_in["wo"],
        "B_x": _B_in["x"],
        "B_w_gate": _B_in["w_gate"],
        "B_w_up": _B_in["w_up"],
        "B_w_down": _B_in["w_down"],
    }

def reference(A_x, A_wqkv, A_wo, B_x, B_w_gate, B_w_up, B_w_down):
    def _A_ref(x, wqkv, wo):
        B, S, D = x.shape
        H = 32; Dh = D // H
        W, dil = 8, 2
        qkv = (lambda x, *d: x.permute(*d))((x @ wqkv).reshape(B, S, 3, H, Dh), 2, 0, 3, 1, 4)
        q, k, v = qkv[0], qkv[1], qkv[2]
        scale = Dh ** -0.5
        pos = torch.arange(S)
        off = torch.arange(W) * dil
        idx = pos[:, None] - off[None, :]
        mask = idx >= 0
        idx_c = torch.where(mask, idx, 0).reshape(-1)
        kg = k[:, :, idx_c, :].reshape(B, H, S, W, Dh)
        vg = v[:, :, idx_c, :].reshape(B, H, S, W, Dh)
        logits = torch.einsum("bhsd,bhswd->bhsw", q, kg) * scale
        logits = torch.where(mask[None, None, :, :], logits, -1e9)
        attn = _softmax(logits, axis=-1)
        o = torch.einsum("bhsw,bhswd->bhsd", attn, vg)
        return (lambda x, *d: x.permute(*d))(o, 0, 2, 1, 3).reshape(B, S, D) @ wo

    def _B_ref(x, w_gate, w_up, w_down):
        return (_gelu(x @ w_gate) * (x @ w_up)) @ w_down

    x = A_x
    xn = _layernorm(x, axis=-1)
    s = _A_ref(xn, A_wqkv, A_wo)
    g = _silu(_B_ref(xn, B_w_gate, B_w_up, B_w_down))
    return x + s * g
```

```cpp
#include <hip/hip_runtime.h>
#include <hip/hip_bf16.h>
typedef __hip_bfloat16 bf16;
#define S_ 2048
#define D_ 576
#define F_ 4608
__device__ __forceinline__ float b2f(bf16 v){ return __bfloat162float(v); }
__device__ __forceinline__ bf16 f2b(float v){ return __float2bfloat16(v); }
__device__ __forceinline__ float rb(float v){ return b2f(f2b(v)); }

__global__ void ln_kernel(const bf16* x, bf16* xn){
  int row = blockIdx.x; int t = threadIdx.x;
  float v[9]; float s=0;
  for(int i=0;i<9;i++){ v[i]=b2f(x[row*D_+t+64*i]); s+=v[i]; }
  for(int o=32;o>0;o>>=1) s+=__shfl_xor(s,o);
  float mean=s/D_;
  float q=0; for(int i=0;i<9;i++){ float d=v[i]-mean; q+=d*d; }
  for(int o=32;o>0;o>>=1) q+=__shfl_xor(q,o);
  float var=q/D_;
  float mb=rb(mean), vb=rb(var);
  float sd=rb(sqrtf(rb(vb+1e-5f)));
  for(int i=0;i<9;i++){ xn[row*D_+t+64*i]=f2b(rb(v[i]-mb)/sd); }
}
__global__ void gemm_kernel(const bf16* A, const bf16* B, bf16* C, int M, int N, int K){
  __shared__ float sA[32][33]; __shared__ float sB[32][33];
  int tx=threadIdx.x, ty=threadIdx.y;
  int row=blockIdx.y*32+ty, col=blockIdx.x*32+tx;
  float acc=0;
  for(int k0=0;k0<K;k0+=32){
    sA[ty][tx]=b2f(A[(long)row*K+k0+tx]);
    sB[ty][tx]=b2f(B[(long)(k0+ty)*N+col]);
    __syncthreads();
    for(int k=0;k<32;k++) acc+=sA[ty][k]*sB[k][tx];
    __syncthreads();
  }
  C[(long)row*N+col]=f2b(acc);
}
__global__ void attn_kernel(const bf16* qkv, bf16* o){
  int idx=blockIdx.x*blockDim.x+threadIdx.x;
  int t=idx>>5, h=idx&31;
  const float scale = 0.23570226039551584f;
  float q[18]; for(int c=0;c<18;c++) q[c]=b2f(qkv[(long)t*1728+h*18+c]);
  float lg[8]; float mx=-1e30f;
  for(int w=0;w<8;w++){
    int tt=t-2*w;
    if(tt>=0){ float a=0; for(int c=0;c<18;c++) a+=q[c]*b2f(qkv[(long)tt*1728+576+h*18+c]); lg[w]=rb(rb(a)*scale); }
    else lg[w]=rb(-1e9f);
    mx=fmaxf(mx,lg[w]);
  }
  float sum=0; float p[8];
  for(int w=0;w<8;w++){ p[w]=__expf(lg[w]-mx); sum+=p[w]; }
  float acc[18]; for(int c=0;c<18;c++) acc[c]=0;
  for(int w=0;w<8;w++){
    int tt=t-2*w; if(tt<0) tt=0;
    float pw=rb(p[w]/sum);
    for(int c=0;c<18;c++) acc[c]+=pw*b2f(qkv[(long)tt*1728+1152+h*18+c]);
  }
  for(int c=0;c<18;c++) o[(long)t*D_+h*18+c]=f2b(acc[c]);
}
__global__ void geglu_kernel(const bf16* g, const bf16* u, bf16* h, long n){
  long i=(long)blockIdx.x*blockDim.x+threadIdx.x; if(i>=n) return;
  float x=b2f(g[i]);
  float inner=0.7978845608028654f*(x+0.044715f*x*x*x);
  float ge=rb(0.5f*x*(1.f+tanhf(inner)));
  h[i]=f2b(ge*b2f(u[i]));
}
__global__ void final_kernel(const bf16* x, const bf16* s, const bf16* d, bf16* out, long n){
  long i=(long)blockIdx.x*blockDim.x+threadIdx.x; if(i>=n) return;
  float dv=b2f(d[i]);
  float g=rb(dv/(1.f+__expf(-dv)));
  float sg=rb(b2f(s[i])*g);
  out[i]=f2b(b2f(x[i])+sg);
}
extern "C" void kernel_launch(void* const* d_in, const int* in_sizes, int n_in,
                              void* d_out, int out_size, void* d_ws, size_t ws_size,
                              hipStream_t stream) {
  const bf16* x=(const bf16*)d_in[0]; const bf16* wqkv=(const bf16*)d_in[1]; const bf16* wo=(const bf16*)d_in[2];
  const bf16* wg=(const bf16*)d_in[4]; const bf16* wu=(const bf16*)d_in[5]; const bf16* wd=(const bf16*)d_in[6];
  bf16* out=(bf16*)d_out;
  char* ws=(char*)d_ws;
  bf16* xn=(bf16*)ws; ws+= (size_t)S_*D_*2;
  bf16* qkv=(bf16*)ws; ws+=(size_t)S_*1728*2;
  bf16* o=(bf16*)ws; ws+=(size_t)S_*D_*2;
  bf16* s=(bf16*)ws; ws+=(size_t)S_*D_*2;
  bf16* g=(bf16*)ws; ws+=(size_t)S_*F_*2;
  bf16* u=(bf16*)ws; ws+=(size_t)S_*F_*2;
  bf16* h=(bf16*)ws; ws+=(size_t)S_*F_*2;
  bf16* dn=(bf16*)ws; ws+=(size_t)S_*D_*2;
  ln_kernel<<<S_,64,0,stream>>>(x,xn);
  dim3 b(32,32);
  gemm_kernel<<<dim3(1728/32,S_/32),b,0,stream>>>(xn,wqkv,qkv,S_,1728,D_);
  attn_kernel<<<S_*32/256,256,0,stream>>>(qkv,o);
  gemm_kernel<<<dim3(D_/32,S_/32),b,0,stream>>>(o,wo,s,S_,D_,D_);
  gemm_kernel<<<dim3(F_/32,S_/32),b,0,stream>>>(xn,wg,g,S_,F_,D_);
  gemm_kernel<<<dim3(F_/32,S_/32),b,0,stream>>>(xn,wu,u,S_,F_,D_);
  long n=(long)S_*F_;
  geglu_kernel<<<(n+255)/256,256,0,stream>>>(g,u,h,n);
  gemm_kernel<<<dim3(D_/32,S_/32),b,0,stream>>>(h,wd,dn,S_,D_,F_);
  long n2=(long)S_*D_;
  final_kernel<<<(n2+255)/256,256,0,stream>>>(x,s,dn,out,n2);
}
```

```cpp
#define GLSPLIT
#define GLA 1
#define GLB 3
#include <hip/hip_runtime.h>
#include <hip/hip_bf16.h>
#include <stdint.h>
typedef __hip_bfloat16 bf16;
typedef __attribute__((ext_vector_type(8))) __bf16 bfx8;
typedef __attribute__((ext_vector_type(2))) __bf16 bfx2;
typedef __attribute__((ext_vector_type(4))) short s16x4;
typedef __attribute__((ext_vector_type(8))) short s16x8;
typedef __attribute__((ext_vector_type(16))) float f32x16;
typedef __attribute__((ext_vector_type(2))) float f32x2;
typedef __attribute__((ext_vector_type(4))) unsigned u32x4;
typedef __attribute__((ext_vector_type(2))) unsigned u32x2;
#define AS3 __attribute__((address_space(3)))
#define DI __device__ __forceinline__

#define S_ 2048
#define D_ 576
#define F_ 4608
#define RING_SLOTS 9
#define SLOT_BYTES 8192
#define H_OFF (RING_SLOTS*SLOT_BYTES)
#define X_OFF 16384
#define LDS_BYTES 163840

DI float b2f(bf16 v){ return __bfloat162float(v); }
DI bf16 f2b(float v){ return __float2bfloat16(v); }
DI float rb(float v){ return b2f(f2b(v)); }

DI unsigned pk_bf16(float lo, float hi){
  f32x2 v = {lo, hi};
  bfx2 r = __builtin_convertvector(v, bfx2);
  return __builtin_bit_cast(unsigned, r);
}
DI void glds16(unsigned voff, const void* sbase, unsigned lds_off){
  unsigned keep;
  asm volatile("s_mov_b32 %0, m0\n\ts_mov_b32 m0, %3\n\ts_nop 0\n\tglobal_load_lds_dwordx4 %1, %2\n\ts_mov_b32 m0, %0"
               : "=&s"(keep) : "v"(voff), "s"(sbase), "s"(lds_off) : "memory");
}
DI bfx8 trread(unsigned addr, int off0, int off1){
  s16x4 a = __builtin_amdgcn_ds_read_tr16_b64_v4i16((AS3 s16x4*)(uintptr_t)(addr + off0));
  s16x4 b = __builtin_amdgcn_ds_read_tr16_b64_v4i16((AS3 s16x4*)(uintptr_t)(addr + off1));
  s16x8 c = __builtin_shufflevector(a, b, 0,1,2,3,4,5,6,7);
  return __builtin_bit_cast(bfx8, c);
}
#define MFMA(a,b,c) __builtin_amdgcn_mfma_f32_32x32x16_bf16((a),(b),(c),0,0,0)

struct KArgs {
  const char* x; const char* wqkv; const char* wo; const char* wg; const char* wu; const char* wd;
  char* qkv; char* part; unsigned long long* ts;
};
#define STAMP(i)

DI float dot2bf(unsigned a, unsigned b, float c){
  return __builtin_amdgcn_fdot2_f32_bf16(__builtin_bit_cast(bfx2, a), __builtin_bit_cast(bfx2, b), c, false);
}
DI void glds16n(unsigned voff, const void* sbase, unsigned lds_off){
  asm volatile("s_mov_b32 m0, %2\n\ts_nop 0\n\tglobal_load_lds_dwordx4 %0, %1"
               :: "v"(voff), "s"(sbase), "s"(lds_off) : "memory");
}
#define SB() __builtin_amdgcn_sched_barrier(0)

typedef __attribute__((ext_vector_type(4))) float f32x4;
#define MFMA16(a,b,c) __builtin_amdgcn_mfma_f32_16x16x32_bf16((a),(b),(c),0,0,0)
#define BC(x) __builtin_bit_cast(bfx8, (x))

template<int NP>
DI void body(const KArgs& a, int m, int s, int nq, int qp0, int wave, int lane) {
  unsigned long long tsv[12], csv[12];
  STAMP(0);
  const int th = wave & 1, cg = wave >> 1;
  const int t16 = lane & 15, g = lane >> 4;
  const int f0 = (s >> 1) * 576 + (s & 1) * 320;
  const unsigned r8 = lane >> 3;
  const unsigned c8 = lane & 7;
  const unsigned rowA = 16 * wave + r8;
  const unsigned c16A1 = (c8 ^ (((r8 >> 1) & 1) << 1)) * 16;
  const unsigned c16B1 = (c8 ^ ((((r8 >> 1) & 1) | 2) << 1)) * 16;
  const unsigned c16P2 = (c8 ^ (((r8 >> 1) & 3) << 1)) * 16;
  const char* lbase; unsigned lstep; unsigned lvoffA, lvoffB; unsigned lslot = 0;
  const unsigned wv2k = wave * 2048;
  auto issue_raw = [&]() {
    glds16n(lvoffA, lbase, lslot + wv2k);
    glds16n(lvoffB, lbase, lslot + wv2k + 1024);
    lslot = (lslot == (RING_SLOTS - 1) * SLOT_BYTES) ? 0u : lslot + SLOT_BYTES;
  };
  const char* pbase = nullptr; unsigned pvA = 0, pvB = 0, pslot = 0;
  auto issue_def = [&]() { pbase = lbase; pvA = lvoffA; pvB = lvoffB; pslot = lslot + wv2k;
    lslot = (lslot == (RING_SLOTS - 1) * SLOT_BYTES) ? 0u : lslot + SLOT_BYTES; };
  auto issue_A = [&]() {
    glds16n(pvA, pbase, pslot);
  };
  auto issue_B = [&]() {
    glds16n(pvB, pbase, pslot + 1024);
  };
  auto p1_desc = [&](int b, const char*& base, unsigned& stride) {
    if (b < nq) { base = a.wqkv + (qp0 + b) * 128; stride = 3456; }
    else { int r = (b - nq) >> 1; base = (((b - nq) & 1) ? a.wu : a.wg) + (f0 + 64 * r) * 2; stride = 9216; }
  };
  {
    unsigned stride; p1_desc(0, lbase, stride);
    lstep = 64 * stride; lvoffA = rowA * stride + c16A1; lvoffB = (rowA + 8) * stride + c16B1;
  }
  {
    #pragma unroll
    for (int i = 0; i < 36; ++i) {
      const int rblk = 4 * wave + (i / 9);
      const int cb = i % 9;
      const int tk = (rblk & 1) * 8 + r8;
      const int sw = (tk >> 1) & 7;
      unsigned voff = (unsigned)(r8 * 1152 + ((c8 ^ sw) * 16));
      glds16n(voff, a.x + (size_t)(m * 128 + rblk * 8) * 1152 + cb * 128, X_OFF + (cb * 16 + rblk) * 1024);
      if (i == 8) { issue_raw(); lbase += lstep; issue_raw(); lbase += lstep; }
    }
  }
  float myrs[4];
  #pragma unroll
  for (int rb4 = 0; rb4 < 4; ++rb4) {
    if (rb4 == 0) asm volatile("s_waitcnt vmcnt(31)" ::: "memory");
    if (rb4 == 1) asm volatile("s_waitcnt vmcnt(18)" ::: "memory");
    if (rb4 == 2) asm volatile("s_waitcnt vmcnt(9)" ::: "memory");
    if (rb4 == 3) asm volatile("s_waitcnt vmcnt(0)" ::: "memory");
    if (rb4 == 0) STAMP(1);
    const unsigned ad = X_OFF + (4 * wave + rb4) * 1024 + lane * 16;
    u32x4 v[9];
    #pragma unroll
    for (int cb = 0; cb < 9; ++cb) v[cb] = *(AS3 u32x4*)(uintptr_t)(ad + cb * 16384);
    float s0 = 0.f, s1 = 0.f;
    #pragma unroll
    for (int cb = 0; cb < 9; ++cb) {
      unsigned u0 = v[cb][0], u1 = v[cb][1], u2 = v[cb][2], u3 = v[cb][3];
      s0 = dot2bf(u0, 0x3f803f80u, s0);
      s1 = dot2bf(u1, 0x3f803f80u, s1);
      s0 = dot2bf(u2, 0x3f803f80u, s0);
      s1 = dot2bf(u3, 0x3f803f80u, s1);
    }
    float sm = s0 + s1;
    sm += __shfl_xor(sm, 1); sm += __shfl_xor(sm, 2); sm += __shfl_xor(sm, 4);
    const float mean = sm * (1.0f / 576.0f);
    float q0 = 0.f, q1 = 0.f;
    #pragma unroll
    for (int cb = 0; cb < 9; ++cb) {
      #pragma unroll
      for (int i = 0; i < 4; ++i) {
        unsigned u = v[cb][i];
        float lo = __uint_as_float(u << 16) - mean;
        float hi = __uint_as_float(u & 0xffff0000u) - mean;
        unsigned d = pk_bf16(lo, hi);
        v[cb][i] = d;
        if (i & 1) q1 = dot2bf(d, d, q1); else q0 = dot2bf(d, d, q0);
      }
      *(AS3 u32x4*)(uintptr_t)(ad + cb * 16384) = v[cb];
    }
    float qs = q0 + q1;
    qs += __shfl_xor(qs, 1); qs += __shfl_xor(qs, 2); qs += __shfl_xor(qs, 4);
    myrs[rb4] = __builtin_amdgcn_rsqf(qs * (1.0f / 576.0f) + 1e-5f);
  }
  asm volatile("s_waitcnt lgkmcnt(0)\n\ts_barrier" ::: "memory");
  STAMP(2);
  u32x4 xb[4][18];
  const unsigned xrd = X_OFF + (th * 8 + (t16 >> 3)) * 1024 + (t16 & 7) * 128;
  const unsigned xsw0 = ((unsigned)(g) ^ ((t16 >> 1) & 7)) * 16, xsw1 = ((unsigned)(4 + g) ^ ((t16 >> 1) & 7)) * 16;
  #pragma unroll
  for (int ks = 0; ks < 8; ++ks)
    #pragma unroll
    for (int tt = 0; tt < 4; ++tt)
      xb[tt][ks] = *(AS3 u32x4*)(uintptr_t)(xrd + ((ks & 1) ? xsw1 : xsw0) + (ks >> 1) * 16384 + tt * 2048);
  asm volatile("s_waitcnt lgkmcnt(0)\n\ts_barrier" ::: "memory");
  #pragma unroll
  for (int i = 0; i < 6; ++i) { issue_raw(); lbase += lstep; }
  if (c8 == 0) {
    #pragma unroll
    for (int rb4 = 0; rb4 < 4; ++rb4) *(AS3 float*)(uintptr_t)(H_OFF + (32 * wave + 8 * rb4 + r8) * 4) = myrs[rb4];
  }
  #pragma unroll
  for (int ks = 8; ks < 18; ++ks)
    #pragma unroll
    for (int tt = 0; tt < 4; ++tt)
      xb[tt][ks] = *(AS3 u32x4*)(uintptr_t)(xrd + ((ks & 1) ? xsw1 : xsw0) + (ks >> 1) * 16384 + tt * 2048);
  #pragma unroll
  for (int ks = 0; ks < 16; ++ks)
    #pragma unroll
    for (int tt = 0; tt < 4; ++tt) asm volatile("" : "+a"(xb[tt][ks]));
  const int q4 = t16 >> 2, p4 = lane & 3;
  const unsigned swl = (unsigned)(((q4 >> 1) & 1) | ((g & 1) << 1));
  const unsigned colp = (((unsigned)(2 * cg) ^ swl) * 32) + 8 * p4;
  const unsigned lb1 = (unsigned)((8 * g + q4) * 128) + colp;
  const unsigned lb2 = (unsigned)((4 * g + q4) * 128) + colp;
  unsigned cslot = 0;
  unsigned ra_cur = 0, ra_nxt = lb1;
  bfx8 af[4];
  af[0] = trread(ra_nxt, 0, 512);
  af[1] = trread(ra_nxt ^ 32, 0, 512);
  af[2] = trread(ra_nxt, 4096, 4096 + 512);

  auto stage_sync = [&](unsigned lb_next) {
    SB();
    asm volatile("s_waitcnt vmcnt(12)\n\ts_barrier" ::: "memory");
    issue_def();
    ra_cur = ra_nxt;
    cslot = (cslot == (RING_SLOTS - 1) * SLOT_BYTES) ? 0u : cslot + SLOT_BYTES;
    ra_nxt = lb_next + cslot;
  };
  const f32x4 zero4 = {0,0,0,0};
  float rs[4] = {1.f, 1.f, 1.f, 1.f};
  const unsigned P2STEP = 64 * 1152;
  const int P2WRAP = 128 - (NP - 1) * 64 * 1152;

  #define GLS(e) { if ((e) == GLA) issue_A(); if ((e) == GLB) issue_B(); }
  #define PREF(e, TOC, TON) \
    ((e) == 0 ? trread(ra_cur ^ 32, 4096, 4096 + (TOC)) : \
     (e) == 1 ? trread(ra_nxt, 0, (TON)) : \
     (e) == 2 ? trread(ra_nxt ^ 32, 0, (TON)) : trread(ra_nxt, 4096, 4096 + (TON)))

  #define STEP(ACC, BF, n, e, first, TOC, TON, SA, SBb, SC) { \
        const int ks_ = (n) >> 1, ft_ = (n) & 1; \
        SB(); ACC[ft_][0] = MFMA16(af[(n) & 3], BC(BF[0][ks_]), (first) ? zero4 : ACC[ft_][0]); SB(); \
        af[((n) + 3) & 3] = PREF(e, TOC, TON); \
        SB(); ACC[ft_][1] = MFMA16(af[(n) & 3], BC(BF[1][ks_]), (first) ? zero4 : ACC[ft_][1]); SB(); \
        SA; \
        SB(); ACC[ft_][2] = MFMA16(af[(n) & 3], BC(BF[2][ks_]), (first) ? zero4 : ACC[ft_][2]); SB(); \
        GLS(e); SBb; \
        SB(); ACC[ft_][3] = MFMA16(af[(n) & 3], BC(BF[3][ks_]), (first) ? zero4 : ACC[ft_][3]); SB(); \
        SC; }

  #define P1_BLOCK(ACC, LASTP1, NB, SA, SBb, SC) { \
    const char* nbase; unsigned nstride; \
    if (LASTP1) { nbase = a.wd + (size_t)f0 * 1152; nstride = 1152; } else p1_desc(NB, nbase, nstride); \
    _Pragma("unroll") for (int j = 0; j < 9; ++j) { \
      stage_sync((LASTP1 && j == 8) ? lb2 : lb1); \
      if (j == 0) { lbase = nbase; lstep = 64 * nstride; \
        lvoffA = rowA * nstride + (LASTP1 ? c16P2 : c16A1); lvoffB = (rowA + 8) * nstride + (LASTP1 ? c16P2 : c16B1); } \
      else if (LASTP1) { lbase += (((j - 1) % NP) == NP - 1) ? P2WRAP : (int)P2STEP; } \
      else lbase += lstep; \
      _Pragma("unroll") for (int e = 0; e < 4; ++e) { \
        const int n = 4 * j + e; \
        STEP(ACC, xb, n, e, n < 2, 512, ((LASTP1 && j == 8) ? 2048 : 512), SA, SBb, SC); \
      } \
    } }
  f32x4 X[2][4], Y[2][4];
  auto q_epilogue = [&](int qp) {
    #pragma unroll
    for (int tt = 0; tt < 4; ++tt) {
      char* dst = a.qkv + (size_t)(m * 128 + th * 64 + tt * 16 + t16) * 3456 + (qp * 64 + cg * 32 + 4 * g) * 2;
      #pragma unroll
      for (int ft = 0; ft < 2; ++ft) {
        u32x2 v;
        v[0] = pk_bf16(X[ft][tt][0] * rs[tt], X[ft][tt][1] * rs[tt]);
        v[1] = pk_bf16(X[ft][tt][2] * rs[tt], X[ft][tt][3] * rs[tt]);
        *(u32x2*)(dst + ft * 32) = v;
      }
    }
  };
  {
    P1_BLOCK(X, false, 1, (void)0, (void)0, (void)0);
    #pragma unroll
    for (int tt = 0; tt < 4; ++tt) rs[tt] = *(AS3 float*)(uintptr_t)(H_OFF + (th * 64 + tt * 16 + t16) * 4);
    q_epilogue(qp0);
  }
  STAMP(3);
  for (int b = 1; b < nq; ++b) {
    P1_BLOCK(X, false, b + 1, (void)0, (void)0, (void)0);
    q_epilogue(qp0 + b);
  }
  const float C0 = -1.5957691216057308f * 1.4426950408889634f;
  const float C1 = C0 * 0.044715f;
  float k0[4], k1[4], k2[4];
  #pragma unroll
  for (int tt = 0; tt < 4; ++tt) { k0[tt] = C0 * rs[tt]; k1[tt] = C1 * rs[tt] * rs[tt] * rs[tt]; k2[tt] = __builtin_amdgcn_rcpf(rs[tt] * rs[tt]); }
  float T[32];
  float gb = 0.f;
  #define GEL(i) X[(i) >> 4][((i) >> 2) & 3][(i) & 3]
  #define UEL(i) Y[(i) >> 4][((i) >> 2) & 3][(i) & 3]
  auto g_a = [&](int i) { const int tt = (i >> 2) & 3;
      float t = T[i] * T[i];
      float w = __builtin_fmaf(t, k1[tt], k0[tt]);
      gb = T[i] * w; asm volatile("" : "+v"(gb)); };
  auto g_b = [&](int i) { const int tt = (i >> 2) & 3;
      gb = __builtin_amdgcn_exp2f(gb);
      gb = __builtin_fmaf(gb, k2[tt], k2[tt]); asm volatile("" : "+v"(gb)); };
  auto g_c = [&](int i) {
      gb = __builtin_amdgcn_rcpf(gb);
      float res = T[i] * gb; asm volatile("" : "+v"(res)); T[i] = res; };
  auto cpy = [&](int i) { float v = GEL(i); asm volatile("" : "+v"(v)); T[i] = v; };
  auto h_tile = [&](int r, int tt) {
      u32x4 hh;
      hh[0] = pk_bf16(T[4 * tt + 0] * UEL(4 * tt + 0), T[4 * tt + 1] * UEL(4 * tt + 1));
      hh[1] = pk_bf16(T[4 * tt + 2] * UEL(4 * tt + 2), T[4 * tt + 3] * UEL(4 * tt + 3));
      hh[2] = pk_bf16(T[16 + 4 * tt + 0] * UEL(16 + 4 * tt + 0), T[16 + 4 * tt + 1] * UEL(16 + 4 * tt + 1));
      hh[3] = pk_bf16(T[16 + 4 * tt + 2] * UEL(16 + 4 * tt + 2), T[16 + 4 * tt + 3] * UEL(16 + 4 * tt + 3));
      unsigned ad = H_OFF + (((th * 4 + tt) * 2 * NP) + 2 * r + cg) * 1024 + lane * 16;
      *(AS3 u32x4*)(uintptr_t)ad = hh;
  };
  #define EORD(o) ((((o) >> 2) & 1) * 16 + ((o) >> 3) * 4 + ((o) & 3))
  auto u_a = [&](int n) { if (n < 4) { cpy(8 * n); cpy(8 * n + 1); cpy(8 * n + 2); } else if ((n & 1) == 0) g_a(EORD((n - 4) >> 1)); else g_c(EORD((n - 4) >> 1)); };
  auto u_b = [&](int n) { if (n < 4) { cpy(8 * n + 3); cpy(8 * n + 4); cpy(8 * n + 5); } };
  auto u_c = [&](int n) { if (n < 4) { cpy(8 * n + 6); cpy(8 * n + 7); } else if ((n & 1) == 0) g_b(EORD((n - 4) >> 1)); };
  auto p_a = [&](int n) { if (n < 32) { if ((n & 1) == 0) g_a(EORD(16 + (n >> 1))); else g_c(EORD(16 + (n >> 1))); } };
  auto p_c = [&](int n) { if (n < 32 && (n & 1) == 0) g_b(EORD(16 + (n >> 1))); };
  #define P_B(r) { if (n == 1) h_tile(r, 0); if (n == 3) h_tile(r, 1); if (n == 17) h_tile(r, 2); if (n == 33) h_tile(r, 3); }
  auto d_a = [&](int n) { if (n < 4) { cpy(8 * n); cpy(8 * n + 1); cpy(8 * n + 2); } else g_a(EORD(n - 4)); };
  auto d_b = [&](int n) { if (n < 4) { cpy(8 * n + 3); cpy(8 * n + 4); cpy(8 * n + 5); } else g_b(EORD(n - 4)); };
  auto d_c = [&](int n) { if (n < 4) { cpy(8 * n + 6); cpy(8 * n + 7); } else g_c(EORD(n - 4)); };

  STAMP(4);
  P1_BLOCK(X, false, nq + 1, (void)0, (void)0, (void)0);
  STAMP(5);
  for (int r = 0; r < NP - 1; ++r) {
    P1_BLOCK(Y, false, nq + 2 * r + 2, u_a(n), u_b(n), u_c(n));
    P1_BLOCK(X, false, nq + 2 * r + 3, p_a(n), P_B(r), p_c(n));
    if (r == 0) STAMP(6);
    if (r == 1) STAMP(7);
  }
  STAMP(8);
  P1_BLOCK(Y, true, 0, d_a(n), d_b(n), d_c(n));
  h_tile(NP - 1, 0); h_tile(NP - 1, 1); h_tile(NP - 1, 2); h_tile(NP - 1, 3);
  asm volatile("s_waitcnt lgkmcnt(0)\n\ts_barrier" ::: "memory");
  STAMP(9);
  u32x4 hb[4][2 * NP];
  #pragma unroll
  for (int tt = 0; tt < 4; ++tt) {
    unsigned ad = H_OFF + ((th * 4 + tt) * 2 * NP) * 1024 + lane * 16;
    #pragma unroll
    for (int i = 0; i < 2 * NP; ++i) hb[tt][i] = *(AS3 u32x4*)(uintptr_t)(ad + i * 1024);
  }
  char* pdst = a.part + ((size_t)(((m * 8 + th * 4) * 18 + cg) * 16 + s)) * 1024 + lane * 16;
  int lstep2a = P2STEP, lstep2b = P2WRAP;
  for (int dp = 0; dp < 9; ++dp) {
    #pragma unroll
    for (int j = 0; j < NP; ++j) {
      stage_sync(lb2);
      lbase += (((j + 8) % NP) == NP - 1) ? lstep2b : lstep2a;
      if (((j + 8 + 1) % NP) == 0 && dp + (j + 9) / NP == 9) { lbase = a.wd; lvoffA = 0; lvoffB = 0; lstep2a = 0; lstep2b = 0; }
      #pragma unroll
      for (int e = 0; e < 4; ++e) {
        const int n = 4 * j + e;
        STEP(X, hb, n, e, n < 2, 2048, 2048, (void)0, (void)0, (void)0);
      }
    }
    #pragma unroll
    for (int tt = 0; tt < 4; ++tt) {
      u32x4 o;
      o[0] = pk_bf16(X[0][tt][0], X[0][tt][1]);
      o[1] = pk_bf16(X[0][tt][2], X[0][tt][3]);
      o[2] = pk_bf16(X[1][tt][0], X[1][tt][1]);
      o[3] = pk_bf16(X[1][tt][2], X[1][tt][3]);
      *(u32x4*)(pdst + tt * (18 * 16 * 1024)) = o;
    }
    pdst += 2 * 16 * 1024;
  }
  STAMP(10);
  asm volatile("s_waitcnt vmcnt(0)" ::: "memory");
  STAMP(11);
}

__global__ __launch_bounds__(256, 1) void k1(KArgs a) {
  __shared__ __attribute__((aligned(16))) char smem[LDS_BYTES];
  asm volatile("" :: "v"((AS3 char*)smem) : "memory");
  const int bid = blockIdx.x;
  const int xcd = bid & 7, idx = bid >> 3;
  const int s = 2 * xcd + (idx & 1), m = idx >> 1;
  const int wave = __builtin_amdgcn_readfirstlane(threadIdx.x >> 6);
  const int lane = threadIdx.x & 63;
  const int j = s >> 1;
  if ((s & 1) == 0) body<5>(a, m, s, 1, j, wave, lane);
  else {
    int nq = j < 3 ? 3 : 2;
    int qp0 = 8 + (j < 3 ? 3 * j : 9 + 2 * (j - 3));
    body<4>(a, m, s, nq, qp0, wave, lane);
  }
}

__global__ void gemm_kernel(const bf16* A, const bf16* B, bf16* C, int M, int N, int K){
  __shared__ float sA[32][33]; __shared__ float sB[32][33];
  int tx=threadIdx.x, ty=threadIdx.y;
  int row=blockIdx.y*32+ty, col=blockIdx.x*32+tx;
  float acc=0;
  for(int k0=0;k0<K;k0+=32){
    sA[ty][tx]=b2f(A[(long)row*K+k0+tx]);
    sB[ty][tx]=b2f(B[(long)(k0+ty)*N+col]);
    __syncthreads();
    for(int k=0;k<32;k++) acc+=sA[ty][k]*sB[k][tx];
    __syncthreads();
  }
  C[(long)row*N+col]=f2b(acc);
}
__global__ void attn_kernel(const bf16* qkv, bf16* o){
  int idx=blockIdx.x*blockDim.x+threadIdx.x;
  int t=idx>>5, h=idx&31;
  const float scale = 0.23570226039551584f;
  float q[18]; for(int c=0;c<18;c++) q[c]=b2f(qkv[(long)t*1728+h*18+c]);
  float lg[8]; float mx=-1e30f;
  for(int w=0;w<8;w++){
    int tt=t-2*w;
    if(tt>=0){ float a=0; for(int c=0;c<18;c++) a+=q[c]*b2f(qkv[(long)tt*1728+576+h*18+c]); lg[w]=rb(rb(a)*scale); }
    else lg[w]=rb(-1e9f);
    mx=fmaxf(mx,lg[w]);
  }
  float sum=0; float p[8];
  for(int w=0;w<8;w++){ p[w]=__expf(lg[w]-mx); sum+=p[w]; }
  float acc[18]; for(int c=0;c<18;c++) acc[c]=0;
  for(int w=0;w<8;w++){
    int tt=t-2*w; if(tt<0) tt=0;
    float pw=rb(p[w]/sum);
    for(int c=0;c<18;c++) acc[c]+=pw*b2f(qkv[(long)tt*1728+1152+h*18+c]);
  }
  for(int c=0;c<18;c++) o[(long)t*D_+h*18+c]=f2b(acc[c]);
}
__global__ __launch_bounds__(256) void k2(const char* part, const char* x, const char* sbuf, char* out){
  const int wid = blockIdx.x * 4 + (threadIdx.x >> 6);
  const int lane = threadIdx.x & 63;
  const int dp = wid % 9, tt = wid / 9;
  const char* p = part + (size_t)wid * 32 * 1024 + lane * 16;
  u32x4 v[32];
  #pragma unroll
  for (int i = 0; i < 32; ++i) v[i] = *(const u32x4*)(p + i * 1024);
  const int t16 = lane & 15, g = lane >> 4;
  const size_t off = (size_t)(tt * 16 + t16) * 1152 + (dp * 64 + 4 * g) * 2;
  u32x2 xv[4], sv[4];
  #pragma unroll
  for (int q = 0; q < 4; ++q) { xv[q] = *(const u32x2*)(x + off + q * 32); sv[q] = *(const u32x2*)(sbuf + off + q * 32); }
  float acc[16];
  #pragma unroll
  for (int i = 0; i < 16; ++i) acc[i] = 0.f;
  #pragma unroll
  for (int hh = 0; hh < 2; ++hh)
    #pragma unroll
    for (int sl = 0; sl < 16; ++sl)
      #pragma unroll
      for (int i = 0; i < 4; ++i) {
        unsigned u = v[hh * 16 + sl][i];
        acc[hh * 8 + 2 * i] += __uint_as_float(u << 16);
        acc[hh * 8 + 2 * i + 1] += __uint_as_float(u & 0xffff0000u);
      }
  #pragma unroll
  for (int q = 0; q < 4; ++q) {
    float r[4];
    #pragma unroll
    for (int i = 0; i < 4; ++i) {
      float d = acc[4 * q + i];
      float gg = d * __builtin_amdgcn_rcpf(1.0f + __builtin_amdgcn_exp2f(-1.4426950408889634f * d));
      unsigned xu = xv[q][i >> 1], su = sv[q][i >> 1];
      float xx = (i & 1) ? __uint_as_float(xu & 0xffff0000u) : __uint_as_float(xu << 16);
      float ss = (i & 1) ? __uint_as_float(su & 0xffff0000u) : __uint_as_float(su << 16);
      r[i] = xx + ss * gg;
    }
    u32x2 o; o[0] = pk_bf16(r[0], r[1]); o[1] = pk_bf16(r[2], r[3]);
    *(u32x2*)(out + off + q * 32) = o;
  }
}

__global__ void empty_k(int* p){ if(p==nullptr) *p=1; }
extern "C" void kernel_launch(void* const* d_in, const int* in_sizes, int n_in,
                              void* d_out, int out_size, void* d_ws, size_t ws_size,
                              hipStream_t stream) {
  char* ws=(char*)d_ws;
  char* qkv = ws; ws += (size_t)S_*1728*2;
  char* o = ws; ws += (size_t)S_*D_*2;
  char* sb = ws; ws += (size_t)S_*D_*2;
  char* part = ws; ws += (size_t)16*S_*D_*2;
  KArgs a;
  a.x=(const char*)d_in[0]; a.wqkv=(const char*)d_in[1]; a.wo=(const char*)d_in[2];
  a.wg=(const char*)d_in[4]; a.wu=(const char*)d_in[5]; a.wd=(const char*)d_in[6];
  a.qkv=qkv; a.part=part; a.ts=(unsigned long long*)((char*)d_ws + (200u<<20));
  k1<<<256,256,0,stream>>>(a);
  attn_kernel<<<S_*32/256,256,0,stream>>>((const bf16*)qkv,(bf16*)o);
  gemm_kernel<<<dim3(D_/32,S_/32),dim3(32,32),0,stream>>>((const bf16*)o,(const bf16*)d_in[2],(bf16*)sb,S_,D_,D_);
  k2<<<288,256,0,stream>>>(part,(const char*)d_in[0],sb,(char*)d_out);
}
```

```cpp
#include <hip/hip_runtime.h>
#include <hip/hip_bf16.h>
#include <stdint.h>
typedef __hip_bfloat16 bf16;
typedef __attribute__((ext_vector_type(8))) __bf16 bfx8;
typedef __attribute__((ext_vector_type(2))) __bf16 bfx2;
typedef __attribute__((ext_vector_type(4))) short s16x4;
typedef __attribute__((ext_vector_type(8))) short s16x8;
typedef __attribute__((ext_vector_type(4))) float f32x4;
typedef __attribute__((ext_vector_type(16))) float f32x16;
typedef __attribute__((ext_vector_type(2))) float f32x2;
typedef __attribute__((ext_vector_type(4))) unsigned u32x4;
typedef __attribute__((ext_vector_type(2))) unsigned u32x2;
#define AS3 __attribute__((address_space(3)))
#define DI __device__ __forceinline__

#define S_ 2048
#define D_ 576
#define F_ 4608
#define RING_SLOTS 9
#define SLOT_BYTES 8192
#define H_OFF (RING_SLOTS*SLOT_BYTES)
#define ATT_OFF (H_OFF + 49152)
#define LDS_BYTES 163840
#define NSL 9
#define PF_CODE
#define PF_W
#define SPIN_MAX (1 << 22)

DI float b2f(bf16 v){ return __bfloat162float(v); }
DI bf16 f2b(float v){ return __float2bfloat16(v); }
DI float rb(float v){ return b2f(f2b(v)); }
DI unsigned pk_bf16(float lo, float hi){
  f32x2 v = {lo, hi};
  bfx2 r = __builtin_convertvector(v, bfx2);
  return __builtin_bit_cast(unsigned, r);
}
DI float dot2bf(unsigned a, unsigned b, float c){
  return __builtin_amdgcn_fdot2_f32_bf16(__builtin_bit_cast(bfx2, a), __builtin_bit_cast(bfx2, b), c, false);
}
DI float bflo(unsigned u){ return __uint_as_float(u << 16); }
DI float bfhi(unsigned u){ return __uint_as_float(u & 0xffff0000u); }
#define SB() __builtin_amdgcn_sched_barrier(0)
#define MFMA16(a,b,c) __builtin_amdgcn_mfma_f32_16x16x32_bf16((a),(b),(c),0,0,0)
#define MFMA32(a,b,c) __builtin_amdgcn_mfma_f32_32x32x16_bf16((a),(b),(c),0,0,0)
#define TR(base, off) __builtin_amdgcn_ds_read_tr16_b64_v4i16((AS3 s16x4*)(uintptr_t)((base) + (off)))
#define AF(i) __builtin_bit_cast(bfx8, __builtin_shufflevector(afl[i], afh[i], 0,1,2,3,4,5,6,7))
#define SETM0(v) asm volatile("s_mov_b32 m0, %0" :: "s"(v) : "memory")
#define GLDS(voff, sbase, off) asm volatile("global_load_lds_dwordx4 %0, %1 offset:" #off :: "v"(voff), "s"(sbase) : "memory")
#define GLDS1(voff, sbase, off) asm volatile("global_load_lds_dwordx4 %0, %1 offset:" #off " sc1" :: "v"(voff), "s"(sbase) : "memory")
#define M32A(acc, a_, b_) asm volatile("v_mfma_f32_32x32x16_bf16 %0, %1, %2, %0" : "+v"(acc) : "v"(a_), "a"(b_))
#define M32V(acc, a_, b_) asm volatile("v_mfma_f32_32x32x16_bf16 %0, %1, %2, %0" : "+v"(acc) : "v"(a_), "v"(b_))
#define M32AS(acc, a_, b_) asm volatile("s_nop 1\n\tv_mfma_f32_32x32x16_bf16 %0, %1, %2, %0" : "+v"(acc) : "v"(a_), "a"(b_))
#define M32VS(acc, a_, b_) asm volatile("s_nop 1\n\tv_mfma_f32_32x32x16_bf16 %0, %1, %2, %0" : "+v"(acc) : "v"(a_), "v"(b_))
#define M32ZA(acc, a_, b_) asm volatile("v_mfma_f32_32x32x16_bf16 %0, %1, %2, 0" : "=&v"(acc) : "v"(a_), "a"(b_))
#define M32ZV(acc, a_, b_) asm volatile("v_mfma_f32_32x32x16_bf16 %0, %1, %2, 0" : "=&v"(acc) : "v"(a_), "v"(b_))
#define M16A(acc, a_, b_) asm volatile("v_mfma_f32_16x16x32_bf16 %0, %1, %2, %0" : "+v"(acc) : "v"(a_), "a"(b_))
#define M16V(acc, a_, b_) asm volatile("v_mfma_f32_16x16x32_bf16 %0, %1, %2, %0" : "+v"(acc) : "v"(a_), "v"(b_))
#define M16ZA(acc, a_, b_) asm volatile("v_mfma_f32_16x16x32_bf16 %0, %1, %2, 0" : "=&v"(acc) : "v"(a_), "a"(b_))
#define M16ZV(acc, a_, b_) asm volatile("v_mfma_f32_16x16x32_bf16 %0, %1, %2, 0" : "=&v"(acc) : "v"(a_), "v"(b_))
#define MNOPX(A_) { SB(); asm volatile("s_nop 15\n\ts_nop 3" : "+v"(A_[0]), "+v"(A_[1]) :: "memory"); SB(); }
#define MNOP() { SB(); asm volatile("s_nop 15\n\ts_nop 3" ::: "memory"); SB(); }
#define BC(x) __builtin_bit_cast(bfx8, (x))
#define PIN(x) asm volatile("" : "+v"(x))
#define LDA(dst, voff, sbase) asm volatile("global_load_dwordx4 %0, %1, %2" : "=a"(dst) : "v"(voff), "s"(sbase) : "memory")
#define LDV(dst, voff, sbase) asm volatile("global_load_dwordx4 %0, %1, %2" : "=v"(dst) : "v"(voff), "s"(sbase) : "memory")
#define LDA1(dst, voff, sbase) asm volatile("global_load_dwordx4 %0, %1, %2 sc1" : "=a"(dst) : "v"(voff), "s"(sbase) : "memory")
#define LDV1(dst, voff, sbase) asm volatile("global_load_dwordx4 %0, %1, %2 sc1" : "=v"(dst) : "v"(voff), "s"(sbase) : "memory")
#define LDW1(dst, voff, sbase) asm volatile("s_nop 4\n\tglobal_load_dword %0, %1, %2 sc1" : "=v"(dst) : "v"(voff), "s"(sbase) : "memory")
#define ST4(vo, val, dst) asm volatile("global_store_dwordx4 %0, %1, %2\n\ts_nop 1" :: "v"(vo), "v"(val), "s"(dst) : "memory")
#define ST41(vo, val, dst) asm volatile("global_store_dwordx4 %0, %1, %2 sc1\n\ts_nop 1" :: "v"(vo), "v"(val), "s"(dst) : "memory")
#define PSTM 1
#define PST ST41
#define ST21(vo, val, dst) asm volatile("global_store_dwordx2 %0, %1, %2 sc1" :: "v"(vo), "v"(val), "s"(dst) : "memory")
#define ST11(vo, val, dst) asm volatile("global_store_dword %0, %1, %2 sc1" :: "v"(vo), "v"(val), "s"(dst) : "memory")

struct KArgs {
  char* ws; const char* x; char* out; const char* wqkv; const char* wo; const char* wg; const char* wu; const char* wd;
};
#define WS_QKV  0u
#define WS_OF   (WS_QKV + 2048u * 1728u * 2u)
#define WS_XN   (WS_OF + 2048u * 576u * 2u)
#define WS_PART (WS_XN + 2048u * 576u * 2u)
#define WS_HX   (WS_PART + NSL * 2048u * 576u * 2u)
#define WS_FLAGS (WS_HX + 2048u * 4608u * 2u)
#define WS_SINK (WS_FLAGS + 65536u)
#define WS_TS   (200u << 20)
#define FLAG_Q(m) ((m) * 32)
#define FLAG_O(m) ((16 + (m)) * 32)
#define FLAG_H(m, j, t) ((32 + ((m) * 8 + (j)) * 2 + (t)) * 32)
#define FLAG_P(m) ((288 + (m) * 3) * 32)
#define NFLAGS 336

#define STAMP(i)

DI unsigned lane_id() { unsigned l; asm volatile("v_mbcnt_lo_u32_b32 %0, -1, 0\n\tv_mbcnt_hi_u32_b32 %0, -1, %0" : "=v"(l)); return l; }
#define JIT(k)
DI void spin_ge(const unsigned* p, unsigned target) {
  for (int it = 0; it < SPIN_MAX; ++it) {
    unsigned v = __hip_atomic_load(p, __ATOMIC_RELAXED, __HIP_MEMORY_SCOPE_AGENT);
    if (__builtin_amdgcn_readfirstlane(v) >= target) break;
    __builtin_amdgcn_s_sleep(2);
  }
}

template<int CTRL> DI float dpp_add(float v){
  int t = __builtin_amdgcn_update_dpp(0, __float_as_int(v), CTRL, 0xf, 0xf, true);
  return v + __int_as_float(t);
}
struct K0Args { const char* x; char* xn; unsigned* flags; const char* wqkv; const char* wg; const char* wu; const char* wo; unsigned* sink; unsigned long long* ts; };
__global__ __launch_bounds__(192) void k0(K0Args ka) {
  const char* x = ka.x; char* xn = ka.xn; unsigned* flags = ka.flags;
  if (threadIdx.x >= 128) {
    const unsigned id = blockIdx.x * 64 + (threadIdx.x - 128);
    unsigned acc = 0;
#define PF_DELAY 16
    __builtin_amdgcn_s_sleep(PF_DELAY);
#define PFM 15
    if ((PFM & 1) && id < 15552u) acc += *(const unsigned*)(ka.wqkv + (size_t)id * 128);
    if (id < 9216u) {
      const unsigned row = id >> 4, sl = id & 15;
      const size_t off = (size_t)row * 9216 + (sl >> 1) * 1152 + (sl & 1) * 640;
      if (PFM & 2) acc += *(const unsigned*)(ka.wg + off);
      if (PFM & 4) acc += *(const unsigned*)(ka.wg + off + 128);
      if (PFM & 8) acc += *(const unsigned*)(ka.wu + off);
      if (PFM & 32) acc += *(const unsigned*)(ka.wu + off + 128);
      if (PFM & 32) acc += *(const unsigned*)(ka.wg + off + 256);
      if (PFM & 32) acc += *(const unsigned*)(ka.wu + off + 256);
    }
    else if ((PFM & 16) && id < 9216u + 5184u) acc += *(const unsigned*)(ka.wo + (size_t)(id - 9216u) * 128);
    if (acc == 0x9e3779b9u) *ka.sink = acc;
    return;
  }
  const int c16 = threadIdx.x & 15;
  const int row = blockIdx.x * 8 + (threadIdx.x >> 4);
  const char* src = x + (size_t)row * 1152 + c16 * 8;
  u32x2 v[9];
  #pragma unroll
  for (int cb = 0; cb < 9; ++cb) v[cb] = *(const u32x2*)(src + cb * 128);
  if (threadIdx.x < 2 && blockIdx.x * 2 + threadIdx.x < NFLAGS) flags[(blockIdx.x * 2 + threadIdx.x) * 32] = 0u;
  float s0 = 0.f, s1 = 0.f;
  #pragma unroll
  for (int cb = 0; cb < 9; ++cb) {
    unsigned u0 = v[cb][0], u1 = v[cb][1];
    s0 = dot2bf(u0, 0x3f803f80u, s0); s1 = dot2bf(u1, 0x3f803f80u, s1);
  }
  float sm = s0 + s1;
  sm = dpp_add<0xB1>(sm); sm = dpp_add<0x4E>(sm); sm = dpp_add<0x141>(sm); sm = dpp_add<0x140>(sm);
  const float mean = sm * (1.0f / 576.0f);
  float q0 = 0.f, q1 = 0.f;
  #pragma unroll
  for (int cb = 0; cb < 9; ++cb)
    #pragma unroll
    for (int i = 0; i < 2; ++i) {
      unsigned u = v[cb][i];
      float lo = bflo(u) - mean, hi = bfhi(u) - mean;
      q0 = __builtin_fmaf(lo, lo, q0); q1 = __builtin_fmaf(hi, hi, q1);
    }
  float qs = q0 + q1;
  qs = dpp_add<0xB1>(qs); qs = dpp_add<0x4E>(qs); qs = dpp_add<0x141>(qs); qs = dpp_add<0x140>(qs);
  const float mb = rb(mean);
  const float sd = rb(__builtin_sqrtf(rb(rb(qs * (1.0f / 576.0f)) + 1e-5f)));
  const float inv = 1.0f / sd;
  const int tile = row >> 5, tok = row & 31;
  char* dst = xn + (size_t)tile * 36864 + tok * 16 + (c16 >> 2) * 1024 + ((c16 >> 1) & 1) * 512 + (c16 & 1) * 8;
  #pragma unroll
  for (int cb = 0; cb < 9; ++cb) {
    u32x2 o;
    #pragma unroll
    for (int i = 0; i < 2; ++i) {
      unsigned u = v[cb][i];
      unsigned d = pk_bf16(bflo(u) - mb, bfhi(u) - mb);
      o[i] = pk_bf16(bflo(d) * inv, bfhi(d) * inv);
    }
    *(u32x2*)(dst + cb * 4096) = o;
  }
}

DI void body0(const KArgs& a, int m, int j, int nq, int qp0, int nw, int wp0, int wm, int wave, const char* code_base, unsigned code_len) {
const int lane = (int)lane_id();
constexpr int NP = 5, TYPE = 0;
JIT(0);
  unsigned long long tsv[16], csv[16];
  STAMP(0);
  const int th = wave & 1, cg = wave >> 1;
  const int t16 = lane & 15, g = lane >> 4;
  const int tok = lane & 31, h = lane >> 5;
  const int s = 2 * j + TYPE;
  const int mp = __builtin_amdgcn_readfirstlane(m > 0 ? m - 1 : 0);
  const int f0 = j * 576 + TYPE * 320;
  const unsigned r8 = lane >> 3, c8 = lane & 7;
  const unsigned rowA = 16 * wave + r8;
  const unsigned c16P1 = (c8 ^ (((r8 >> 1) & 1) << 2)) * 16;
  const char* lbase; unsigned lstep = 0; unsigned lvoffA, lvoffB;
  const unsigned wv2k = wave * 2048;
  const char* const wqb = a.wqkv + qp0 * 128;
  const char* const wgb = a.wg + f0 * 2;
  const char* const wub = a.wu + f0 * 2;
  const char* const wob = a.wo + wp0 * 128;
  const char* wdb = a.wd + (size_t)j * 576 * 1152;
  u32x4 xb[2][36];
  const char* xsrc = (a.ws + WS_XN) + (size_t)(m * 4 + wave) * 36864;
  const char* osrc = (a.ws + WS_OF) + (size_t)(wm * 4 + wave) * 36864;
  const unsigned xo0 = lane * 16;
  const unsigned wv1k = wave * 1024, wv4k = wave * 4096;
  const unsigned xrd = H_OFF + th * 2048 + xo0;
  const unsigned trow = (unsigned)((m * 4 + wave) * 9 + (lane < 8 ? lane : 8));
  #define TOUCH(voff, sbase) asm volatile("s_mov_b32 m0, %2\n\ts_nop 3\n\tglobal_load_lds_dword %0, %1" :: "v"(voff), "s"(sbase), "s"(LDS_BYTES - 1024 + wave * 256) : "memory")
  const int q4 = t16 >> 2, p4 = lane & 3;
  const unsigned lbA = (unsigned)((8 * h + q4) * 128) + (((unsigned)(64 * cg)) ^ ((unsigned)(q4 >> 1) << 6)) + 32 * (g & 1) + 8 * p4;
  s16x4 afl[4], afh[4];
  const f32x4 zero4 = {0,0,0,0};
  const f32x16 zero16 = {0,0,0,0,0,0,0,0,0,0,0,0,0,0,0,0};
  f32x16 X[2], Y[2];
  const unsigned qvo = (unsigned)(h * 1024 + tok * 8);
  const char* qdst = (a.ws + WS_QKV) + ((size_t)(m * 432 + cg * 8) * 128 + th * 64) * 8;
  const float C0 = -1.5957691216057308f * 1.4426950408889634f;
  const float C1 = C0 * 0.044715f;
  float T[32];
  float gt0 = 0.f, gt1 = 0.f, gt2 = 0.f, hy0 = 0.f, hy1 = 0.f, pa = 0.f, pb = 0.f;
  u32x4 hh; u32x2 h2;
  unsigned pw0 = 0, pw1 = 0;
  const unsigned hw_ad = H_OFF + (cg * 8 + th * 4 + (tok >> 4)) * 1024 + (h * 16 + t16) * 16;
  const char* hxb = (a.ws + WS_HX) + (size_t)(m * 8 + j) * 147456;
  const unsigned hxvo = (unsigned)((tok >> 4) * 1024 + (h * 16 + t16) * 16);
  const char* hxw = hxb + (TYPE * 10 + cg) * 8192 + th * 4096;
  const char* hxr4 = hxb + wave * 4096;
  const char* pbase0 = (a.ws + WS_PART) + ((size_t)(((m * 8 + th * 4) * 18 + cg) * NSL + j)) * 1024;
  const char* sbase0 = (a.ws + WS_PART) + ((size_t)(((wm * 8 + wave * 2) * 18) * NSL + 8)) * 1024;
  const unsigned zvo = 0;
  unsigned* const fQ = ((unsigned*)(a.ws + WS_FLAGS)) + FLAG_Q(m);
  unsigned* const fQp = ((unsigned*)(a.ws + WS_FLAGS)) + FLAG_Q(mp);
  unsigned* const fO = ((unsigned*)(a.ws + WS_FLAGS)) + FLAG_O(m);
  unsigned* const fOw = ((unsigned*)(a.ws + WS_FLAGS)) + FLAG_O(wm);
  unsigned* const fP = ((unsigned*)(a.ws + WS_FLAGS)) + FLAG_P(m);
  unsigned* const fPw = ((unsigned*)(a.ws + WS_FLAGS)) + FLAG_P(wm);
  unsigned* const fHme = ((unsigned*)(a.ws + WS_FLAGS)) + FLAG_H(m, j, TYPE);
  unsigned* const fHpa = ((unsigned*)(a.ws + WS_FLAGS)) + FLAG_H(m, j, 1 - TYPE);
  #define SIGNAL_ADD(p, val, cnd) { const unsigned sv_ = (unsigned)(val); \
      asm volatile("s_cmp_eq_u32 %3, 0\n\ts_cselect_b64 exec, 1, 0\n\ts_nop 3\n\tglobal_atomic_add %0, %1, %2\n\ts_mov_b64 exec, -1" :: "v"(zvo), "v"(sv_), "s"(p), "s"(wave) : "memory", "scc"); }
  #define SIGNAL_SET(p, val, cnd) { const unsigned sv_ = (unsigned)(val); \
      asm volatile("s_cmp_eq_u32 %3, 0\n\ts_cselect_b64 exec, 1, 0\n\ts_nop 3\n\tglobal_store_dword %0, %1, %2 sc1\n\ts_mov_b64 exec, -1" :: "v"(zvo), "v"(sv_), "s"(p), "s"(wave) : "memory", "scc"); }
  #define CHECK_GE(w, p, target) { unsigned t1_, t2_; asm volatile( \
      "s_nop 1\n\tv_readfirstlane_b32 %1, %0\n\ts_nop 0\n\ts_cmp_ge_u32 %1, %5\n\ts_cbranch_scc1 .Lck_done%=\n\ts_mov_b32 %2, 0x400000\n" \
      ".Lck_loop%=:\n\ts_sleep 2\n\tglobal_load_dword %0, %3, %4 sc1\n\ts_waitcnt vmcnt(0)\n\tv_readfirstlane_b32 %1, %0\n\ts_nop 0\n\t" \
      "s_cmp_ge_u32 %1, %5\n\ts_cbranch_scc1 .Lck_done%=\n\ts_sub_u32 %2, %2, 1\n\ts_cmp_lg_u32 %2, 0\n\ts_cbranch_scc1 .Lck_loop%=\n.Lck_done%=:" \
      : "+&v"(w), "=&s"(t1_), "=&s"(t2_) : "v"(zvo), "s"(p), "i"(target) : "memory", "scc"); }
  #define CHECK_GE0(w, p, target) { unsigned t1_, t2_; asm volatile( \
      "s_nop 1\n\tv_readfirstlane_b32 %1, %0\n\ts_nop 0\n\ts_cmp_ge_u32 %1, %5\n\ts_cbranch_scc1 .Lcz_done%=\n\ts_mov_b32 %2, 0x400000\n" \
      ".Lcz_loop%=:\n\tglobal_load_dword %0, %3, %4 sc1\n\ts_waitcnt vmcnt(0)\n\tv_readfirstlane_b32 %1, %0\n\ts_nop 0\n\t" \
      "s_cmp_ge_u32 %1, %5\n\ts_cbranch_scc1 .Lcz_done%=\n\ts_sub_u32 %2, %2, 1\n\ts_cmp_lg_u32 %2, 0\n\ts_cbranch_scc1 .Lcz_loop%=\n.Lcz_done%=:" \
      : "+&v"(w), "=&s"(t1_), "=&s"(t2_) : "v"(zvo), "s"(p), "i"(target) : "memory", "scc"); }

const unsigned vA0 = rowA * 3456 + c16P1, vB0 = (rowA + 8) * 3456 + c16P1 - 1024;
const unsigned st1 = 9216; const char* const b1b = wgb;
const unsigned vA1 = rowA * st1 + c16P1, vB1 = (rowA + 8) * st1 + c16P1 - 1024;
asm volatile("s_mov_b32 m0, %0\n\ts_nop 0" :: "s"(wv2k + 0) : "memory"); GLDS(vA0, wqb + 0, 0); GLDS(vB0, wqb + 0, 1024);
asm volatile("s_mov_b32 m0, %0\n\ts_nop 0" :: "s"(wv1k + 73728) : "memory"); GLDS(xo0, xsrc + 0, 0);
asm volatile("s_mov_b32 m0, %0\n\ts_nop 0" :: "s"(wv1k + 77824) : "memory"); GLDS(xo0, xsrc + 1024, 0);
asm volatile("s_mov_b32 m0, %0\n\ts_nop 0" :: "s"(wv1k + 81920) : "memory"); GLDS(xo0, xsrc + 2048, 0);
asm volatile("s_mov_b32 m0, %0\n\ts_nop 0" :: "s"(wv1k + 86016) : "memory"); GLDS(xo0, xsrc + 3072, 0);
asm volatile("s_mov_b32 m0, %0\n\ts_nop 0" :: "s"(wv2k + 8192) : "memory"); GLDS(vA1, b1b + 0 * st1, 0); GLDS(vB1, b1b + 0 * st1, 1024);
asm volatile("s_mov_b32 m0, %0\n\ts_nop 0" :: "s"(wv2k + 16384) : "memory"); GLDS(vA0, wqb + 221184, 0); GLDS(vB0, wqb + 221184, 1024);
asm volatile("s_mov_b32 m0, %0\n\ts_nop 0" :: "s"(wv1k + 90112) : "memory"); GLDS(xo0, xsrc + 4096, 0);
asm volatile("s_mov_b32 m0, %0\n\ts_nop 0" :: "s"(wv1k + 94208) : "memory"); GLDS(xo0, xsrc + 5120, 0);
asm volatile("s_mov_b32 m0, %0\n\ts_nop 0" :: "s"(wv1k + 98304) : "memory"); GLDS(xo0, xsrc + 6144, 0);
asm volatile("s_mov_b32 m0, %0\n\ts_nop 0" :: "s"(wv1k + 102400) : "memory"); GLDS(xo0, xsrc + 7168, 0);
asm volatile("s_mov_b32 m0, %0\n\ts_nop 0" :: "s"(wv2k + 24576) : "memory"); GLDS(vA1, b1b + 64 * st1, 0); GLDS(vB1, b1b + 64 * st1, 1024);
asm volatile("s_mov_b32 m0, %0\n\ts_nop 0" :: "s"(wv2k + 32768) : "memory"); GLDS(vA0, wqb + 442368, 0); GLDS(vB0, wqb + 442368, 1024);
lbase = wqb + 884736; lvoffA = vA0; lvoffB = vB0;
asm volatile("s_waitcnt vmcnt(12)\n\ts_barrier" ::: "memory");
STAMP(1);
afl[0] = TR(lbA, 0); afh[0] = TR(lbA, 512);
afl[1] = TR(lbA, 2048); afh[1] = TR(lbA, 2560);
afl[2] = TR(lbA, 4096); afh[2] = TR(lbA, 4608);
xb[0][0] = *(AS3 u32x4*)(uintptr_t)(xrd + 0);
xb[1][0] = *(AS3 u32x4*)(uintptr_t)(xrd + 1024);
xb[0][1] = *(AS3 u32x4*)(uintptr_t)(xrd + 4096);
xb[1][1] = *(AS3 u32x4*)(uintptr_t)(xrd + 5120);
xb[0][2] = *(AS3 u32x4*)(uintptr_t)(xrd + 8192);
xb[1][2] = *(AS3 u32x4*)(uintptr_t)(xrd + 9216);
xb[0][3] = *(AS3 u32x4*)(uintptr_t)(xrd + 12288);
xb[1][3] = *(AS3 u32x4*)(uintptr_t)(xrd + 13312);
asm volatile("s_mov_b32 m0, %0\n\ts_nop 0" :: "s"(wv1k + 106496) : "memory"); GLDS(xo0, xsrc + 8192, 0);
asm volatile("s_mov_b32 m0, %0\n\ts_nop 0" :: "s"(wv1k + 110592) : "memory"); GLDS(xo0, xsrc + 9216, 0);
asm volatile("s_mov_b32 m0, %0\n\ts_nop 0" :: "s"(wv1k + 114688) : "memory"); GLDS(xo0, xsrc + 10240, 0);
asm volatile("s_mov_b32 m0, %0\n\ts_nop 0" :: "s"(wv1k + 118784) : "memory"); GLDS(xo0, xsrc + 11264, 0);
asm volatile("s_mov_b32 m0, %0\n\ts_nop 0" :: "s"(wv2k + 40960) : "memory"); GLDS(vA1, b1b + 128 * st1, 0); GLDS(vB1, b1b + 128 * st1, 1024);
asm volatile("s_mov_b32 m0, %0\n\ts_nop 0" :: "s"(wv2k + 49152) : "memory"); GLDS(vA0, wqb + 663552, 0); GLDS(vB0, wqb + 663552, 1024);
{
const char* nbase = wub; const unsigned nstride = 9216u;
SB();
asm volatile("s_waitcnt vmcnt(18)\n\ts_barrier" : "+a"(xb[0][0]), "+a"(xb[1][0]), "+a"(xb[0][1]), "+a"(xb[1][1]), "+a"(xb[0][2]), "+a"(xb[1][2]), "+a"(xb[0][3]), "+a"(xb[1][3]) :: "memory");
SB(); M32ZA(Y[0], AF(0), xb[0][0]); SB();
afl[3] = TR(lbA, 6144);
asm volatile("s_mov_b32 m0, %0\n\ts_nop 0" :: "s"(wv1k + 122880) : "memory"); GLDS(xo0, xsrc + 12288, 0);
asm volatile("s_mov_b32 m0, %0\n\ts_nop 0" :: "s"(wv1k + 126976) : "memory"); GLDS(xo0, xsrc + 13312, 0);
SB(); M32ZA(Y[1], AF(0), xb[1][0]); SB();
afh[3] = TR(lbA, 6656);
asm volatile("s_mov_b32 m0, %0\n\ts_nop 0" :: "s"(wv1k + 131072) : "memory"); GLDS(xo0, xsrc + 14336, 0);
asm volatile("s_mov_b32 m0, %0\n\ts_nop 0" :: "s"(wv1k + 135168) : "memory"); GLDS(xo0, xsrc + 15360, 0);
asm volatile("s_mov_b32 m0, %0\n\ts_nop 0" :: "s"(wv2k + 57344) : "memory"); GLDS(vA1, b1b + 192 * st1, 0); GLDS(vB1, b1b + 192 * st1, 1024);
SB(); M32A(Y[0], AF(1), xb[0][1]); SB();
afl[0] = TR(lbA, 8192);
SETM0(wv2k + 65536);
SB(); M32A(Y[1], AF(1), xb[1][1]); SB();
afh[0] = TR(lbA, 8704);
GLDS(lvoffA, lbase, 0);
SB(); M32A(Y[0], AF(2), xb[0][2]); SB();
afl[1] = TR(lbA, 10240);
SB(); M32A(Y[1], AF(2), xb[1][2]); SB();
afh[1] = TR(lbA, 10752);
SB(); M32A(Y[0], AF(3), xb[0][3]); SB();
afl[2] = TR(lbA, 12288);
GLDS(lvoffB, lbase, 1024);
SB(); M32A(Y[1], AF(3), xb[1][3]); SB();
afh[2] = TR(lbA, 12800);
lbase = b1b + 256 * st1; lvoffA = vA1; lvoffB = vB1;
SB();
asm volatile("s_waitcnt vmcnt(20)\n\ts_barrier" ::: "memory");
SB(); M32ZA(X[0], AF(0), xb[0][0]); SB();
afl[3] = TR(lbA, 14336);
asm volatile("s_mov_b32 m0, %0\n\ts_nop 0" :: "s"(wv1k + 139264) : "memory"); GLDS(xo0, xsrc + 16384, 0);
SB(); M32ZA(X[1], AF(0), xb[1][0]); SB();
afh[3] = TR(lbA, 14848);
asm volatile("s_mov_b32 m0, %0\n\ts_nop 0" :: "s"(wv1k + 143360) : "memory"); GLDS(xo0, xsrc + 17408, 0);
xb[0][4] = *(AS3 u32x4*)(uintptr_t)(xrd + 16384);
xb[1][4] = *(AS3 u32x4*)(uintptr_t)(xrd + 17408);
SB(); M32A(X[0], AF(1), xb[0][1]); SB();
afl[0] = TR(lbA, 16384);
SETM0(wv2k + 0);
xb[0][5] = *(AS3 u32x4*)(uintptr_t)(xrd + 20480);
xb[1][5] = *(AS3 u32x4*)(uintptr_t)(xrd + 21504);
SB(); M32A(X[1], AF(1), xb[1][1]); SB();
afh[0] = TR(lbA, 16896);
GLDS(lvoffA, lbase, 0);
xb[0][6] = *(AS3 u32x4*)(uintptr_t)(xrd + 24576);
SB(); M32A(X[0], AF(2), xb[0][2]); SB();
afl[1] = TR(lbA, 18432);
xb[1][6] = *(AS3 u32x4*)(uintptr_t)(xrd + 25600);
xb[0][7] = *(AS3 u32x4*)(uintptr_t)(xrd + 28672);
SB(); M32A(X[1], AF(2), xb[1][2]); SB();
afh[1] = TR(lbA, 18944);
xb[1][7] = *(AS3 u32x4*)(uintptr_t)(xrd + 29696);
SB(); M32A(X[0], AF(3), xb[0][3]); SB();
afl[2] = TR(lbA, 20480);
GLDS(lvoffB, lbase, 1024);
SB(); M32A(X[1], AF(3), xb[1][3]); SB();
afh[2] = TR(lbA, 20992);
lbase = wqb + 1105920; lvoffA = vA0; lvoffB = vB0;
SB();
asm volatile("s_waitcnt vmcnt(22)\n\ts_barrier" : "+a"(xb[0][4]), "+a"(xb[1][4]), "+a"(xb[0][5]), "+a"(xb[1][5]), "+a"(xb[0][6]), "+a"(xb[1][6]), "+a"(xb[0][7]), "+a"(xb[1][7]) :: "memory");
SB(); M32A(Y[0], AF(0), xb[0][4]); SB();
afl[3] = TR(lbA, 22528);
asm volatile("s_mov_b32 m0, %0\n\ts_nop 0" :: "s"(wv1k + 147456) : "memory"); GLDS(xo0, xsrc + 18432, 0);
SB(); M32A(Y[1], AF(0), xb[1][4]); SB();
afh[3] = TR(lbA, 23040);
asm volatile("s_mov_b32 m0, %0\n\ts_nop 0" :: "s"(wv1k + 151552) : "memory"); GLDS(xo0, xsrc + 19456, 0);
SB(); M32A(Y[0], AF(1), xb[0][5]); SB();
afl[0] = TR(lbA, 24576);
SETM0(wv2k + 8192);
SB(); M32A(Y[1], AF(1), xb[1][5]); SB();
afh[0] = TR(lbA, 25088);
GLDS(lvoffA, lbase, 0);
SB(); M32A(Y[0], AF(2), xb[0][6]); SB();
afl[1] = TR(lbA, 26624);
SB(); M32A(Y[1], AF(2), xb[1][6]); SB();
afh[1] = TR(lbA, 27136);
SB(); M32A(Y[0], AF(3), xb[0][7]); SB();
afl[2] = TR(lbA, 28672);
GLDS(lvoffB, lbase, 1024);
SB(); M32A(Y[1], AF(3), xb[1][7]); SB();
afh[2] = TR(lbA, 29184);
lbase = wqb + 1327104; lvoffA = vA0; lvoffB = vB0;
  {
    unsigned off = ((unsigned)((m * 2 + TYPE) * 4 + wave) & 15u) * 8192u + (unsigned)lane * 128u;
    off = off < code_len ? off : 0u;
    asm volatile("s_mov_b32 m0, %2\n\ts_nop 4\n\tglobal_load_lds_dword %0, %1" :: "v"(off), "s"(code_base), "s"(wv1k + H_OFF + 4 * 4096) : "memory");
  }
SB();
asm volatile("s_waitcnt vmcnt(21)\n\ts_barrier" ::: "memory");
SB(); M32A(X[0], AF(0), xb[0][4]); SB();
afl[3] = TR(lbA, 30720);
asm volatile("s_mov_b32 m0, %0\n\ts_nop 0" :: "s"(wv1k + 155648) : "memory"); GLDS(xo0, xsrc + 20480, 0);
SB(); M32A(X[1], AF(0), xb[1][4]); SB();
afh[3] = TR(lbA, 31232);
asm volatile("s_mov_b32 m0, %0\n\ts_nop 0" :: "s"(wv1k + 159744) : "memory"); GLDS(xo0, xsrc + 21504, 0);
xb[0][8] = *(AS3 u32x4*)(uintptr_t)(xrd + 32768);
xb[1][8] = *(AS3 u32x4*)(uintptr_t)(xrd + 33792);
SB(); M32A(X[0], AF(1), xb[0][5]); SB();
afl[0] = TR(lbA, 32768);
SETM0(wv2k + 16384);
xb[0][9] = *(AS3 u32x4*)(uintptr_t)(xrd + 36864);
xb[1][9] = *(AS3 u32x4*)(uintptr_t)(xrd + 37888);
SB(); M32A(X[1], AF(1), xb[1][5]); SB();
afh[0] = TR(lbA, 33280);
GLDS(lvoffA, lbase, 0);
xb[0][10] = *(AS3 u32x4*)(uintptr_t)(xrd + 40960);
SB(); M32A(X[0], AF(2), xb[0][6]); SB();
afl[1] = TR(lbA, 34816);
xb[1][10] = *(AS3 u32x4*)(uintptr_t)(xrd + 41984);
xb[0][11] = *(AS3 u32x4*)(uintptr_t)(xrd + 45056);
SB(); M32A(X[1], AF(2), xb[1][6]); SB();
afh[1] = TR(lbA, 35328);
xb[1][11] = *(AS3 u32x4*)(uintptr_t)(xrd + 46080);
SB(); M32A(X[0], AF(3), xb[0][7]); SB();
afl[2] = TR(lbA, 36864);
GLDS(lvoffB, lbase, 1024);
SB(); M32A(X[1], AF(3), xb[1][7]); SB();
afh[2] = TR(lbA, 37376);
lbase = b1b + 320 * st1; lvoffA = vA1; lvoffB = vB1;
SB();
asm volatile("s_waitcnt vmcnt(23)\n\ts_barrier" : "+a"(xb[0][8]), "+a"(xb[1][8]), "+a"(xb[0][9]), "+a"(xb[1][9]), "+a"(xb[0][10]), "+a"(xb[1][10]), "+a"(xb[0][11]), "+a"(xb[1][11]) :: "memory");
SB(); M32A(Y[0], AF(0), xb[0][8]); SB();
afl[3] = TR(lbA, 38912);
asm volatile("s_mov_b32 m0, %0\n\ts_nop 0" :: "s"(wv1k + 73728) : "memory"); GLDS(xo0, xsrc + 22528, 0);
SB(); M32A(Y[1], AF(0), xb[1][8]); SB();
afh[3] = TR(lbA, 39424);
asm volatile("s_mov_b32 m0, %0\n\ts_nop 0" :: "s"(wv1k + 77824) : "memory"); GLDS(xo0, xsrc + 23552, 0);
SB(); M32A(Y[0], AF(1), xb[0][9]); SB();
afl[0] = TR(lbA, 40960);
SETM0(wv2k + 24576);
SB(); M32A(Y[1], AF(1), xb[1][9]); SB();
afh[0] = TR(lbA, 41472);
GLDS(lvoffA, lbase, 0);
SB(); M32A(Y[0], AF(2), xb[0][10]); SB();
afl[1] = TR(lbA, 43008);
SB(); M32A(Y[1], AF(2), xb[1][10]); SB();
afh[1] = TR(lbA, 43520);
SB(); M32A(Y[0], AF(3), xb[0][11]); SB();
afl[2] = TR(lbA, 45056);
GLDS(lvoffB, lbase, 1024);
SB(); M32A(Y[1], AF(3), xb[1][11]); SB();
afh[2] = TR(lbA, 45568);
lbase = wqb + 1548288; lvoffA = vA0; lvoffB = vB0;
SB();
asm volatile("s_waitcnt vmcnt(21)\n\ts_barrier" ::: "memory");
SB(); M32A(X[0], AF(0), xb[0][8]); SB();
afl[3] = TR(lbA, 47104);
asm volatile("s_mov_b32 m0, %0\n\ts_nop 0" :: "s"(wv1k + 81920) : "memory"); GLDS(xo0, xsrc + 24576, 0);
SB(); M32A(X[1], AF(0), xb[1][8]); SB();
afh[3] = TR(lbA, 47616);
asm volatile("s_mov_b32 m0, %0\n\ts_nop 0" :: "s"(wv1k + 86016) : "memory"); GLDS(xo0, xsrc + 25600, 0);
xb[0][12] = *(AS3 u32x4*)(uintptr_t)(xrd + 49152);
xb[1][12] = *(AS3 u32x4*)(uintptr_t)(xrd + 50176);
SB(); M32A(X[0], AF(1), xb[0][9]); SB();
afl[0] = TR(lbA, 49152);
SETM0(wv2k + 32768);
xb[0][13] = *(AS3 u32x4*)(uintptr_t)(xrd + 53248);
xb[1][13] = *(AS3 u32x4*)(uintptr_t)(xrd + 54272);
SB(); M32A(X[1], AF(1), xb[1][9]); SB();
afh[0] = TR(lbA, 49664);
GLDS(lvoffA, lbase, 0);
xb[0][14] = *(AS3 u32x4*)(uintptr_t)(xrd + 57344);
SB(); M32A(X[0], AF(2), xb[0][10]); SB();
afl[1] = TR(lbA, 51200);
xb[1][14] = *(AS3 u32x4*)(uintptr_t)(xrd + 58368);
xb[0][15] = *(AS3 u32x4*)(uintptr_t)(xrd + 61440);
SB(); M32A(X[1], AF(2), xb[1][10]); SB();
afh[1] = TR(lbA, 51712);
xb[1][15] = *(AS3 u32x4*)(uintptr_t)(xrd + 62464);
SB(); M32A(X[0], AF(3), xb[0][11]); SB();
afl[2] = TR(lbA, 53248);
GLDS(lvoffB, lbase, 1024);
SB(); M32A(X[1], AF(3), xb[1][11]); SB();
afh[2] = TR(lbA, 53760);
lbase = wqb + 1769472; lvoffA = vA0; lvoffB = vB0;
SB();
asm volatile("s_waitcnt vmcnt(23)\n\ts_barrier" : "+a"(xb[0][12]), "+a"(xb[1][12]), "+a"(xb[0][13]), "+a"(xb[1][13]), "+a"(xb[0][14]), "+a"(xb[1][14]), "+a"(xb[0][15]), "+a"(xb[1][15]) :: "memory");
SB(); M32A(Y[0], AF(0), xb[0][12]); SB();
afl[3] = TR(lbA, 55296);
asm volatile("s_mov_b32 m0, %0\n\ts_nop 0" :: "s"(wv1k + 90112) : "memory"); GLDS(xo0, xsrc + 26624, 0);
SB(); M32A(Y[1], AF(0), xb[1][12]); SB();
afh[3] = TR(lbA, 55808);
asm volatile("s_mov_b32 m0, %0\n\ts_nop 0" :: "s"(wv1k + 94208) : "memory"); GLDS(xo0, xsrc + 27648, 0);
SB(); M32A(Y[0], AF(1), xb[0][13]); SB();
afl[0] = TR(lbA, 57344);
SETM0(wv2k + 40960);
SB(); M32A(Y[1], AF(1), xb[1][13]); SB();
afh[0] = TR(lbA, 57856);
GLDS(lvoffA, lbase, 0);
SB(); M32A(Y[0], AF(2), xb[0][14]); SB();
afl[1] = TR(lbA, 59392);
SB(); M32A(Y[1], AF(2), xb[1][14]); SB();
afh[1] = TR(lbA, 59904);
SB(); M32A(Y[0], AF(3), xb[0][15]); SB();
afl[2] = TR(lbA, 61440);
GLDS(lvoffB, lbase, 1024);
SB(); M32A(Y[1], AF(3), xb[1][15]); SB();
afh[2] = TR(lbA, 61952);
lbase = b1b + 384 * st1; lvoffA = vA1; lvoffB = vB1;
SB();
asm volatile("s_waitcnt vmcnt(19)\n\ts_barrier" ::: "memory");
SB(); M32A(X[0], AF(0), xb[0][12]); SB();
afl[3] = TR(lbA, 63488);
asm volatile("s_mov_b32 m0, %0\n\ts_nop 0" :: "s"(wv1k + 98304) : "memory"); GLDS(xo0, xsrc + 28672, 0);
SB(); M32A(X[1], AF(0), xb[1][12]); SB();
afh[3] = TR(lbA, 64000);
asm volatile("s_mov_b32 m0, %0\n\ts_nop 0" :: "s"(wv1k + 102400) : "memory"); GLDS(xo0, xsrc + 29696, 0);
xb[0][16] = *(AS3 u32x4*)(uintptr_t)(xrd + 65536);
xb[1][16] = *(AS3 u32x4*)(uintptr_t)(xrd + 66560);
SB(); M32A(X[0], AF(1), xb[0][13]); SB();
afl[0] = TR(lbA, 65536);
SETM0(wv2k + 49152);
xb[0][17] = *(AS3 u32x4*)(uintptr_t)(xrd + 69632);
xb[1][17] = *(AS3 u32x4*)(uintptr_t)(xrd + 70656);
SB(); M32A(X[1], AF(1), xb[1][13]); SB();
afh[0] = TR(lbA, 66048);
GLDS(lvoffA, lbase, 0);
xb[0][18] = *(AS3 u32x4*)(uintptr_t)(xrd + 73728);
SB(); M32A(X[0], AF(2), xb[0][14]); SB();
afl[1] = TR(lbA, 67584);
xb[1][18] = *(AS3 u32x4*)(uintptr_t)(xrd + 74752);
xb[0][19] = *(AS3 u32x4*)(uintptr_t)(xrd + 77824);
SB(); M32A(X[1], AF(2), xb[1][14]); SB();
afh[1] = TR(lbA, 68096);
xb[1][19] = *(AS3 u32x4*)(uintptr_t)(xrd + 78848);
SB(); M32A(X[0], AF(3), xb[0][15]); SB();
afl[2] = TR(lbA, 69632);
GLDS(lvoffB, lbase, 1024);
SB(); M32A(X[1], AF(3), xb[1][15]); SB();
afh[2] = TR(lbA, 70144);
lbase = b1b + 448 * st1; lvoffA = vA1; lvoffB = vB1;
SB();
asm volatile("s_waitcnt vmcnt(25)\n\ts_barrier" : "+a"(xb[0][16]), "+a"(xb[1][16]), "+a"(xb[0][17]), "+a"(xb[1][17]), "+a"(xb[0][18]), "+a"(xb[1][18]), "+a"(xb[0][19]), "+a"(xb[1][19]) :: "memory");
SB(); M32A(Y[0], AF(0), xb[0][16]); SB();
afl[3] = TR(lbA, 71680);
asm volatile("s_mov_b32 m0, %0\n\ts_nop 0" :: "s"(wv1k + 106496) : "memory"); GLDS(xo0, xsrc + 30720, 0);
SB(); M32A(Y[1], AF(0), xb[1][16]); SB();
afh[3] = TR(lbA, 72192);
asm volatile("s_mov_b32 m0, %0\n\ts_nop 0" :: "s"(wv1k + 110592) : "memory"); GLDS(xo0, xsrc + 31744, 0);
SB(); M32A(Y[0], AF(1), xb[0][17]); SB();
afl[0] = TR(lbA, 0);
SETM0(wv2k + 57344);
SB(); M32A(Y[1], AF(1), xb[1][17]); SB();
afh[0] = TR(lbA, 512);
GLDS(lvoffA, lbase, 0);
SB(); M32A(Y[0], AF(2), xb[0][18]); SB();
afl[1] = TR(lbA, 2048);
SB(); M32A(Y[1], AF(2), xb[1][18]); SB();
afh[1] = TR(lbA, 2560);
SB(); M32A(Y[0], AF(3), xb[0][19]); SB();
afl[2] = TR(lbA, 4096);
GLDS(lvoffB, lbase, 1024);
SB(); M32A(Y[1], AF(3), xb[1][19]); SB();
afh[2] = TR(lbA, 4608);
lbase = b1b + 512 * st1; lvoffA = vA1; lvoffB = vB1;
STAMP(2);
SB();
asm volatile("s_waitcnt vmcnt(18)\n\ts_barrier" ::: "memory");
SB(); M32A(X[0], AF(0), xb[0][16]); SB();
afl[3] = TR(lbA, 6144);
asm volatile("s_mov_b32 m0, %0\n\ts_nop 0" :: "s"(wv1k + 114688) : "memory"); GLDS(xo0, xsrc + 32768, 0);
SB(); M32A(X[1], AF(0), xb[1][16]); SB();
afh[3] = TR(lbA, 6656);
asm volatile("s_mov_b32 m0, %0\n\ts_nop 0" :: "s"(wv1k + 118784) : "memory"); GLDS(xo0, xsrc + 33792, 0);
xb[0][20] = *(AS3 u32x4*)(uintptr_t)(xrd + 81920);
xb[1][20] = *(AS3 u32x4*)(uintptr_t)(xrd + 82944);
SB(); M32A(X[0], AF(1), xb[0][17]); SB();
afl[0] = TR(lbA, 8192);
SETM0(wv2k + 65536);
xb[0][21] = *(AS3 u32x4*)(uintptr_t)(xrd + 86016);
xb[1][21] = *(AS3 u32x4*)(uintptr_t)(xrd + 87040);
SB(); M32A(X[1], AF(1), xb[1][17]); SB();
afh[0] = TR(lbA, 8704);
GLDS(lvoffA, lbase, 0);
xb[0][22] = *(AS3 u32x4*)(uintptr_t)(xrd + 0);
SB(); M32A(X[0], AF(2), xb[0][18]); SB();
afl[1] = TR(lbA, 10240);
xb[1][22] = *(AS3 u32x4*)(uintptr_t)(xrd + 1024);
xb[0][23] = *(AS3 u32x4*)(uintptr_t)(xrd + 4096);
SB(); M32A(X[1], AF(2), xb[1][18]); SB();
afh[1] = TR(lbA, 10752);
xb[1][23] = *(AS3 u32x4*)(uintptr_t)(xrd + 5120);
SB(); M32A(X[0], AF(3), xb[0][19]); SB();
afl[2] = TR(lbA, 12288);
GLDS(lvoffB, lbase, 1024);
SB(); M32A(X[1], AF(3), xb[1][19]); SB();
afh[2] = TR(lbA, 12800);
lbase = nbase; lstep = 64 * nstride; lvoffA = rowA * nstride + c16P1; lvoffB = (rowA + 8) * nstride + c16P1 - 1024;
SB();
asm volatile("s_waitcnt vmcnt(14)\n\ts_barrier" : "+a"(xb[0][20]), "+a"(xb[1][20]), "+a"(xb[0][21]), "+a"(xb[1][21]), "+a"(xb[0][22]), "+a"(xb[1][22]), "+a"(xb[0][23]), "+a"(xb[1][23]) :: "memory");
SB(); M32A(Y[0], AF(0), xb[0][20]); SB();
afl[3] = TR(lbA, 14336);
asm volatile("s_mov_b32 m0, %0\n\ts_nop 0" :: "s"(wv1k + 122880) : "memory"); GLDS(xo0, xsrc + 34816, 0);
SB(); M32A(Y[1], AF(0), xb[1][20]); SB();
afh[3] = TR(lbA, 14848);
asm volatile("s_mov_b32 m0, %0\n\ts_nop 0" :: "s"(wv1k + 126976) : "memory"); GLDS(xo0, xsrc + 35840, 0);
xb[0][24] = *(AS3 u32x4*)(uintptr_t)(xrd + 8192);
xb[1][24] = *(AS3 u32x4*)(uintptr_t)(xrd + 9216);
SB(); M32A(Y[0], AF(1), xb[0][21]); SB();
afl[0] = TR(lbA, 16384);
SETM0(wv2k + 0);
xb[0][25] = *(AS3 u32x4*)(uintptr_t)(xrd + 12288);
xb[1][25] = *(AS3 u32x4*)(uintptr_t)(xrd + 13312);
SB(); M32A(Y[1], AF(1), xb[1][21]); SB();
afh[0] = TR(lbA, 16896);
GLDS(lvoffA, lbase, 0);
xb[0][26] = *(AS3 u32x4*)(uintptr_t)(xrd + 16384);
SB(); M32A(Y[0], AF(2), xb[0][22]); SB();
afl[1] = TR(lbA, 18432);
xb[1][26] = *(AS3 u32x4*)(uintptr_t)(xrd + 17408);
xb[0][27] = *(AS3 u32x4*)(uintptr_t)(xrd + 20480);
SB(); M32A(Y[1], AF(2), xb[1][22]); SB();
afh[1] = TR(lbA, 18944);
xb[1][27] = *(AS3 u32x4*)(uintptr_t)(xrd + 21504);
SB(); M32A(Y[0], AF(3), xb[0][23]); SB();
afl[2] = TR(lbA, 20480);
GLDS(lvoffB, lbase, 1024);
SB(); M32A(Y[1], AF(3), xb[1][23]); SB();
afh[2] = TR(lbA, 20992);
lbase += lstep;
SB();
asm volatile("s_waitcnt vmcnt(24)\n\ts_barrier" : "+a"(xb[0][24]), "+a"(xb[1][24]), "+a"(xb[0][25]), "+a"(xb[1][25]), "+a"(xb[0][26]), "+a"(xb[1][26]), "+a"(xb[0][27]), "+a"(xb[1][27]) :: "memory");
SB(); M32A(Y[0], AF(0), xb[0][24]); SB();
afl[3] = TR(lbA, 22528);
SB(); M32A(Y[1], AF(0), xb[1][24]); SB();
afh[3] = TR(lbA, 23040);
SB(); M32A(Y[0], AF(1), xb[0][25]); SB();
afl[0] = TR(lbA, 24576);
SETM0(wv2k + 8192);
SB(); M32A(Y[1], AF(1), xb[1][25]); SB();
afh[0] = TR(lbA, 25088);
GLDS(lvoffA, lbase, 0);
SB(); M32A(Y[0], AF(2), xb[0][26]); SB();
afl[1] = TR(lbA, 26624);
SB(); M32A(Y[1], AF(2), xb[1][26]); SB();
afh[1] = TR(lbA, 27136);
SB(); M32A(Y[0], AF(3), xb[0][27]); SB();
afl[2] = TR(lbA, 28672);
GLDS(lvoffB, lbase, 1024);
SB(); M32A(Y[1], AF(3), xb[1][27]); SB();
afh[2] = TR(lbA, 29184);
lbase += lstep;
SB();
asm volatile("s_waitcnt vmcnt(12)\n\ts_barrier" ::: "memory");
SB(); M32A(X[0], AF(0), xb[0][20]); SB();
afl[3] = TR(lbA, 30720);
SB(); M32A(X[1], AF(0), xb[1][20]); SB();
afh[3] = TR(lbA, 31232);
xb[0][28] = *(AS3 u32x4*)(uintptr_t)(xrd + 24576);
xb[1][28] = *(AS3 u32x4*)(uintptr_t)(xrd + 25600);
SB(); M32A(X[0], AF(1), xb[0][21]); SB();
afl[0] = TR(lbA, 32768);
SETM0(wv2k + 16384);
xb[0][29] = *(AS3 u32x4*)(uintptr_t)(xrd + 28672);
xb[1][29] = *(AS3 u32x4*)(uintptr_t)(xrd + 29696);
SB(); M32A(X[1], AF(1), xb[1][21]); SB();
afh[0] = TR(lbA, 33280);
GLDS(lvoffA, lbase, 0);
xb[0][30] = *(AS3 u32x4*)(uintptr_t)(xrd + 32768);
SB(); M32A(X[0], AF(2), xb[0][22]); SB();
afl[1] = TR(lbA, 34816);
xb[1][30] = *(AS3 u32x4*)(uintptr_t)(xrd + 33792);
xb[0][31] = *(AS3 u32x4*)(uintptr_t)(xrd + 36864);
SB(); M32A(X[1], AF(2), xb[1][22]); SB();
afh[1] = TR(lbA, 35328);
xb[1][31] = *(AS3 u32x4*)(uintptr_t)(xrd + 37888);
SB(); M32A(X[0], AF(3), xb[0][23]); SB();
afl[2] = TR(lbA, 36864);
GLDS(lvoffB, lbase, 1024);
SB(); M32A(X[1], AF(3), xb[1][23]); SB();
afh[2] = TR(lbA, 37376);
lbase += lstep;
SB();
asm volatile("s_waitcnt vmcnt(6)\n\ts_barrier" : "+a"(xb[0][28]), "+a"(xb[1][28]), "+a"(xb[0][29]), "+a"(xb[1][29]), "+v"(xb[0][30]), "+v"(xb[1][30]), "+v"(xb[0][31]), "+v"(xb[1][31]) :: "memory");
SB(); M32A(Y[0], AF(0), xb[0][28]); SB();
afl[3] = TR(lbA, 38912);
SB(); M32A(Y[1], AF(0), xb[1][28]); SB();
afh[3] = TR(lbA, 39424);
xb[0][32] = *(AS3 u32x4*)(uintptr_t)(xrd + 40960);
xb[1][32] = *(AS3 u32x4*)(uintptr_t)(xrd + 41984);
SB(); M32A(Y[0], AF(1), xb[0][29]); SB();
afl[0] = TR(lbA, 40960);
SETM0(wv2k + 24576);
xb[0][33] = *(AS3 u32x4*)(uintptr_t)(xrd + 45056);
xb[1][33] = *(AS3 u32x4*)(uintptr_t)(xrd + 46080);
SB(); M32A(Y[1], AF(1), xb[1][29]); SB();
afh[0] = TR(lbA, 41472);
GLDS(lvoffA, lbase, 0);
xb[0][34] = *(AS3 u32x4*)(uintptr_t)(xrd + 49152);
SB(); M32V(Y[0], AF(2), xb[0][30]); SB();
afl[1] = TR(lbA, 43008);
xb[1][34] = *(AS3 u32x4*)(uintptr_t)(xrd + 50176);
xb[0][35] = *(AS3 u32x4*)(uintptr_t)(xrd + 53248);
SB(); M32V(Y[1], AF(2), xb[1][30]); SB();
afh[1] = TR(lbA, 43520);
xb[1][35] = *(AS3 u32x4*)(uintptr_t)(xrd + 54272);
SB(); M32V(Y[0], AF(3), xb[0][31]); SB();
afl[2] = TR(lbA, 45056);
GLDS(lvoffB, lbase, 1024);
SB(); M32V(Y[1], AF(3), xb[1][31]); SB();
afh[2] = TR(lbA, 45568);
lbase += lstep;
SB();
asm volatile("s_waitcnt vmcnt(18)\n\ts_barrier" : "+v"(xb[0][32]), "+v"(xb[1][32]), "+v"(xb[0][33]), "+v"(xb[1][33]), "+v"(xb[0][34]), "+v"(xb[1][34]), "+v"(xb[0][35]), "+v"(xb[1][35]) :: "memory");
SB(); M32V(Y[0], AF(0), xb[0][32]); SB();
afl[3] = TR(lbA, 47104);
SB(); M32V(Y[1], AF(0), xb[1][32]); SB();
afh[3] = TR(lbA, 47616);
SB(); M32V(Y[0], AF(1), xb[0][33]); SB();
afl[0] = TR(lbA, 49152);
SETM0(wv2k + 32768);
SB(); M32V(Y[1], AF(1), xb[1][33]); SB();
afh[0] = TR(lbA, 49664);
GLDS(lvoffA, lbase, 0);
SB(); M32V(Y[0], AF(2), xb[0][34]); SB();
afl[1] = TR(lbA, 51200);
SB(); M32V(Y[1], AF(2), xb[1][34]); SB();
afh[1] = TR(lbA, 51712);
SB(); M32V(Y[0], AF(3), xb[0][35]); SB();
afl[2] = TR(lbA, 53248);
GLDS(lvoffB, lbase, 1024);
SB(); M32V(Y[1], AF(3), xb[1][35]); SB();
afh[2] = TR(lbA, 53760);
lbase += lstep;
SB();
asm volatile("s_waitcnt vmcnt(16)\n\ts_barrier" ::: "memory");
SB(); M32A(X[0], AF(0), xb[0][24]); SB();
afl[3] = TR(lbA, 55296);
SB(); M32A(X[1], AF(0), xb[1][24]); SB();
afh[3] = TR(lbA, 55808);
SB(); M32A(X[0], AF(1), xb[0][25]); SB();
afl[0] = TR(lbA, 57344);
SETM0(wv2k + 40960);
SB(); M32A(X[1], AF(1), xb[1][25]); SB();
afh[0] = TR(lbA, 57856);
GLDS(lvoffA, lbase, 0);
SB(); M32A(X[0], AF(2), xb[0][26]); SB();
afl[1] = TR(lbA, 59392);
SB(); M32A(X[1], AF(2), xb[1][26]); SB();
afh[1] = TR(lbA, 59904);
SB(); M32A(X[0], AF(3), xb[0][27]); SB();
afl[2] = TR(lbA, 61440);
GLDS(lvoffB, lbase, 1024);
{ unsigned u = pk_bf16(Y[0][0], Y[0][1]); PIN(u); h2[0] = u; }
{ unsigned u = pk_bf16(Y[0][2], Y[0][3]); PIN(u); h2[1] = u; }
SB(); M32A(X[1], AF(3), xb[1][27]); SB();
afh[2] = TR(lbA, 61952);
lbase += lstep;
SB();
asm volatile("s_waitcnt vmcnt(14)\n\ts_barrier" ::: "memory");
SB(); M32A(X[0], AF(0), xb[0][28]); SB();
afl[3] = TR(lbA, 63488);
ST21(qvo, h2, qdst + (qp0) * 16384 + 0);
{ unsigned u = pk_bf16(Y[0][4], Y[0][5]); PIN(u); h2[0] = u; }
SB(); M32A(X[1], AF(0), xb[1][28]); SB();
afh[3] = TR(lbA, 64000);
{ unsigned u = pk_bf16(Y[0][6], Y[0][7]); PIN(u); h2[1] = u; }
ST21(qvo, h2, qdst + (qp0) * 16384 + 2048);
SB(); M32A(X[0], AF(1), xb[0][29]); SB();
afl[0] = TR(lbA, 65536);
SETM0(wv2k + 49152);
{ unsigned u = pk_bf16(Y[0][8], Y[0][9]); PIN(u); h2[0] = u; }
{ unsigned u = pk_bf16(Y[0][10], Y[0][11]); PIN(u); h2[1] = u; }
SB(); M32A(X[1], AF(1), xb[1][29]); SB();
afh[0] = TR(lbA, 66048);
GLDS(lvoffA, lbase, 0);
SB(); M32V(X[0], AF(2), xb[0][30]); SB();
afl[1] = TR(lbA, 67584);
ST21(qvo, h2, qdst + (qp0) * 16384 + 4096);
{ unsigned u = pk_bf16(Y[0][12], Y[0][13]); PIN(u); h2[0] = u; }
SB(); M32V(X[1], AF(2), xb[1][30]); SB();
afh[1] = TR(lbA, 68096);
{ unsigned u = pk_bf16(Y[0][14], Y[0][15]); PIN(u); h2[1] = u; }
ST21(qvo, h2, qdst + (qp0) * 16384 + 6144);
SB(); M32V(X[0], AF(3), xb[0][31]); SB();
afl[2] = TR(lbA, 69632);
GLDS(lvoffB, lbase, 1024);
{ unsigned u = pk_bf16(Y[1][0], Y[1][1]); PIN(u); h2[0] = u; }
{ unsigned u = pk_bf16(Y[1][2], Y[1][3]); PIN(u); h2[1] = u; }
SB(); M32V(X[1], AF(3), xb[1][31]); SB();
afh[2] = TR(lbA, 70144);
lbase += lstep;
SB();
asm volatile("s_waitcnt vmcnt(16)\n\ts_barrier" ::: "memory");
SB(); M32V(X[0], AF(0), xb[0][32]); SB();
afl[3] = TR(lbA, 71680);
ST21(qvo, h2, qdst + (qp0) * 16384 + 256);
{ unsigned u = pk_bf16(Y[1][4], Y[1][5]); PIN(u); h2[0] = u; }
SB(); M32V(X[1], AF(0), xb[1][32]); SB();
afh[3] = TR(lbA, 72192);
{ unsigned u = pk_bf16(Y[1][6], Y[1][7]); PIN(u); h2[1] = u; }
ST21(qvo, h2, qdst + (qp0) * 16384 + 2304);
SB(); M32V(X[0], AF(1), xb[0][33]); SB();
afl[0] = TR(lbA, 0);
SETM0(wv2k + 57344);
{ unsigned u = pk_bf16(Y[1][8], Y[1][9]); PIN(u); h2[0] = u; }
{ unsigned u = pk_bf16(Y[1][10], Y[1][11]); PIN(u); h2[1] = u; }
SB(); M32V(X[1], AF(1), xb[1][33]); SB();
afh[0] = TR(lbA, 512);
GLDS(lvoffA, lbase, 0);
SB(); M32V(X[0], AF(2), xb[0][34]); SB();
afl[1] = TR(lbA, 2048);
ST21(qvo, h2, qdst + (qp0) * 16384 + 4352);
{ unsigned u = pk_bf16(Y[1][12], Y[1][13]); PIN(u); h2[0] = u; }
SB(); M32V(X[1], AF(2), xb[1][34]); SB();
afh[1] = TR(lbA, 2560);
{ unsigned u = pk_bf16(Y[1][14], Y[1][15]); PIN(u); h2[1] = u; }
ST21(qvo, h2, qdst + (qp0) * 16384 + 6400);
SB(); M32V(X[0], AF(3), xb[0][35]); SB();
afl[2] = TR(lbA, 4096);
GLDS(lvoffB, lbase, 1024);
SB(); M32V(X[1], AF(3), xb[1][35]); SB();
afh[2] = TR(lbA, 4608);
lbase += lstep;
}
STAMP(3); JIT(3);
{
const char* nbase = wgb + 128; const unsigned nstride = 9216u;
SB();
asm volatile("s_waitcnt vmcnt(20)\n\ts_barrier" ::: "memory");
SB(); M32ZA(Y[0], AF(0), xb[0][0]); SB();
afl[3] = TR(lbA, 6144);
T[0] = X[0][0];
T[1] = X[0][1];
T[2] = X[0][2];
T[3] = X[0][3];
T[4] = X[0][8];
T[5] = X[0][9];
T[6] = X[0][10];
T[7] = X[0][11];
T[8] = X[0][4];
T[9] = X[0][5];
T[10] = X[0][6];
T[11] = X[0][7];
T[12] = X[0][12];
T[13] = X[0][13];
T[14] = X[0][14];
T[15] = X[0][15];
T[16] = X[1][0];
T[17] = X[1][1];
T[18] = X[1][2];
T[19] = X[1][3];
T[20] = X[1][8];
T[21] = X[1][9];
T[22] = X[1][10];
T[23] = X[1][11];
T[24] = X[1][4];
T[25] = X[1][5];
T[26] = X[1][6];
T[27] = X[1][7];
T[28] = X[1][12];
T[29] = X[1][13];
T[30] = X[1][14];
T[31] = X[1][15];
SB(); M32ZA(Y[1], AF(0), xb[1][0]); SB();
afh[3] = TR(lbA, 6656);
SB(); M32A(Y[0], AF(1), xb[0][1]); SB();
afl[0] = TR(lbA, 8192);
SETM0(wv2k + 65536);
SB(); M32A(Y[1], AF(1), xb[1][1]); SB();
afh[0] = TR(lbA, 8704);
GLDS(lvoffA, lbase, 0);
SB(); M32A(Y[0], AF(2), xb[0][2]); SB();
afl[1] = TR(lbA, 10240);
SB(); M32A(Y[1], AF(2), xb[1][2]); SB();
afh[1] = TR(lbA, 10752);
SB(); M32A(Y[0], AF(3), xb[0][3]); SB();
afl[2] = TR(lbA, 12288);
GLDS(lvoffB, lbase, 1024);
gt0 = T[0] * T[0]; PIN(gt0);
SB(); M32A(Y[1], AF(3), xb[1][3]); SB();
afh[2] = TR(lbA, 12800);
lbase = nbase; lstep = 64 * nstride; lvoffA = rowA * nstride + c16P1; lvoffB = (rowA + 8) * nstride + c16P1 - 1024;
SB();
asm volatile("s_waitcnt vmcnt(20)\n\ts_barrier" ::: "memory");
SB(); M32A(Y[0], AF(0), xb[0][4]); SB();
afl[3] = TR(lbA, 14336);
gt0 = __builtin_fmaf(gt0, C1, C0); PIN(gt0);
SB(); M32A(Y[1], AF(0), xb[1][4]); SB();
afh[3] = TR(lbA, 14848);
gt0 = T[0] * gt0; PIN(gt0);
gt1 = T[1] * T[1]; PIN(gt1);
SB(); M32A(Y[0], AF(1), xb[0][5]); SB();
afl[0] = TR(lbA, 16384);
SETM0(wv2k + 0);
gt0 = __builtin_amdgcn_exp2f(gt0); PIN(gt0);
gt1 = __builtin_fmaf(gt1, C1, C0); PIN(gt1);
SB(); M32A(Y[1], AF(1), xb[1][5]); SB();
afh[0] = TR(lbA, 16896);
GLDS(lvoffA, lbase, 0);
gt0 = gt0 + 1.0f; PIN(gt0);
gt1 = T[1] * gt1; PIN(gt1);
SB(); M32A(Y[0], AF(2), xb[0][6]); SB();
afl[1] = TR(lbA, 18432);
gt0 = __builtin_amdgcn_rcpf(gt0); PIN(gt0);
gt1 = __builtin_amdgcn_exp2f(gt1); PIN(gt1);
SB(); M32A(Y[1], AF(2), xb[1][6]); SB();
afh[1] = TR(lbA, 18944);
{ float v = T[0] * gt0; PIN(v); T[0] = v; }
gt1 = gt1 + 1.0f; PIN(gt1);
gt2 = T[2] * T[2]; PIN(gt2);
SB(); M32A(Y[0], AF(3), xb[0][7]); SB();
afl[2] = TR(lbA, 20480);
GLDS(lvoffB, lbase, 1024);
gt1 = __builtin_amdgcn_rcpf(gt1); PIN(gt1);
gt2 = __builtin_fmaf(gt2, C1, C0); PIN(gt2);
SB(); M32A(Y[1], AF(3), xb[1][7]); SB();
afh[2] = TR(lbA, 20992);
lbase += lstep;
SB();
asm volatile("s_waitcnt vmcnt(20)\n\ts_barrier" ::: "memory");
SB(); M32A(Y[0], AF(0), xb[0][8]); SB();
afl[3] = TR(lbA, 22528);
{ float v = T[1] * gt1; PIN(v); T[1] = v; }
gt2 = T[2] * gt2; PIN(gt2);
gt0 = T[3] * T[3]; PIN(gt0);
SB(); M32A(Y[1], AF(0), xb[1][8]); SB();
afh[3] = TR(lbA, 23040);
TOUCH(trow * 9216u, wub + 128);
gt2 = __builtin_amdgcn_exp2f(gt2); PIN(gt2);
gt0 = __builtin_fmaf(gt0, C1, C0); PIN(gt0);
SB(); M32A(Y[0], AF(1), xb[0][9]); SB();
afl[0] = TR(lbA, 24576);
SETM0(wv2k + 8192);
gt2 = gt2 + 1.0f; PIN(gt2);
gt0 = T[3] * gt0; PIN(gt0);
gt1 = T[4] * T[4]; PIN(gt1);
SB(); M32A(Y[1], AF(1), xb[1][9]); SB();
afh[0] = TR(lbA, 25088);
GLDS(lvoffA, lbase, 0);
gt2 = __builtin_amdgcn_rcpf(gt2); PIN(gt2);
SB(); M32A(Y[0], AF(2), xb[0][10]); SB();
afl[1] = TR(lbA, 26624);
{ float v = T[2] * gt2; PIN(v); T[2] = v; }
gt0 = __builtin_amdgcn_exp2f(gt0); PIN(gt0);
gt1 = __builtin_fmaf(gt1, C1, C0); PIN(gt1);
SB(); M32A(Y[1], AF(2), xb[1][10]); SB();
afh[1] = TR(lbA, 27136);
gt0 = gt0 + 1.0f; PIN(gt0);
gt1 = T[4] * gt1; PIN(gt1);
gt2 = T[5] * T[5]; PIN(gt2);
SB(); M32A(Y[0], AF(3), xb[0][11]); SB();
afl[2] = TR(lbA, 28672);
GLDS(lvoffB, lbase, 1024);
gt0 = __builtin_amdgcn_rcpf(gt0); PIN(gt0);
gt2 = __builtin_fmaf(gt2, C1, C0); PIN(gt2);
SB(); M32A(Y[1], AF(3), xb[1][11]); SB();
afh[2] = TR(lbA, 29184);
lbase += lstep;
SB();
asm volatile("s_waitcnt vmcnt(21)\n\ts_barrier" ::: "memory");
SB(); M32A(Y[0], AF(0), xb[0][12]); SB();
afl[3] = TR(lbA, 30720);
{ float v = T[3] * gt0; PIN(v); T[3] = v; }
gt1 = __builtin_amdgcn_exp2f(gt1); PIN(gt1);
gt2 = T[5] * gt2; PIN(gt2);
SB(); M32A(Y[1], AF(0), xb[1][12]); SB();
afh[3] = TR(lbA, 31232);
gt1 = gt1 + 1.0f; PIN(gt1);
gt2 = __builtin_amdgcn_exp2f(gt2); PIN(gt2);
SB(); M32A(Y[0], AF(1), xb[0][13]); SB();
afl[0] = TR(lbA, 32768);
SETM0(wv2k + 16384);
gt1 = __builtin_amdgcn_rcpf(gt1); PIN(gt1);
gt2 = gt2 + 1.0f; PIN(gt2);
SB(); M32A(Y[1], AF(1), xb[1][13]); SB();
afh[0] = TR(lbA, 33280);
GLDS(lvoffA, lbase, 0);
{ float v = T[4] * gt1; PIN(v); T[4] = v; }
gt0 = T[6] * T[6]; PIN(gt0);
SB(); M32A(Y[0], AF(2), xb[0][14]); SB();
afl[1] = TR(lbA, 34816);
gt2 = __builtin_amdgcn_rcpf(gt2); PIN(gt2);
gt0 = __builtin_fmaf(gt0, C1, C0); PIN(gt0);
gt1 = T[7] * T[7]; PIN(gt1);
SB(); M32A(Y[1], AF(2), xb[1][14]); SB();
afh[1] = TR(lbA, 35328);
{ float v = T[5] * gt2; PIN(v); T[5] = v; }
gt0 = T[6] * gt0; PIN(gt0);
gt1 = __builtin_fmaf(gt1, C1, C0); PIN(gt1);
SB(); M32A(Y[0], AF(3), xb[0][15]); SB();
afl[2] = TR(lbA, 36864);
GLDS(lvoffB, lbase, 1024);
gt0 = __builtin_amdgcn_exp2f(gt0); PIN(gt0);
gt1 = T[7] * gt1; PIN(gt1);
SB(); M32A(Y[1], AF(3), xb[1][15]); SB();
afh[2] = TR(lbA, 37376);
lbase += lstep;
SB();
asm volatile("s_waitcnt vmcnt(21)\n\ts_barrier" ::: "memory");
SB(); M32A(Y[0], AF(0), xb[0][16]); SB();
afl[3] = TR(lbA, 38912);
gt0 = gt0 + 1.0f; PIN(gt0);
gt1 = __builtin_amdgcn_exp2f(gt1); PIN(gt1);
gt2 = T[8] * T[8]; PIN(gt2);
SB(); M32A(Y[1], AF(0), xb[1][16]); SB();
afh[3] = TR(lbA, 39424);
TOUCH(trow * 9216u, wgb + 256);
gt0 = __builtin_amdgcn_rcpf(gt0); PIN(gt0);
gt1 = gt1 + 1.0f; PIN(gt1);
SB(); M32A(Y[0], AF(1), xb[0][17]); SB();
afl[0] = TR(lbA, 40960);
SETM0(wv2k + 24576);
{ float v = T[6] * gt0; PIN(v); T[6] = v; }
gt1 = __builtin_amdgcn_rcpf(gt1); PIN(gt1);
SB(); M32A(Y[1], AF(1), xb[1][17]); SB();
afh[0] = TR(lbA, 41472);
GLDS(lvoffA, lbase, 0);
{ float v = T[7] * gt1; PIN(v); T[7] = v; }
gt2 = __builtin_fmaf(gt2, C1, C0); PIN(gt2);
SB(); M32A(Y[0], AF(2), xb[0][18]); SB();
afl[1] = TR(lbA, 43008);
gt2 = T[8] * gt2; PIN(gt2);
gt0 = T[9] * T[9]; PIN(gt0);
gt1 = T[10] * T[10]; PIN(gt1);
SB(); M32A(Y[1], AF(2), xb[1][18]); SB();
afh[1] = TR(lbA, 43520);
gt2 = __builtin_amdgcn_exp2f(gt2); PIN(gt2);
gt0 = __builtin_fmaf(gt0, C1, C0); PIN(gt0);
SB(); M32A(Y[0], AF(3), xb[0][19]); SB();
afl[2] = TR(lbA, 45056);
GLDS(lvoffB, lbase, 1024);
gt2 = gt2 + 1.0f; PIN(gt2);
gt0 = T[9] * gt0; PIN(gt0);
gt1 = __builtin_fmaf(gt1, C1, C0); PIN(gt1);
SB(); M32A(Y[1], AF(3), xb[1][19]); SB();
afh[2] = TR(lbA, 45568);
lbase += lstep;
SB();
asm volatile("s_waitcnt vmcnt(18)\n\ts_barrier" ::: "memory");
SB(); M32A(Y[0], AF(0), xb[0][20]); SB();
afl[3] = TR(lbA, 47104);
gt2 = __builtin_amdgcn_rcpf(gt2); PIN(gt2);
gt0 = __builtin_amdgcn_exp2f(gt0); PIN(gt0);
SB(); M32A(Y[1], AF(0), xb[1][20]); SB();
afh[3] = TR(lbA, 47616);
{ float v = T[8] * gt2; PIN(v); T[8] = v; }
gt0 = gt0 + 1.0f; PIN(gt0);
gt1 = T[10] * gt1; PIN(gt1);
SB(); M32A(Y[0], AF(1), xb[0][21]); SB();
afl[0] = TR(lbA, 49152);
SETM0(wv2k + 32768);
gt0 = __builtin_amdgcn_rcpf(gt0); PIN(gt0);
gt2 = T[11] * T[11]; PIN(gt2);
SB(); M32A(Y[1], AF(1), xb[1][21]); SB();
afh[0] = TR(lbA, 49664);
GLDS(lvoffA, lbase, 0);
{ float v = T[9] * gt0; PIN(v); T[9] = v; }
gt2 = __builtin_fmaf(gt2, C1, C0); PIN(gt2);
SB(); M32A(Y[0], AF(2), xb[0][22]); SB();
afl[1] = TR(lbA, 51200);
gt1 = __builtin_amdgcn_exp2f(gt1); PIN(gt1);
gt2 = T[11] * gt2; PIN(gt2);
gt0 = T[12] * T[12]; PIN(gt0);
SB(); M32A(Y[1], AF(2), xb[1][22]); SB();
afh[1] = TR(lbA, 51712);
gt1 = gt1 + 1.0f; PIN(gt1);
gt2 = __builtin_amdgcn_exp2f(gt2); PIN(gt2);
SB(); M32A(Y[0], AF(3), xb[0][23]); SB();
afl[2] = TR(lbA, 53248);
GLDS(lvoffB, lbase, 1024);
gt1 = __builtin_amdgcn_rcpf(gt1); PIN(gt1);
gt2 = gt2 + 1.0f; PIN(gt2);
SB(); M32A(Y[1], AF(3), xb[1][23]); SB();
afh[2] = TR(lbA, 53760);
lbase += lstep;
SB();
asm volatile("s_waitcnt vmcnt(14)\n\ts_barrier" ::: "memory");
SB(); M32A(Y[0], AF(0), xb[0][24]); SB();
afl[3] = TR(lbA, 55296);
{ float v = T[10] * gt1; PIN(v); T[10] = v; }
gt2 = __builtin_amdgcn_rcpf(gt2); PIN(gt2);
gt0 = __builtin_fmaf(gt0, C1, C0); PIN(gt0);
SB(); M32A(Y[1], AF(0), xb[1][24]); SB();
afh[3] = TR(lbA, 55808);
{ float v = T[11] * gt2; PIN(v); T[11] = v; }
gt0 = T[12] * gt0; PIN(gt0);
gt1 = T[13] * T[13]; PIN(gt1);
SB(); M32A(Y[0], AF(1), xb[0][25]); SB();
afl[0] = TR(lbA, 57344);
SETM0(wv2k + 40960);
gt0 = __builtin_amdgcn_exp2f(gt0); PIN(gt0);
gt1 = __builtin_fmaf(gt1, C1, C0); PIN(gt1);
SB(); M32A(Y[1], AF(1), xb[1][25]); SB();
afh[0] = TR(lbA, 57856);
GLDS(lvoffA, lbase, 0);
gt0 = gt0 + 1.0f; PIN(gt0);
gt1 = T[13] * gt1; PIN(gt1);
SB(); M32A(Y[0], AF(2), xb[0][26]); SB();
afl[1] = TR(lbA, 59392);
gt0 = __builtin_amdgcn_rcpf(gt0); PIN(gt0);
gt1 = __builtin_amdgcn_exp2f(gt1); PIN(gt1);
SB(); M32A(Y[1], AF(2), xb[1][26]); SB();
afh[1] = TR(lbA, 59904);
{ float v = T[12] * gt0; PIN(v); T[12] = v; }
gt1 = gt1 + 1.0f; PIN(gt1);
gt2 = T[14] * T[14]; PIN(gt2);
SB(); M32A(Y[0], AF(3), xb[0][27]); SB();
afl[2] = TR(lbA, 61440);
GLDS(lvoffB, lbase, 1024);
gt1 = __builtin_amdgcn_rcpf(gt1); PIN(gt1);
gt2 = __builtin_fmaf(gt2, C1, C0); PIN(gt2);
SB(); M32A(Y[1], AF(3), xb[1][27]); SB();
afh[2] = TR(lbA, 61952);
lbase += lstep;
SB();
asm volatile("s_waitcnt vmcnt(14)\n\ts_barrier" ::: "memory");
SIGNAL_ADD(fQ, nq, nq > 0);
SB(); M32A(Y[0], AF(0), xb[0][28]); SB();
afl[3] = TR(lbA, 63488);
{ float v = T[13] * gt1; PIN(v); T[13] = v; }
gt2 = T[14] * gt2; PIN(gt2);
gt0 = T[15] * T[15]; PIN(gt0);
SB(); M32A(Y[1], AF(0), xb[1][28]); SB();
afh[3] = TR(lbA, 64000);
gt2 = __builtin_amdgcn_exp2f(gt2); PIN(gt2);
gt0 = __builtin_fmaf(gt0, C1, C0); PIN(gt0);
SB(); M32A(Y[0], AF(1), xb[0][29]); SB();
afl[0] = TR(lbA, 65536);
SETM0(wv2k + 49152);
gt2 = gt2 + 1.0f; PIN(gt2);
gt0 = T[15] * gt0; PIN(gt0);
gt1 = T[16] * T[16]; PIN(gt1);
SB(); M32A(Y[1], AF(1), xb[1][29]); SB();
afh[0] = TR(lbA, 66048);
GLDS(lvoffA, lbase, 0);
gt2 = __builtin_amdgcn_rcpf(gt2); PIN(gt2);
SB(); M32V(Y[0], AF(2), xb[0][30]); SB();
afl[1] = TR(lbA, 67584);
{ float v = T[14] * gt2; PIN(v); T[14] = v; }
gt0 = __builtin_amdgcn_exp2f(gt0); PIN(gt0);
gt1 = __builtin_fmaf(gt1, C1, C0); PIN(gt1);
SB(); M32V(Y[1], AF(2), xb[1][30]); SB();
afh[1] = TR(lbA, 68096);
gt0 = gt0 + 1.0f; PIN(gt0);
gt1 = T[16] * gt1; PIN(gt1);
gt2 = T[17] * T[17]; PIN(gt2);
SB(); M32V(Y[0], AF(3), xb[0][31]); SB();
afl[2] = TR(lbA, 69632);
GLDS(lvoffB, lbase, 1024);
gt0 = __builtin_amdgcn_rcpf(gt0); PIN(gt0);
gt2 = __builtin_fmaf(gt2, C1, C0); PIN(gt2);
SB(); M32V(Y[1], AF(3), xb[1][31]); SB();
afh[2] = TR(lbA, 70144);
lbase += lstep;
SB();
asm volatile("s_waitcnt vmcnt(14)\n\ts_barrier" ::: "memory");
SB(); M32V(Y[0], AF(0), xb[0][32]); SB();
afl[3] = TR(lbA, 71680);
{ float v = T[15] * gt0; PIN(v); T[15] = v; }
gt1 = __builtin_amdgcn_exp2f(gt1); PIN(gt1);
gt2 = T[17] * gt2; PIN(gt2);
SB(); M32V(Y[1], AF(0), xb[1][32]); SB();
afh[3] = TR(lbA, 72192);
gt1 = gt1 + 1.0f; PIN(gt1);
gt2 = __builtin_amdgcn_exp2f(gt2); PIN(gt2);
SB(); M32V(Y[0], AF(1), xb[0][33]); SB();
afl[0] = TR(lbA, 0);
SETM0(wv2k + 57344);
gt1 = __builtin_amdgcn_rcpf(gt1); PIN(gt1);
gt2 = gt2 + 1.0f; PIN(gt2);
SB(); M32V(Y[1], AF(1), xb[1][33]); SB();
afh[0] = TR(lbA, 512);
GLDS(lvoffA, lbase, 0);
{ float v = T[16] * gt1; PIN(v); T[16] = v; }
gt0 = T[18] * T[18]; PIN(gt0);
SB(); M32V(Y[0], AF(2), xb[0][34]); SB();
afl[1] = TR(lbA, 2048);
gt2 = __builtin_amdgcn_rcpf(gt2); PIN(gt2);
gt0 = __builtin_fmaf(gt0, C1, C0); PIN(gt0);
gt1 = T[19] * T[19]; PIN(gt1);
SB(); M32V(Y[1], AF(2), xb[1][34]); SB();
afh[1] = TR(lbA, 2560);
{ float v = T[17] * gt2; PIN(v); T[17] = v; }
gt0 = T[18] * gt0; PIN(gt0);
gt1 = __builtin_fmaf(gt1, C1, C0); PIN(gt1);
SB(); M32V(Y[0], AF(3), xb[0][35]); SB();
afl[2] = TR(lbA, 4096);
GLDS(lvoffB, lbase, 1024);
gt0 = __builtin_amdgcn_exp2f(gt0); PIN(gt0);
gt1 = T[19] * gt1; PIN(gt1);
SB(); M32V(Y[1], AF(3), xb[1][35]); SB();
afh[2] = TR(lbA, 4608);
lbase += lstep;
}
{
const char* nbase = wub + 128; const unsigned nstride = 9216u;
SB();
asm volatile("s_waitcnt vmcnt(13)\n\ts_barrier" ::: "memory");
SB(); M32ZA(X[0], AF(0), xb[0][0]); SB();
afl[3] = TR(lbA, 6144);
gt0 = gt0 + 1.0f; PIN(gt0);
gt1 = __builtin_amdgcn_exp2f(gt1); PIN(gt1);
gt2 = T[20] * T[20]; PIN(gt2);
SB(); M32ZA(X[1], AF(0), xb[1][0]); SB();
afh[3] = TR(lbA, 6656);
gt0 = __builtin_amdgcn_rcpf(gt0); PIN(gt0);
gt1 = gt1 + 1.0f; PIN(gt1);
SB(); M32A(X[0], AF(1), xb[0][1]); SB();
afl[0] = TR(lbA, 8192);
SETM0(wv2k + 65536);
{ float v = T[18] * gt0; PIN(v); T[18] = v; }
gt1 = __builtin_amdgcn_rcpf(gt1); PIN(gt1);
SB(); M32A(X[1], AF(1), xb[1][1]); SB();
afh[0] = TR(lbA, 8704);
GLDS(lvoffA, lbase, 0);
{ float v = T[19] * gt1; PIN(v); T[19] = v; }
gt2 = __builtin_fmaf(gt2, C1, C0); PIN(gt2);
SB(); M32A(X[0], AF(2), xb[0][2]); SB();
afl[1] = TR(lbA, 10240);
gt2 = T[20] * gt2; PIN(gt2);
gt0 = T[21] * T[21]; PIN(gt0);
gt1 = T[22] * T[22]; PIN(gt1);
SB(); M32A(X[1], AF(2), xb[1][2]); SB();
afh[1] = TR(lbA, 10752);
gt2 = __builtin_amdgcn_exp2f(gt2); PIN(gt2);
gt0 = __builtin_fmaf(gt0, C1, C0); PIN(gt0);
SB(); M32A(X[0], AF(3), xb[0][3]); SB();
afl[2] = TR(lbA, 12288);
GLDS(lvoffB, lbase, 1024);
gt2 = gt2 + 1.0f; PIN(gt2);
gt0 = T[21] * gt0; PIN(gt0);
gt1 = __builtin_fmaf(gt1, C1, C0); PIN(gt1);
SB(); M32A(X[1], AF(3), xb[1][3]); SB();
afh[2] = TR(lbA, 12800);
lbase = nbase; lstep = 64 * nstride; lvoffA = rowA * nstride + c16P1; lvoffB = (rowA + 8) * nstride + c16P1 - 1024;
SB();
asm volatile("s_waitcnt vmcnt(13)\n\ts_barrier" ::: "memory");
SB(); M32A(X[0], AF(0), xb[0][4]); SB();
afl[3] = TR(lbA, 14336);
gt2 = __builtin_amdgcn_rcpf(gt2); PIN(gt2);
gt0 = __builtin_amdgcn_exp2f(gt0); PIN(gt0);
SB(); M32A(X[1], AF(0), xb[1][4]); SB();
afh[3] = TR(lbA, 14848);
{ float v = T[20] * gt2; PIN(v); T[20] = v; }
gt0 = gt0 + 1.0f; PIN(gt0);
gt1 = T[22] * gt1; PIN(gt1);
SB(); M32A(X[0], AF(1), xb[0][5]); SB();
afl[0] = TR(lbA, 16384);
SETM0(wv2k + 0);
gt0 = __builtin_amdgcn_rcpf(gt0); PIN(gt0);
gt2 = T[23] * T[23]; PIN(gt2);
SB(); M32A(X[1], AF(1), xb[1][5]); SB();
afh[0] = TR(lbA, 16896);
GLDS(lvoffA, lbase, 0);
{ float v = T[21] * gt0; PIN(v); T[21] = v; }
gt2 = __builtin_fmaf(gt2, C1, C0); PIN(gt2);
SB(); M32A(X[0], AF(2), xb[0][6]); SB();
afl[1] = TR(lbA, 18432);
gt1 = __builtin_amdgcn_exp2f(gt1); PIN(gt1);
gt2 = T[23] * gt2; PIN(gt2);
gt0 = T[24] * T[24]; PIN(gt0);
SB(); M32A(X[1], AF(2), xb[1][6]); SB();
afh[1] = TR(lbA, 18944);
gt1 = gt1 + 1.0f; PIN(gt1);
gt2 = __builtin_amdgcn_exp2f(gt2); PIN(gt2);
SB(); M32A(X[0], AF(3), xb[0][7]); SB();
afl[2] = TR(lbA, 20480);
GLDS(lvoffB, lbase, 1024);
gt1 = __builtin_amdgcn_rcpf(gt1); PIN(gt1);
gt2 = gt2 + 1.0f; PIN(gt2);
SB(); M32A(X[1], AF(3), xb[1][7]); SB();
afh[2] = TR(lbA, 20992);
lbase += lstep;
SB();
asm volatile("s_waitcnt vmcnt(12)\n\ts_barrier" ::: "memory");
SB(); M32A(X[0], AF(0), xb[0][8]); SB();
afl[3] = TR(lbA, 22528);
{ float v = T[22] * gt1; PIN(v); T[22] = v; }
gt2 = __builtin_amdgcn_rcpf(gt2); PIN(gt2);
gt0 = __builtin_fmaf(gt0, C1, C0); PIN(gt0);
SB(); M32A(X[1], AF(0), xb[1][8]); SB();
afh[3] = TR(lbA, 23040);
TOUCH(trow * 9216u, wub + 256);
{ float v = T[23] * gt2; PIN(v); T[23] = v; }
gt0 = T[24] * gt0; PIN(gt0);
gt1 = T[25] * T[25]; PIN(gt1);
SB(); M32A(X[0], AF(1), xb[0][9]); SB();
afl[0] = TR(lbA, 24576);
SETM0(wv2k + 8192);
gt0 = __builtin_amdgcn_exp2f(gt0); PIN(gt0);
gt1 = __builtin_fmaf(gt1, C1, C0); PIN(gt1);
SB(); M32A(X[1], AF(1), xb[1][9]); SB();
afh[0] = TR(lbA, 25088);
GLDS(lvoffA, lbase, 0);
gt0 = gt0 + 1.0f; PIN(gt0);
gt1 = T[25] * gt1; PIN(gt1);
SB(); M32A(X[0], AF(2), xb[0][10]); SB();
afl[1] = TR(lbA, 26624);
gt0 = __builtin_amdgcn_rcpf(gt0); PIN(gt0);
gt1 = __builtin_amdgcn_exp2f(gt1); PIN(gt1);
SB(); M32A(X[1], AF(2), xb[1][10]); SB();
afh[1] = TR(lbA, 27136);
{ float v = T[24] * gt0; PIN(v); T[24] = v; }
gt1 = gt1 + 1.0f; PIN(gt1);
gt2 = T[26] * T[26]; PIN(gt2);
SB(); M32A(X[0], AF(3), xb[0][11]); SB();
afl[2] = TR(lbA, 28672);
GLDS(lvoffB, lbase, 1024);
gt1 = __builtin_amdgcn_rcpf(gt1); PIN(gt1);
gt2 = __builtin_fmaf(gt2, C1, C0); PIN(gt2);
SB(); M32A(X[1], AF(3), xb[1][11]); SB();
afh[2] = TR(lbA, 29184);
lbase += lstep;
SB();
asm volatile("s_waitcnt vmcnt(13)\n\ts_barrier" ::: "memory");
SB(); M32A(X[0], AF(0), xb[0][12]); SB();
afl[3] = TR(lbA, 30720);
{ float v = T[25] * gt1; PIN(v); T[25] = v; }
gt2 = T[26] * gt2; PIN(gt2);
gt0 = T[27] * T[27]; PIN(gt0);
{ float v = T[0] * Y[0][0]; PIN(v); T[0] = v; }
SB(); M32A(X[1], AF(0), xb[1][12]); SB();
afh[3] = TR(lbA, 31232);
gt2 = __builtin_amdgcn_exp2f(gt2); PIN(gt2);
gt0 = __builtin_fmaf(gt0, C1, C0); PIN(gt0);
SB(); M32A(X[0], AF(1), xb[0][13]); SB();
afl[0] = TR(lbA, 32768);
SETM0(wv2k + 16384);
gt2 = gt2 + 1.0f; PIN(gt2);
gt0 = T[27] * gt0; PIN(gt0);
gt1 = T[28] * T[28]; PIN(gt1);
SB(); M32A(X[1], AF(1), xb[1][13]); SB();
afh[0] = TR(lbA, 33280);
GLDS(lvoffA, lbase, 0);
gt2 = __builtin_amdgcn_rcpf(gt2); PIN(gt2);
SB(); M32A(X[0], AF(2), xb[0][14]); SB();
afl[1] = TR(lbA, 34816);
{ float v = T[26] * gt2; PIN(v); T[26] = v; }
gt0 = __builtin_amdgcn_exp2f(gt0); PIN(gt0);
gt1 = __builtin_fmaf(gt1, C1, C0); PIN(gt1);
SB(); M32A(X[1], AF(2), xb[1][14]); SB();
afh[1] = TR(lbA, 35328);
gt0 = gt0 + 1.0f; PIN(gt0);
gt1 = T[28] * gt1; PIN(gt1);
gt2 = T[29] * T[29]; PIN(gt2);
SB(); M32A(X[0], AF(3), xb[0][15]); SB();
afl[2] = TR(lbA, 36864);
GLDS(lvoffB, lbase, 1024);
gt0 = __builtin_amdgcn_rcpf(gt0); PIN(gt0);
gt2 = __builtin_fmaf(gt2, C1, C0); PIN(gt2);
SB(); M32A(X[1], AF(3), xb[1][15]); SB();
afh[2] = TR(lbA, 37376);
lbase += lstep;
SB();
asm volatile("s_waitcnt vmcnt(13)\n\ts_barrier" ::: "memory");
SB(); M32A(X[0], AF(0), xb[0][16]); SB();
afl[3] = TR(lbA, 38912);
{ float v = T[27] * gt0; PIN(v); T[27] = v; }
gt1 = __builtin_amdgcn_exp2f(gt1); PIN(gt1);
gt2 = T[29] * gt2; PIN(gt2);
SB(); M32A(X[1], AF(0), xb[1][16]); SB();
afh[3] = TR(lbA, 39424);
gt1 = gt1 + 1.0f; PIN(gt1);
gt2 = __builtin_amdgcn_exp2f(gt2); PIN(gt2);
SB(); M32A(X[0], AF(1), xb[0][17]); SB();
afl[0] = TR(lbA, 40960);
SETM0(wv2k + 24576);
gt1 = __builtin_amdgcn_rcpf(gt1); PIN(gt1);
gt2 = gt2 + 1.0f; PIN(gt2);
SB(); M32A(X[1], AF(1), xb[1][17]); SB();
afh[0] = TR(lbA, 41472);
GLDS(lvoffA, lbase, 0);
{ float v = T[28] * gt1; PIN(v); T[28] = v; }
gt0 = T[30] * T[30]; PIN(gt0);
SB(); M32A(X[0], AF(2), xb[0][18]); SB();
afl[1] = TR(lbA, 43008);
gt2 = __builtin_amdgcn_rcpf(gt2); PIN(gt2);
gt0 = __builtin_fmaf(gt0, C1, C0); PIN(gt0);
gt1 = T[31] * T[31]; PIN(gt1);
SB(); M32A(X[1], AF(2), xb[1][18]); SB();
afh[1] = TR(lbA, 43520);
{ float v = T[29] * gt2; PIN(v); T[29] = v; }
gt0 = T[30] * gt0; PIN(gt0);
gt1 = __builtin_fmaf(gt1, C1, C0); PIN(gt1);
SB(); M32A(X[0], AF(3), xb[0][19]); SB();
afl[2] = TR(lbA, 45056);
GLDS(lvoffB, lbase, 1024);
gt0 = __builtin_amdgcn_exp2f(gt0); PIN(gt0);
gt1 = T[31] * gt1; PIN(gt1);
SB(); M32A(X[1], AF(3), xb[1][19]); SB();
afh[2] = TR(lbA, 45568);
lbase += lstep;
SB();
asm volatile("s_waitcnt vmcnt(13)\n\ts_barrier" ::: "memory");
SB(); M32A(X[0], AF(0), xb[0][20]); SB();
afl[3] = TR(lbA, 47104);
gt0 = gt0 + 1.0f; PIN(gt0);
gt1 = __builtin_amdgcn_exp2f(gt1); PIN(gt1);
{ float v = T[1] * Y[0][1]; PIN(v); T[1] = v; }
SB(); M32A(X[1], AF(0), xb[1][20]); SB();
afh[3] = TR(lbA, 47616);
gt0 = __builtin_amdgcn_rcpf(gt0); PIN(gt0);
gt1 = gt1 + 1.0f; PIN(gt1);
SB(); M32A(X[0], AF(1), xb[0][21]); SB();
afl[0] = TR(lbA, 49152);
SETM0(wv2k + 32768);
{ float v = T[30] * gt0; PIN(v); T[30] = v; }
gt1 = __builtin_amdgcn_rcpf(gt1); PIN(gt1);
SB(); M32A(X[1], AF(1), xb[1][21]); SB();
afh[0] = TR(lbA, 49664);
GLDS(lvoffA, lbase, 0);
{ float v = T[31] * gt1; PIN(v); T[31] = v; }
{ unsigned u = pk_bf16(T[0], T[1]); PIN(u); hh[0] = u; }
SB(); M32A(X[0], AF(2), xb[0][22]); SB();
afl[1] = TR(lbA, 51200);
{ float v = T[2] * Y[0][2]; PIN(v); T[2] = v; }
{ float v = T[3] * Y[0][3]; PIN(v); T[3] = v; }
{ float v = T[4] * Y[0][8]; PIN(v); T[4] = v; }
{ float v = T[5] * Y[0][9]; PIN(v); T[5] = v; }
SB(); M32A(X[1], AF(2), xb[1][22]); SB();
afh[1] = TR(lbA, 51712);
{ unsigned u = pk_bf16(T[2], T[3]); PIN(u); hh[1] = u; }
{ unsigned u = pk_bf16(T[4], T[5]); PIN(u); hh[2] = u; }
{ float v = T[6] * Y[0][10]; PIN(v); T[6] = v; }
SB(); M32A(X[0], AF(3), xb[0][23]); SB();
afl[2] = TR(lbA, 53248);
GLDS(lvoffB, lbase, 1024);
{ float v = T[7] * Y[0][11]; PIN(v); T[7] = v; }
{ float v = T[8] * Y[0][4]; PIN(v); T[8] = v; }
{ float v = T[9] * Y[0][5]; PIN(v); T[9] = v; }
SB(); M32A(X[1], AF(3), xb[1][23]); SB();
afh[2] = TR(lbA, 53760);
lbase += lstep;
SB();
asm volatile("s_waitcnt vmcnt(13)\n\ts_barrier" ::: "memory");
SB(); M32A(X[0], AF(0), xb[0][24]); SB();
afl[3] = TR(lbA, 55296);
{ unsigned u = pk_bf16(T[6], T[7]); PIN(u); hh[3] = u; }
{ float v = T[10] * Y[0][6]; PIN(v); T[10] = v; }
{ float v = T[11] * Y[0][7]; PIN(v); T[11] = v; }
{ float v = T[12] * Y[0][12]; PIN(v); T[12] = v; }
SB(); M32A(X[1], AF(0), xb[1][24]); SB();
afh[3] = TR(lbA, 55808);
*(AS3 u32x4*)(uintptr_t)(hw_ad + 0) = hh;
{ float v = T[13] * Y[0][13]; PIN(v); T[13] = v; }
SB(); M32A(X[0], AF(1), xb[0][25]); SB();
afl[0] = TR(lbA, 57344);
SETM0(wv2k + 40960);
ST41(hxvo, hh, hxw + 0);
SB(); M32A(X[1], AF(1), xb[1][25]); SB();
afh[0] = TR(lbA, 57856);
GLDS(lvoffA, lbase, 0);
{ unsigned u = pk_bf16(T[8], T[9]); PIN(u); hh[0] = u; }
{ unsigned u = pk_bf16(T[10], T[11]); PIN(u); hh[1] = u; }
SB(); M32A(X[0], AF(2), xb[0][26]); SB();
afl[1] = TR(lbA, 59392);
{ unsigned u = pk_bf16(T[12], T[13]); PIN(u); hh[2] = u; }
{ float v = T[14] * Y[0][14]; PIN(v); T[14] = v; }
{ float v = T[15] * Y[0][15]; PIN(v); T[15] = v; }
{ float v = T[16] * Y[1][0]; PIN(v); T[16] = v; }
SB(); M32A(X[1], AF(2), xb[1][26]); SB();
afh[1] = TR(lbA, 59904);
{ unsigned u = pk_bf16(T[14], T[15]); PIN(u); hh[3] = u; }
{ float v = T[17] * Y[1][1]; PIN(v); T[17] = v; }
{ float v = T[18] * Y[1][2]; PIN(v); T[18] = v; }
SB(); M32A(X[0], AF(3), xb[0][27]); SB();
afl[2] = TR(lbA, 61440);
GLDS(lvoffB, lbase, 1024);
*(AS3 u32x4*)(uintptr_t)(hw_ad + 512) = hh;
{ float v = T[19] * Y[1][3]; PIN(v); T[19] = v; }
SB(); M32A(X[1], AF(3), xb[1][27]); SB();
afh[2] = TR(lbA, 61952);
lbase += lstep;
SB();
asm volatile("s_waitcnt vmcnt(14)\n\ts_barrier" ::: "memory");
SB(); M32A(X[0], AF(0), xb[0][28]); SB();
afl[3] = TR(lbA, 63488);
ST41(hxvo, hh, hxw + 512);
{ float v = T[20] * Y[1][8]; PIN(v); T[20] = v; }
SB(); M32A(X[1], AF(0), xb[1][28]); SB();
afh[3] = TR(lbA, 64000);
{ unsigned u = pk_bf16(T[16], T[17]); PIN(u); hh[0] = u; }
{ unsigned u = pk_bf16(T[18], T[19]); PIN(u); hh[1] = u; }
{ float v = T[21] * Y[1][9]; PIN(v); T[21] = v; }
SB(); M32A(X[0], AF(1), xb[0][29]); SB();
afl[0] = TR(lbA, 65536);
SETM0(wv2k + 49152);
{ unsigned u = pk_bf16(T[20], T[21]); PIN(u); hh[2] = u; }
{ float v = T[22] * Y[1][10]; PIN(v); T[22] = v; }
{ float v = T[23] * Y[1][11]; PIN(v); T[23] = v; }
SB(); M32A(X[1], AF(1), xb[1][29]); SB();
afh[0] = TR(lbA, 66048);
GLDS(lvoffA, lbase, 0);
{ unsigned u = pk_bf16(T[22], T[23]); PIN(u); hh[3] = u; }
{ float v = T[24] * Y[1][4]; PIN(v); T[24] = v; }
SB(); M32V(X[0], AF(2), xb[0][30]); SB();
afl[1] = TR(lbA, 67584);
*(AS3 u32x4*)(uintptr_t)(hw_ad + 2048) = hh;
{ float v = T[25] * Y[1][5]; PIN(v); T[25] = v; }
{ float v = T[26] * Y[1][6]; PIN(v); T[26] = v; }
SB(); M32V(X[1], AF(2), xb[1][30]); SB();
afh[1] = TR(lbA, 68096);
ST41(hxvo, hh, hxw + 2048);
SB(); M32V(X[0], AF(3), xb[0][31]); SB();
afl[2] = TR(lbA, 69632);
GLDS(lvoffB, lbase, 1024);
{ unsigned u = pk_bf16(T[24], T[25]); PIN(u); hh[0] = u; }
{ float v = T[27] * Y[1][7]; PIN(v); T[27] = v; }
{ float v = T[28] * Y[1][12]; PIN(v); T[28] = v; }
SB(); M32V(X[1], AF(3), xb[1][31]); SB();
afh[2] = TR(lbA, 70144);
lbase += lstep;
SB();
asm volatile("s_waitcnt vmcnt(16)\n\ts_barrier" ::: "memory");
SB(); M32V(X[0], AF(0), xb[0][32]); SB();
afl[3] = TR(lbA, 71680);
{ unsigned u = pk_bf16(T[26], T[27]); PIN(u); hh[1] = u; }
{ float v = T[29] * Y[1][13]; PIN(v); T[29] = v; }
{ float v = T[30] * Y[1][14]; PIN(v); T[30] = v; }
{ float v = T[31] * Y[1][15]; PIN(v); T[31] = v; }
SB(); M32V(X[1], AF(0), xb[1][32]); SB();
afh[3] = TR(lbA, 72192);
{ unsigned u = pk_bf16(T[28], T[29]); PIN(u); hh[2] = u; }
{ unsigned u = pk_bf16(T[30], T[31]); PIN(u); hh[3] = u; }
SB(); M32V(X[0], AF(1), xb[0][33]); SB();
afl[0] = TR(lbA, 0);
SETM0(wv2k + 57344);
*(AS3 u32x4*)(uintptr_t)(hw_ad + 2560) = hh;
SB(); M32V(X[1], AF(1), xb[1][33]); SB();
afh[0] = TR(lbA, 512);
GLDS(lvoffA, lbase, 0);
SB(); M32V(X[0], AF(2), xb[0][34]); SB();
afl[1] = TR(lbA, 2048);
ST41(hxvo, hh, hxw + 2560);
SB(); M32V(X[1], AF(2), xb[1][34]); SB();
afh[1] = TR(lbA, 2560);
SB(); M32V(X[0], AF(3), xb[0][35]); SB();
afl[2] = TR(lbA, 4096);
GLDS(lvoffB, lbase, 1024);
SB(); M32V(X[1], AF(3), xb[1][35]); SB();
afh[2] = TR(lbA, 4608);
lbase += lstep;
}
{
const char* nbase = wgb + 256; const unsigned nstride = 9216u;
SB();
asm volatile("s_waitcnt vmcnt(16)\n\ts_barrier" ::: "memory");
SB(); M32ZA(Y[0], AF(0), xb[0][0]); SB();
afl[3] = TR(lbA, 6144);
T[0] = X[0][0];
T[1] = X[0][1];
T[2] = X[0][2];
T[3] = X[0][3];
T[4] = X[0][8];
T[5] = X[0][9];
T[6] = X[0][10];
T[7] = X[0][11];
T[8] = X[0][4];
T[9] = X[0][5];
T[10] = X[0][6];
T[11] = X[0][7];
T[12] = X[0][12];
T[13] = X[0][13];
T[14] = X[0][14];
T[15] = X[0][15];
T[16] = X[1][0];
T[17] = X[1][1];
T[18] = X[1][2];
T[19] = X[1][3];
T[20] = X[1][8];
T[21] = X[1][9];
T[22] = X[1][10];
T[23] = X[1][11];
T[24] = X[1][4];
T[25] = X[1][5];
T[26] = X[1][6];
T[27] = X[1][7];
T[28] = X[1][12];
T[29] = X[1][13];
T[30] = X[1][14];
T[31] = X[1][15];
SB(); M32ZA(Y[1], AF(0), xb[1][0]); SB();
afh[3] = TR(lbA, 6656);
SB(); M32A(Y[0], AF(1), xb[0][1]); SB();
afl[0] = TR(lbA, 8192);
SETM0(wv2k + 65536);
SB(); M32A(Y[1], AF(1), xb[1][1]); SB();
afh[0] = TR(lbA, 8704);
GLDS(lvoffA, lbase, 0);
SB(); M32A(Y[0], AF(2), xb[0][2]); SB();
afl[1] = TR(lbA, 10240);
SB(); M32A(Y[1], AF(2), xb[1][2]); SB();
afh[1] = TR(lbA, 10752);
SB(); M32A(Y[0], AF(3), xb[0][3]); SB();
afl[2] = TR(lbA, 12288);
GLDS(lvoffB, lbase, 1024);
gt0 = T[0] * T[0]; PIN(gt0);
SB(); M32A(Y[1], AF(3), xb[1][3]); SB();
afh[2] = TR(lbA, 12800);
lbase = nbase; lstep = 64 * nstride; lvoffA = rowA * nstride + c16P1; lvoffB = (rowA + 8) * nstride + c16P1 - 1024;
SB();
asm volatile("s_waitcnt vmcnt(16)\n\ts_barrier" ::: "memory");
SB(); M32A(Y[0], AF(0), xb[0][4]); SB();
afl[3] = TR(lbA, 14336);
gt0 = __builtin_fmaf(gt0, C1, C0); PIN(gt0);
SB(); M32A(Y[1], AF(0), xb[1][4]); SB();
afh[3] = TR(lbA, 14848);
gt0 = T[0] * gt0; PIN(gt0);
gt1 = T[1] * T[1]; PIN(gt1);
SB(); M32A(Y[0], AF(1), xb[0][5]); SB();
afl[0] = TR(lbA, 16384);
SETM0(wv2k + 0);
gt0 = __builtin_amdgcn_exp2f(gt0); PIN(gt0);
gt1 = __builtin_fmaf(gt1, C1, C0); PIN(gt1);
SB(); M32A(Y[1], AF(1), xb[1][5]); SB();
afh[0] = TR(lbA, 16896);
GLDS(lvoffA, lbase, 0);
gt0 = gt0 + 1.0f; PIN(gt0);
gt1 = T[1] * gt1; PIN(gt1);
SB(); M32A(Y[0], AF(2), xb[0][6]); SB();
afl[1] = TR(lbA, 18432);
gt0 = __builtin_amdgcn_rcpf(gt0); PIN(gt0);
gt1 = __builtin_amdgcn_exp2f(gt1); PIN(gt1);
SB(); M32A(Y[1], AF(2), xb[1][6]); SB();
afh[1] = TR(lbA, 18944);
{ float v = T[0] * gt0; PIN(v); T[0] = v; }
gt1 = gt1 + 1.0f; PIN(gt1);
gt2 = T[2] * T[2]; PIN(gt2);
SB(); M32A(Y[0], AF(3), xb[0][7]); SB();
afl[2] = TR(lbA, 20480);
GLDS(lvoffB, lbase, 1024);
gt1 = __builtin_amdgcn_rcpf(gt1); PIN(gt1);
gt2 = __builtin_fmaf(gt2, C1, C0); PIN(gt2);
SB(); M32A(Y[1], AF(3), xb[1][7]); SB();
afh[2] = TR(lbA, 20992);
lbase += lstep;
SB();
asm volatile("s_waitcnt vmcnt(16)\n\ts_barrier" ::: "memory");
SB(); M32A(Y[0], AF(0), xb[0][8]); SB();
afl[3] = TR(lbA, 22528);
{ float v = T[1] * gt1; PIN(v); T[1] = v; }
gt2 = T[2] * gt2; PIN(gt2);
gt0 = T[3] * T[3]; PIN(gt0);
SB(); M32A(Y[1], AF(0), xb[1][8]); SB();
afh[3] = TR(lbA, 23040);
TOUCH(trow * 9216u, wgb + 384);
gt2 = __builtin_amdgcn_exp2f(gt2); PIN(gt2);
gt0 = __builtin_fmaf(gt0, C1, C0); PIN(gt0);
SB(); M32A(Y[0], AF(1), xb[0][9]); SB();
afl[0] = TR(lbA, 24576);
SETM0(wv2k + 8192);
gt2 = gt2 + 1.0f; PIN(gt2);
gt0 = T[3] * gt0; PIN(gt0);
gt1 = T[4] * T[4]; PIN(gt1);
SB(); M32A(Y[1], AF(1), xb[1][9]); SB();
afh[0] = TR(lbA, 25088);
GLDS(lvoffA, lbase, 0);
gt2 = __builtin_amdgcn_rcpf(gt2); PIN(gt2);
SB(); M32A(Y[0], AF(2), xb[0][10]); SB();
afl[1] = TR(lbA, 26624);
{ float v = T[2] * gt2; PIN(v); T[2] = v; }
gt0 = __builtin_amdgcn_exp2f(gt0); PIN(gt0);
gt1 = __builtin_fmaf(gt1, C1, C0); PIN(gt1);
SB(); M32A(Y[1], AF(2), xb[1][10]); SB();
afh[1] = TR(lbA, 27136);
gt0 = gt0 + 1.0f; PIN(gt0);
gt1 = T[4] * gt1; PIN(gt1);
gt2 = T[5] * T[5]; PIN(gt2);
SB(); M32A(Y[0], AF(3), xb[0][11]); SB();
afl[2] = TR(lbA, 28672);
GLDS(lvoffB, lbase, 1024);
gt0 = __builtin_amdgcn_rcpf(gt0); PIN(gt0);
gt2 = __builtin_fmaf(gt2, C1, C0); PIN(gt2);
SB(); M32A(Y[1], AF(3), xb[1][11]); SB();
afh[2] = TR(lbA, 29184);
lbase += lstep;
SB();
asm volatile("s_waitcnt vmcnt(8)\n\ts_barrier" ::: "memory");
SIGNAL_SET(fHme, 1, true);
SB(); M32A(Y[0], AF(0), xb[0][12]); SB();
afl[3] = TR(lbA, 30720);
{ float v = T[3] * gt0; PIN(v); T[3] = v; }
gt1 = __builtin_amdgcn_exp2f(gt1); PIN(gt1);
gt2 = T[5] * gt2; PIN(gt2);
SB(); M32A(Y[1], AF(0), xb[1][12]); SB();
afh[3] = TR(lbA, 31232);
gt1 = gt1 + 1.0f; PIN(gt1);
gt2 = __builtin_amdgcn_exp2f(gt2); PIN(gt2);
SB(); M32A(Y[0], AF(1), xb[0][13]); SB();
afl[0] = TR(lbA, 32768);
SETM0(wv2k + 16384);
gt1 = __builtin_amdgcn_rcpf(gt1); PIN(gt1);
gt2 = gt2 + 1.0f; PIN(gt2);
SB(); M32A(Y[1], AF(1), xb[1][13]); SB();
afh[0] = TR(lbA, 33280);
GLDS(lvoffA, lbase, 0);
{ float v = T[4] * gt1; PIN(v); T[4] = v; }
gt0 = T[6] * T[6]; PIN(gt0);
SB(); M32A(Y[0], AF(2), xb[0][14]); SB();
afl[1] = TR(lbA, 34816);
gt2 = __builtin_amdgcn_rcpf(gt2); PIN(gt2);
gt0 = __builtin_fmaf(gt0, C1, C0); PIN(gt0);
gt1 = T[7] * T[7]; PIN(gt1);
SB(); M32A(Y[1], AF(2), xb[1][14]); SB();
afh[1] = TR(lbA, 35328);
{ float v = T[5] * gt2; PIN(v); T[5] = v; }
gt0 = T[6] * gt0; PIN(gt0);
gt1 = __builtin_fmaf(gt1, C1, C0); PIN(gt1);
SB(); M32A(Y[0], AF(3), xb[0][15]); SB();
afl[2] = TR(lbA, 36864);
GLDS(lvoffB, lbase, 1024);
gt0 = __builtin_amdgcn_exp2f(gt0); PIN(gt0);
gt1 = T[7] * gt1; PIN(gt1);
SB(); M32A(Y[1], AF(3), xb[1][15]); SB();
afh[2] = TR(lbA, 37376);
lbase += lstep;
SB();
asm volatile("s_waitcnt vmcnt(16)\n\ts_barrier" ::: "memory");
SB(); M32A(Y[0], AF(0), xb[0][16]); SB();
afl[3] = TR(lbA, 38912);
gt0 = gt0 + 1.0f; PIN(gt0);
gt1 = __builtin_amdgcn_exp2f(gt1); PIN(gt1);
gt2 = T[8] * T[8]; PIN(gt2);
SB(); M32A(Y[1], AF(0), xb[1][16]); SB();
afh[3] = TR(lbA, 39424);
gt0 = __builtin_amdgcn_rcpf(gt0); PIN(gt0);
gt1 = gt1 + 1.0f; PIN(gt1);
SB(); M32A(Y[0], AF(1), xb[0][17]); SB();
afl[0] = TR(lbA, 40960);
SETM0(wv2k + 24576);
{ float v = T[6] * gt0; PIN(v); T[6] = v; }
gt1 = __builtin_amdgcn_rcpf(gt1); PIN(gt1);
SB(); M32A(Y[1], AF(1), xb[1][17]); SB();
afh[0] = TR(lbA, 41472);
GLDS(lvoffA, lbase, 0);
{ float v = T[7] * gt1; PIN(v); T[7] = v; }
gt2 = __builtin_fmaf(gt2, C1, C0); PIN(gt2);
SB(); M32A(Y[0], AF(2), xb[0][18]); SB();
afl[1] = TR(lbA, 43008);
gt2 = T[8] * gt2; PIN(gt2);
gt0 = T[9] * T[9]; PIN(gt0);
gt1 = T[10] * T[10]; PIN(gt1);
SB(); M32A(Y[1], AF(2), xb[1][18]); SB();
afh[1] = TR(lbA, 43520);
gt2 = __builtin_amdgcn_exp2f(gt2); PIN(gt2);
gt0 = __builtin_fmaf(gt0, C1, C0); PIN(gt0);
SB(); M32A(Y[0], AF(3), xb[0][19]); SB();
afl[2] = TR(lbA, 45056);
GLDS(lvoffB, lbase, 1024);
gt2 = gt2 + 1.0f; PIN(gt2);
gt0 = T[9] * gt0; PIN(gt0);
gt1 = __builtin_fmaf(gt1, C1, C0); PIN(gt1);
SB(); M32A(Y[1], AF(3), xb[1][19]); SB();
afh[2] = TR(lbA, 45568);
lbase += lstep;
SB();
asm volatile("s_waitcnt vmcnt(14)\n\ts_barrier" ::: "memory");
SB(); M32A(Y[0], AF(0), xb[0][20]); SB();
afl[3] = TR(lbA, 47104);
gt2 = __builtin_amdgcn_rcpf(gt2); PIN(gt2);
gt0 = __builtin_amdgcn_exp2f(gt0); PIN(gt0);
SB(); M32A(Y[1], AF(0), xb[1][20]); SB();
afh[3] = TR(lbA, 47616);
{ float v = T[8] * gt2; PIN(v); T[8] = v; }
gt0 = gt0 + 1.0f; PIN(gt0);
gt1 = T[10] * gt1; PIN(gt1);
SB(); M32A(Y[0], AF(1), xb[0][21]); SB();
afl[0] = TR(lbA, 49152);
SETM0(wv2k + 32768);
gt0 = __builtin_amdgcn_rcpf(gt0); PIN(gt0);
gt2 = T[11] * T[11]; PIN(gt2);
SB(); M32A(Y[1], AF(1), xb[1][21]); SB();
afh[0] = TR(lbA, 49664);
GLDS(lvoffA, lbase, 0);
{ float v = T[9] * gt0; PIN(v); T[9] = v; }
gt2 = __builtin_fmaf(gt2, C1, C0); PIN(gt2);
SB(); M32A(Y[0], AF(2), xb[0][22]); SB();
afl[1] = TR(lbA, 51200);
gt1 = __builtin_amdgcn_exp2f(gt1); PIN(gt1);
gt2 = T[11] * gt2; PIN(gt2);
gt0 = T[12] * T[12]; PIN(gt0);
SB(); M32A(Y[1], AF(2), xb[1][22]); SB();
afh[1] = TR(lbA, 51712);
gt1 = gt1 + 1.0f; PIN(gt1);
gt2 = __builtin_amdgcn_exp2f(gt2); PIN(gt2);
SB(); M32A(Y[0], AF(3), xb[0][23]); SB();
afl[2] = TR(lbA, 53248);
GLDS(lvoffB, lbase, 1024);
gt1 = __builtin_amdgcn_rcpf(gt1); PIN(gt1);
gt2 = gt2 + 1.0f; PIN(gt2);
SB(); M32A(Y[1], AF(3), xb[1][23]); SB();
afh[2] = TR(lbA, 53760);
lbase += lstep;
SB();
asm volatile("s_waitcnt vmcnt(13)\n\ts_barrier" ::: "memory");
SB(); M32A(Y[0], AF(0), xb[0][24]); SB();
afl[3] = TR(lbA, 55296);
{ float v = T[10] * gt1; PIN(v); T[10] = v; }
gt2 = __builtin_amdgcn_rcpf(gt2); PIN(gt2);
gt0 = __builtin_fmaf(gt0, C1, C0); PIN(gt0);
SB(); M32A(Y[1], AF(0), xb[1][24]); SB();
afh[3] = TR(lbA, 55808);
{ float v = T[11] * gt2; PIN(v); T[11] = v; }
gt0 = T[12] * gt0; PIN(gt0);
gt1 = T[13] * T[13]; PIN(gt1);
SB(); M32A(Y[0], AF(1), xb[0][25]); SB();
afl[0] = TR(lbA, 57344);
SETM0(wv2k + 40960);
gt0 = __builtin_amdgcn_exp2f(gt0); PIN(gt0);
gt1 = __builtin_fmaf(gt1, C1, C0); PIN(gt1);
SB(); M32A(Y[1], AF(1), xb[1][25]); SB();
afh[0] = TR(lbA, 57856);
GLDS(lvoffA, lbase, 0);
gt0 = gt0 + 1.0f; PIN(gt0);
gt1 = T[13] * gt1; PIN(gt1);
SB(); M32A(Y[0], AF(2), xb[0][26]); SB();
afl[1] = TR(lbA, 59392);
gt0 = __builtin_amdgcn_rcpf(gt0); PIN(gt0);
gt1 = __builtin_amdgcn_exp2f(gt1); PIN(gt1);
SB(); M32A(Y[1], AF(2), xb[1][26]); SB();
afh[1] = TR(lbA, 59904);
{ float v = T[12] * gt0; PIN(v); T[12] = v; }
gt1 = gt1 + 1.0f; PIN(gt1);
gt2 = T[14] * T[14]; PIN(gt2);
SB(); M32A(Y[0], AF(3), xb[0][27]); SB();
afl[2] = TR(lbA, 61440);
GLDS(lvoffB, lbase, 1024);
gt1 = __builtin_amdgcn_rcpf(gt1); PIN(gt1);
gt2 = __builtin_fmaf(gt2, C1, C0); PIN(gt2);
SB(); M32A(Y[1], AF(3), xb[1][27]); SB();
afh[2] = TR(lbA, 61952);
lbase += lstep;
SB();
asm volatile("s_waitcnt vmcnt(13)\n\ts_barrier" ::: "memory");
SB(); M32A(Y[0], AF(0), xb[0][28]); SB();
afl[3] = TR(lbA, 63488);
{ float v = T[13] * gt1; PIN(v); T[13] = v; }
gt2 = T[14] * gt2; PIN(gt2);
gt0 = T[15] * T[15]; PIN(gt0);
SB(); M32A(Y[1], AF(0), xb[1][28]); SB();
afh[3] = TR(lbA, 64000);
gt2 = __builtin_amdgcn_exp2f(gt2); PIN(gt2);
gt0 = __builtin_fmaf(gt0, C1, C0); PIN(gt0);
SB(); M32A(Y[0], AF(1), xb[0][29]); SB();
afl[0] = TR(lbA, 65536);
SETM0(wv2k + 49152);
gt2 = gt2 + 1.0f; PIN(gt2);
gt0 = T[15] * gt0; PIN(gt0);
gt1 = T[16] * T[16]; PIN(gt1);
SB(); M32A(Y[1], AF(1), xb[1][29]); SB();
afh[0] = TR(lbA, 66048);
GLDS(lvoffA, lbase, 0);
gt2 = __builtin_amdgcn_rcpf(gt2); PIN(gt2);
SB(); M32V(Y[0], AF(2), xb[0][30]); SB();
afl[1] = TR(lbA, 67584);
{ float v = T[14] * gt2; PIN(v); T[14] = v; }
gt0 = __builtin_amdgcn_exp2f(gt0); PIN(gt0);
gt1 = __builtin_fmaf(gt1, C1, C0); PIN(gt1);
SB(); M32V(Y[1], AF(2), xb[1][30]); SB();
afh[1] = TR(lbA, 68096);
gt0 = gt0 + 1.0f; PIN(gt0);
gt1 = T[16] * gt1; PIN(gt1);
gt2 = T[17] * T[17]; PIN(gt2);
SB(); M32V(Y[0], AF(3), xb[0][31]); SB();
afl[2] = TR(lbA, 69632);
GLDS(lvoffB, lbase, 1024);
gt0 = __builtin_amdgcn_rcpf(gt0); PIN(gt0);
gt2 = __builtin_fmaf(gt2, C1, C0); PIN(gt2);
SB(); M32V(Y[1], AF(3), xb[1][31]); SB();
afh[2] = TR(lbA, 70144);
lbase += lstep;
SB();
asm volatile("s_waitcnt vmcnt(13)\n\ts_barrier" ::: "memory");
SB(); M32V(Y[0], AF(0), xb[0][32]); SB();
afl[3] = TR(lbA, 71680);
{ float v = T[15] * gt0; PIN(v); T[15] = v; }
gt1 = __builtin_amdgcn_exp2f(gt1); PIN(gt1);
gt2 = T[17] * gt2; PIN(gt2);
SB(); M32V(Y[1], AF(0), xb[1][32]); SB();
afh[3] = TR(lbA, 72192);
gt1 = gt1 + 1.0f; PIN(gt1);
gt2 = __builtin_amdgcn_exp2f(gt2); PIN(gt2);
SB(); M32V(Y[0], AF(1), xb[0][33]); SB();
afl[0] = TR(lbA, 0);
SETM0(wv2k + 57344);
gt1 = __builtin_amdgcn_rcpf(gt1); PIN(gt1);
gt2 = gt2 + 1.0f; PIN(gt2);
SB(); M32V(Y[1], AF(1), xb[1][33]); SB();
afh[0] = TR(lbA, 512);
GLDS(lvoffA, lbase, 0);
{ float v = T[16] * gt1; PIN(v); T[16] = v; }
gt0 = T[18] * T[18]; PIN(gt0);
SB(); M32V(Y[0], AF(2), xb[0][34]); SB();
afl[1] = TR(lbA, 2048);
gt2 = __builtin_amdgcn_rcpf(gt2); PIN(gt2);
gt0 = __builtin_fmaf(gt0, C1, C0); PIN(gt0);
gt1 = T[19] * T[19]; PIN(gt1);
SB(); M32V(Y[1], AF(2), xb[1][34]); SB();
afh[1] = TR(lbA, 2560);
{ float v = T[17] * gt2; PIN(v); T[17] = v; }
gt0 = T[18] * gt0; PIN(gt0);
gt1 = __builtin_fmaf(gt1, C1, C0); PIN(gt1);
SB(); M32V(Y[0], AF(3), xb[0][35]); SB();
afl[2] = TR(lbA, 4096);
GLDS(lvoffB, lbase, 1024);
gt0 = __builtin_amdgcn_exp2f(gt0); PIN(gt0);
gt1 = T[19] * gt1; PIN(gt1);
SB(); M32V(Y[1], AF(3), xb[1][35]); SB();
afh[2] = TR(lbA, 4608);
lbase += lstep;
}
{
const char* nbase = wub + 256; const unsigned nstride = 9216u;
SB();
asm volatile("s_waitcnt vmcnt(12)\n\ts_barrier" ::: "memory");
SB(); M32ZA(X[0], AF(0), xb[0][0]); SB();
afl[3] = TR(lbA, 6144);
gt0 = gt0 + 1.0f; PIN(gt0);
gt1 = __builtin_amdgcn_exp2f(gt1); PIN(gt1);
gt2 = T[20] * T[20]; PIN(gt2);
SB(); M32ZA(X[1], AF(0), xb[1][0]); SB();
afh[3] = TR(lbA, 6656);
gt0 = __builtin_amdgcn_rcpf(gt0); PIN(gt0);
gt1 = gt1 + 1.0f; PIN(gt1);
SB(); M32A(X[0], AF(1), xb[0][1]); SB();
afl[0] = TR(lbA, 8192);
SETM0(wv2k + 65536);
{ float v = T[18] * gt0; PIN(v); T[18] = v; }
gt1 = __builtin_amdgcn_rcpf(gt1); PIN(gt1);
SB(); M32A(X[1], AF(1), xb[1][1]); SB();
afh[0] = TR(lbA, 8704);
GLDS(lvoffA, lbase, 0);
{ float v = T[19] * gt1; PIN(v); T[19] = v; }
gt2 = __builtin_fmaf(gt2, C1, C0); PIN(gt2);
SB(); M32A(X[0], AF(2), xb[0][2]); SB();
afl[1] = TR(lbA, 10240);
gt2 = T[20] * gt2; PIN(gt2);
gt0 = T[21] * T[21]; PIN(gt0);
gt1 = T[22] * T[22]; PIN(gt1);
SB(); M32A(X[1], AF(2), xb[1][2]); SB();
afh[1] = TR(lbA, 10752);
gt2 = __builtin_amdgcn_exp2f(gt2); PIN(gt2);
gt0 = __builtin_fmaf(gt0, C1, C0); PIN(gt0);
SB(); M32A(X[0], AF(3), xb[0][3]); SB();
afl[2] = TR(lbA, 12288);
GLDS(lvoffB, lbase, 1024);
gt2 = gt2 + 1.0f; PIN(gt2);
gt0 = T[21] * gt0; PIN(gt0);
gt1 = __builtin_fmaf(gt1, C1, C0); PIN(gt1);
SB(); M32A(X[1], AF(3), xb[1][3]); SB();
afh[2] = TR(lbA, 12800);
lbase = nbase; lstep = 64 * nstride; lvoffA = rowA * nstride + c16P1; lvoffB = (rowA + 8) * nstride + c16P1 - 1024;
SB();
asm volatile("s_waitcnt vmcnt(12)\n\ts_barrier" ::: "memory");
SB(); M32A(X[0], AF(0), xb[0][4]); SB();
afl[3] = TR(lbA, 14336);
gt2 = __builtin_amdgcn_rcpf(gt2); PIN(gt2);
gt0 = __builtin_amdgcn_exp2f(gt0); PIN(gt0);
SB(); M32A(X[1], AF(0), xb[1][4]); SB();
afh[3] = TR(lbA, 14848);
{ float v = T[20] * gt2; PIN(v); T[20] = v; }
gt0 = gt0 + 1.0f; PIN(gt0);
gt1 = T[22] * gt1; PIN(gt1);
SB(); M32A(X[0], AF(1), xb[0][5]); SB();
afl[0] = TR(lbA, 16384);
SETM0(wv2k + 0);
gt0 = __builtin_amdgcn_rcpf(gt0); PIN(gt0);
gt2 = T[23] * T[23]; PIN(gt2);
SB(); M32A(X[1], AF(1), xb[1][5]); SB();
afh[0] = TR(lbA, 16896);
GLDS(lvoffA, lbase, 0);
{ float v = T[21] * gt0; PIN(v); T[21] = v; }
gt2 = __builtin_fmaf(gt2, C1, C0); PIN(gt2);
SB(); M32A(X[0], AF(2), xb[0][6]); SB();
afl[1] = TR(lbA, 18432);
gt1 = __builtin_amdgcn_exp2f(gt1); PIN(gt1);
gt2 = T[23] * gt2; PIN(gt2);
gt0 = T[24] * T[24]; PIN(gt0);
SB(); M32A(X[1], AF(2), xb[1][6]); SB();
afh[1] = TR(lbA, 18944);
gt1 = gt1 + 1.0f; PIN(gt1);
gt2 = __builtin_amdgcn_exp2f(gt2); PIN(gt2);
SB(); M32A(X[0], AF(3), xb[0][7]); SB();
afl[2] = TR(lbA, 20480);
GLDS(lvoffB, lbase, 1024);
gt1 = __builtin_amdgcn_rcpf(gt1); PIN(gt1);
gt2 = gt2 + 1.0f; PIN(gt2);
SB(); M32A(X[1], AF(3), xb[1][7]); SB();
afh[2] = TR(lbA, 20992);
lbase += lstep;
SB();
asm volatile("s_waitcnt vmcnt(12)\n\ts_barrier" ::: "memory");
SB(); M32A(X[0], AF(0), xb[0][8]); SB();
afl[3] = TR(lbA, 22528);
{ float v = T[22] * gt1; PIN(v); T[22] = v; }
gt2 = __builtin_amdgcn_rcpf(gt2); PIN(gt2);
gt0 = __builtin_fmaf(gt0, C1, C0); PIN(gt0);
SB(); M32A(X[1], AF(0), xb[1][8]); SB();
afh[3] = TR(lbA, 23040);
TOUCH(trow * 9216u, wub + 384);
{ float v = T[23] * gt2; PIN(v); T[23] = v; }
gt0 = T[24] * gt0; PIN(gt0);
gt1 = T[25] * T[25]; PIN(gt1);
SB(); M32A(X[0], AF(1), xb[0][9]); SB();
afl[0] = TR(lbA, 24576);
SETM0(wv2k + 8192);
gt0 = __builtin_amdgcn_exp2f(gt0); PIN(gt0);
gt1 = __builtin_fmaf(gt1, C1, C0); PIN(gt1);
SB(); M32A(X[1], AF(1), xb[1][9]); SB();
afh[0] = TR(lbA, 25088);
GLDS(lvoffA, lbase, 0);
gt0 = gt0 + 1.0f; PIN(gt0);
gt1 = T[25] * gt1; PIN(gt1);
SB(); M32A(X[0], AF(2), xb[0][10]); SB();
afl[1] = TR(lbA, 26624);
gt0 = __builtin_amdgcn_rcpf(gt0); PIN(gt0);
gt1 = __builtin_amdgcn_exp2f(gt1); PIN(gt1);
SB(); M32A(X[1], AF(2), xb[1][10]); SB();
afh[1] = TR(lbA, 27136);
{ float v = T[24] * gt0; PIN(v); T[24] = v; }
gt1 = gt1 + 1.0f; PIN(gt1);
gt2 = T[26] * T[26]; PIN(gt2);
SB(); M32A(X[0], AF(3), xb[0][11]); SB();
afl[2] = TR(lbA, 28672);
GLDS(lvoffB, lbase, 1024);
gt1 = __builtin_amdgcn_rcpf(gt1); PIN(gt1);
gt2 = __builtin_fmaf(gt2, C1, C0); PIN(gt2);
SB(); M32A(X[1], AF(3), xb[1][11]); SB();
afh[2] = TR(lbA, 29184);
lbase += lstep;
SB();
asm volatile("s_waitcnt vmcnt(13)\n\ts_barrier" ::: "memory");
SB(); M32A(X[0], AF(0), xb[0][12]); SB();
afl[3] = TR(lbA, 30720);
{ float v = T[25] * gt1; PIN(v); T[25] = v; }
gt2 = T[26] * gt2; PIN(gt2);
gt0 = T[27] * T[27]; PIN(gt0);
{ float v = T[0] * Y[0][0]; PIN(v); T[0] = v; }
SB(); M32A(X[1], AF(0), xb[1][12]); SB();
afh[3] = TR(lbA, 31232);
gt2 = __builtin_amdgcn_exp2f(gt2); PIN(gt2);
gt0 = __builtin_fmaf(gt0, C1, C0); PIN(gt0);
SB(); M32A(X[0], AF(1), xb[0][13]); SB();
afl[0] = TR(lbA, 32768);
SETM0(wv2k + 16384);
gt2 = gt2 + 1.0f; PIN(gt2);
gt0 = T[27] * gt0; PIN(gt0);
gt1 = T[28] * T[28]; PIN(gt1);
SB(); M32A(X[1], AF(1), xb[1][13]); SB();
afh[0] = TR(lbA, 33280);
GLDS(lvoffA, lbase, 0);
gt2 = __builtin_amdgcn_rcpf(gt2); PIN(gt2);
SB(); M32A(X[0], AF(2), xb[0][14]); SB();
afl[1] = TR(lbA, 34816);
{ float v = T[26] * gt2; PIN(v); T[26] = v; }
gt0 = __builtin_amdgcn_exp2f(gt0); PIN(gt0);
gt1 = __builtin_fmaf(gt1, C1, C0); PIN(gt1);
SB(); M32A(X[1], AF(2), xb[1][14]); SB();
afh[1] = TR(lbA, 35328);
gt0 = gt0 + 1.0f; PIN(gt0);
gt1 = T[28] * gt1; PIN(gt1);
gt2 = T[29] * T[29]; PIN(gt2);
SB(); M32A(X[0], AF(3), xb[0][15]); SB();
afl[2] = TR(lbA, 36864);
GLDS(lvoffB, lbase, 1024);
gt0 = __builtin_amdgcn_rcpf(gt0); PIN(gt0);
gt2 = __builtin_fmaf(gt2, C1, C0); PIN(gt2);
SB(); M32A(X[1], AF(3), xb[1][15]); SB();
afh[2] = TR(lbA, 37376);
lbase += lstep;
SB();
asm volatile("s_waitcnt vmcnt(13)\n\ts_barrier" ::: "memory");
SB(); M32A(X[0], AF(0), xb[0][16]); SB();
afl[3] = TR(lbA, 38912);
{ float v = T[27] * gt0; PIN(v); T[27] = v; }
gt1 = __builtin_amdgcn_exp2f(gt1); PIN(gt1);
gt2 = T[29] * gt2; PIN(gt2);
SB(); M32A(X[1], AF(0), xb[1][16]); SB();
afh[3] = TR(lbA, 39424);
gt1 = gt1 + 1.0f; PIN(gt1);
gt2 = __builtin_amdgcn_exp2f(gt2); PIN(gt2);
SB(); M32A(X[0], AF(1), xb[0][17]); SB();
afl[0] = TR(lbA, 40960);
SETM0(wv2k + 24576);
gt1 = __builtin_amdgcn_rcpf(gt1); PIN(gt1);
gt2 = gt2 + 1.0f; PIN(gt2);
SB(); M32A(X[1], AF(1), xb[1][17]); SB();
afh[0] = TR(lbA, 41472);
GLDS(lvoffA, lbase, 0);
{ float v = T[28] * gt1; PIN(v); T[28] = v; }
gt0 = T[30] * T[30]; PIN(gt0);
SB(); M32A(X[0], AF(2), xb[0][18]); SB();
afl[1] = TR(lbA, 43008);
gt2 = __builtin_amdgcn_rcpf(gt2); PIN(gt2);
gt0 = __builtin_fmaf(gt0, C1, C0); PIN(gt0);
gt1 = T[31] * T[31]; PIN(gt1);
SB(); M32A(X[1], AF(2), xb[1][18]); SB();
afh[1] = TR(lbA, 43520);
{ float v = T[29] * gt2; PIN(v); T[29] = v; }
gt0 = T[30] * gt0; PIN(gt0);
gt1 = __builtin_fmaf(gt1, C1, C0); PIN(gt1);
SB(); M32A(X[0], AF(3), xb[0][19]); SB();
afl[2] = TR(lbA, 45056);
GLDS(lvoffB, lbase, 1024);
gt0 = __builtin_amdgcn_exp2f(gt0); PIN(gt0);
gt1 = T[31] * gt1; PIN(gt1);
SB(); M32A(X[1], AF(3), xb[1][19]); SB();
afh[2] = TR(lbA, 45568);
lbase += lstep;
SB();
asm volatile("s_waitcnt vmcnt(13)\n\ts_barrier" ::: "memory");
SB(); M32A(X[0], AF(0), xb[0][20]); SB();
afl[3] = TR(lbA, 47104);
gt0 = gt0 + 1.0f; PIN(gt0);
gt1 = __builtin_amdgcn_exp2f(gt1); PIN(gt1);
{ float v = T[1] * Y[0][1]; PIN(v); T[1] = v; }
SB(); M32A(X[1], AF(0), xb[1][20]); SB();
afh[3] = TR(lbA, 47616);
gt0 = __builtin_amdgcn_rcpf(gt0); PIN(gt0);
gt1 = gt1 + 1.0f; PIN(gt1);
SB(); M32A(X[0], AF(1), xb[0][21]); SB();
afl[0] = TR(lbA, 49152);
SETM0(wv2k + 32768);
{ float v = T[30] * gt0; PIN(v); T[30] = v; }
gt1 = __builtin_amdgcn_rcpf(gt1); PIN(gt1);
SB(); M32A(X[1], AF(1), xb[1][21]); SB();
afh[0] = TR(lbA, 49664);
GLDS(lvoffA, lbase, 0);
{ float v = T[31] * gt1; PIN(v); T[31] = v; }
{ unsigned u = pk_bf16(T[0], T[1]); PIN(u); hh[0] = u; }
SB(); M32A(X[0], AF(2), xb[0][22]); SB();
afl[1] = TR(lbA, 51200);
{ float v = T[2] * Y[0][2]; PIN(v); T[2] = v; }
{ float v = T[3] * Y[0][3]; PIN(v); T[3] = v; }
{ float v = T[4] * Y[0][8]; PIN(v); T[4] = v; }
{ float v = T[5] * Y[0][9]; PIN(v); T[5] = v; }
SB(); M32A(X[1], AF(2), xb[1][22]); SB();
afh[1] = TR(lbA, 51712);
{ unsigned u = pk_bf16(T[2], T[3]); PIN(u); hh[1] = u; }
{ unsigned u = pk_bf16(T[4], T[5]); PIN(u); hh[2] = u; }
{ float v = T[6] * Y[0][10]; PIN(v); T[6] = v; }
SB(); M32A(X[0], AF(3), xb[0][23]); SB();
afl[2] = TR(lbA, 53248);
GLDS(lvoffB, lbase, 1024);
{ float v = T[7] * Y[0][11]; PIN(v); T[7] = v; }
{ float v = T[8] * Y[0][4]; PIN(v); T[8] = v; }
{ float v = T[9] * Y[0][5]; PIN(v); T[9] = v; }
SB(); M32A(X[1], AF(3), xb[1][23]); SB();
afh[2] = TR(lbA, 53760);
lbase += lstep;
SB();
asm volatile("s_waitcnt vmcnt(13)\n\ts_barrier" ::: "memory");
SB(); M32A(X[0], AF(0), xb[0][24]); SB();
afl[3] = TR(lbA, 55296);
{ unsigned u = pk_bf16(T[6], T[7]); PIN(u); hh[3] = u; }
{ float v = T[10] * Y[0][6]; PIN(v); T[10] = v; }
{ float v = T[11] * Y[0][7]; PIN(v); T[11] = v; }
{ float v = T[12] * Y[0][12]; PIN(v); T[12] = v; }
SB(); M32A(X[1], AF(0), xb[1][24]); SB();
afh[3] = TR(lbA, 55808);
*(AS3 u32x4*)(uintptr_t)(hw_ad + 16384) = hh;
{ float v = T[13] * Y[0][13]; PIN(v); T[13] = v; }
SB(); M32A(X[0], AF(1), xb[0][25]); SB();
afl[0] = TR(lbA, 57344);
SETM0(wv2k + 40960);
ST41(hxvo, hh, hxw + 16384);
SB(); M32A(X[1], AF(1), xb[1][25]); SB();
afh[0] = TR(lbA, 57856);
GLDS(lvoffA, lbase, 0);
{ unsigned u = pk_bf16(T[8], T[9]); PIN(u); hh[0] = u; }
{ unsigned u = pk_bf16(T[10], T[11]); PIN(u); hh[1] = u; }
SB(); M32A(X[0], AF(2), xb[0][26]); SB();
afl[1] = TR(lbA, 59392);
{ unsigned u = pk_bf16(T[12], T[13]); PIN(u); hh[2] = u; }
{ float v = T[14] * Y[0][14]; PIN(v); T[14] = v; }
{ float v = T[15] * Y[0][15]; PIN(v); T[15] = v; }
{ float v = T[16] * Y[1][0]; PIN(v); T[16] = v; }
SB(); M32A(X[1], AF(2), xb[1][26]); SB();
afh[1] = TR(lbA, 59904);
{ unsigned u = pk_bf16(T[14], T[15]); PIN(u); hh[3] = u; }
{ float v = T[17] * Y[1][1]; PIN(v); T[17] = v; }
{ float v = T[18] * Y[1][2]; PIN(v); T[18] = v; }
SB(); M32A(X[0], AF(3), xb[0][27]); SB();
afl[2] = TR(lbA, 61440);
GLDS(lvoffB, lbase, 1024);
*(AS3 u32x4*)(uintptr_t)(hw_ad + 16896) = hh;
{ float v = T[19] * Y[1][3]; PIN(v); T[19] = v; }
SB(); M32A(X[1], AF(3), xb[1][27]); SB();
afh[2] = TR(lbA, 61952);
lbase += lstep;
SB();
asm volatile("s_waitcnt vmcnt(14)\n\ts_barrier" ::: "memory");
SB(); M32A(X[0], AF(0), xb[0][28]); SB();
afl[3] = TR(lbA, 63488);
ST41(hxvo, hh, hxw + 16896);
{ float v = T[20] * Y[1][8]; PIN(v); T[20] = v; }
SB(); M32A(X[1], AF(0), xb[1][28]); SB();
afh[3] = TR(lbA, 64000);
{ unsigned u = pk_bf16(T[16], T[17]); PIN(u); hh[0] = u; }
{ unsigned u = pk_bf16(T[18], T[19]); PIN(u); hh[1] = u; }
{ float v = T[21] * Y[1][9]; PIN(v); T[21] = v; }
SB(); M32A(X[0], AF(1), xb[0][29]); SB();
afl[0] = TR(lbA, 65536);
SETM0(wv2k + 49152);
{ unsigned u = pk_bf16(T[20], T[21]); PIN(u); hh[2] = u; }
{ float v = T[22] * Y[1][10]; PIN(v); T[22] = v; }
{ float v = T[23] * Y[1][11]; PIN(v); T[23] = v; }
SB(); M32A(X[1], AF(1), xb[1][29]); SB();
afh[0] = TR(lbA, 66048);
GLDS(lvoffA, lbase, 0);
{ unsigned u = pk_bf16(T[22], T[23]); PIN(u); hh[3] = u; }
{ float v = T[24] * Y[1][4]; PIN(v); T[24] = v; }
SB(); M32V(X[0], AF(2), xb[0][30]); SB();
afl[1] = TR(lbA, 67584);
*(AS3 u32x4*)(uintptr_t)(hw_ad + 18432) = hh;
{ float v = T[25] * Y[1][5]; PIN(v); T[25] = v; }
{ float v = T[26] * Y[1][6]; PIN(v); T[26] = v; }
SB(); M32V(X[1], AF(2), xb[1][30]); SB();
afh[1] = TR(lbA, 68096);
ST41(hxvo, hh, hxw + 18432);
SB(); M32V(X[0], AF(3), xb[0][31]); SB();
afl[2] = TR(lbA, 69632);
GLDS(lvoffB, lbase, 1024);
{ unsigned u = pk_bf16(T[24], T[25]); PIN(u); hh[0] = u; }
{ float v = T[27] * Y[1][7]; PIN(v); T[27] = v; }
{ float v = T[28] * Y[1][12]; PIN(v); T[28] = v; }
SB(); M32V(X[1], AF(3), xb[1][31]); SB();
afh[2] = TR(lbA, 70144);
lbase += lstep;
SB();
asm volatile("s_waitcnt vmcnt(16)\n\ts_barrier" ::: "memory");
SB(); M32V(X[0], AF(0), xb[0][32]); SB();
afl[3] = TR(lbA, 71680);
{ unsigned u = pk_bf16(T[26], T[27]); PIN(u); hh[1] = u; }
{ float v = T[29] * Y[1][13]; PIN(v); T[29] = v; }
{ float v = T[30] * Y[1][14]; PIN(v); T[30] = v; }
{ float v = T[31] * Y[1][15]; PIN(v); T[31] = v; }
SB(); M32V(X[1], AF(0), xb[1][32]); SB();
afh[3] = TR(lbA, 72192);
{ unsigned u = pk_bf16(T[28], T[29]); PIN(u); hh[2] = u; }
{ unsigned u = pk_bf16(T[30], T[31]); PIN(u); hh[3] = u; }
SB(); M32V(X[0], AF(1), xb[0][33]); SB();
afl[0] = TR(lbA, 0);
SETM0(wv2k + 57344);
*(AS3 u32x4*)(uintptr_t)(hw_ad + 18944) = hh;
SB(); M32V(X[1], AF(1), xb[1][33]); SB();
afh[0] = TR(lbA, 512);
GLDS(lvoffA, lbase, 0);
SB(); M32V(X[0], AF(2), xb[0][34]); SB();
afl[1] = TR(lbA, 2048);
ST41(hxvo, hh, hxw + 18944);
SB(); M32V(X[1], AF(2), xb[1][34]); SB();
afh[1] = TR(lbA, 2560);
SB(); M32V(X[0], AF(3), xb[0][35]); SB();
afl[2] = TR(lbA, 4096);
GLDS(lvoffB, lbase, 1024);
SB(); M32V(X[1], AF(3), xb[1][35]); SB();
afh[2] = TR(lbA, 4608);
lbase += lstep;
}
  const int ln1 = (int)lane_id();
  const int atok = cg * 64 + ln1;
  const int atg = m * 128 + atok;
  const float ASC = 0.23570226039551584f;
  const char* asrc = (a.ws + WS_QKV) + (size_t)(m * 432 + 9 * s) * 1024;
  const char* hsrc = (a.ws + WS_QKV) + (size_t)(mp * 432 + 144 + 9 * s) * 1024 + 896;
  #define ADMA(i) { const int pc_ = (wave + 4 * (i)) < 26 ? (wave + 4 * (i)) : 26; const int ar_ = (pc_ * 57) >> 9; \
      asm volatile("s_mov_b32 m0, %2\n\ts_nop 0\n\tglobal_load_lds_dwordx4 %0, %1 sc1" :: "v"(xo0), "s"(asrc + pc_ * 1024 + ar_ * 138240), "s"(ATT_OFF + 128 + pc_ * 1152) : "memory"); }
  #define HDMA(i) { const int hp_ = (wave + 4 * (i)) < 17 ? (wave + 4 * (i)) : 17; const int ar_ = (hp_ * 57) >> 9; \
      asm volatile("s_mov_b32 m0, %2\n\ts_mov_b64 exec, 0xff\n\tglobal_load_lds_dwordx4 %0, %1 sc1\n\ts_mov_b64 exec, -1" :: "v"(xo0), "s"(hsrc + hp_ * 1024 + ar_ * 138240), "s"(ATT_OFF + 10368 + hp_ * 1152) : "memory"); }

{
const char* nbase = wgb + 384; const unsigned nstride = 9216u;
SB();
asm volatile("s_waitcnt vmcnt(16)\n\ts_barrier" ::: "memory");
SB(); M32ZA(Y[0], AF(0), xb[0][0]); SB();
afl[3] = TR(lbA, 6144);
T[0] = X[0][0];
T[1] = X[0][1];
T[2] = X[0][2];
T[3] = X[0][3];
T[4] = X[0][8];
T[5] = X[0][9];
T[6] = X[0][10];
T[7] = X[0][11];
T[8] = X[0][4];
T[9] = X[0][5];
T[10] = X[0][6];
T[11] = X[0][7];
T[12] = X[0][12];
T[13] = X[0][13];
T[14] = X[0][14];
T[15] = X[0][15];
T[16] = X[1][0];
T[17] = X[1][1];
T[18] = X[1][2];
T[19] = X[1][3];
T[20] = X[1][8];
T[21] = X[1][9];
T[22] = X[1][10];
T[23] = X[1][11];
T[24] = X[1][4];
T[25] = X[1][5];
T[26] = X[1][6];
T[27] = X[1][7];
T[28] = X[1][12];
T[29] = X[1][13];
T[30] = X[1][14];
T[31] = X[1][15];
SB(); M32ZA(Y[1], AF(0), xb[1][0]); SB();
afh[3] = TR(lbA, 6656);
SB(); M32A(Y[0], AF(1), xb[0][1]); SB();
afl[0] = TR(lbA, 8192);
SETM0(wv2k + 65536);
SB(); M32A(Y[1], AF(1), xb[1][1]); SB();
afh[0] = TR(lbA, 8704);
GLDS(lvoffA, lbase, 0);
SB(); M32A(Y[0], AF(2), xb[0][2]); SB();
afl[1] = TR(lbA, 10240);
SB(); M32A(Y[1], AF(2), xb[1][2]); SB();
afh[1] = TR(lbA, 10752);
SB(); M32A(Y[0], AF(3), xb[0][3]); SB();
afl[2] = TR(lbA, 12288);
GLDS(lvoffB, lbase, 1024);
gt0 = T[0] * T[0]; PIN(gt0);
SB(); M32A(Y[1], AF(3), xb[1][3]); SB();
afh[2] = TR(lbA, 12800);
lbase = nbase; lstep = 64 * nstride; lvoffA = rowA * nstride + c16P1; lvoffB = (rowA + 8) * nstride + c16P1 - 1024;
gt0 = __builtin_fmaf(gt0, C1, C0); PIN(gt0);
SB();
asm volatile("s_waitcnt vmcnt(16)\n\ts_barrier" ::: "memory");
SB(); M32A(Y[0], AF(0), xb[0][4]); SB();
afl[3] = TR(lbA, 14336);
gt0 = T[0] * gt0; PIN(gt0);
SB(); M32A(Y[1], AF(0), xb[1][4]); SB();
afh[3] = TR(lbA, 14848);
gt0 = __builtin_amdgcn_exp2f(gt0); PIN(gt0);
gt1 = T[1] * T[1]; PIN(gt1);
SB(); M32A(Y[0], AF(1), xb[0][5]); SB();
afl[0] = TR(lbA, 16384);
SETM0(wv2k + 0);
gt0 = gt0 + 1.0f; PIN(gt0);
gt1 = __builtin_fmaf(gt1, C1, C0); PIN(gt1);
SB(); M32A(Y[1], AF(1), xb[1][5]); SB();
afh[0] = TR(lbA, 16896);
GLDS(lvoffA, lbase, 0);
gt0 = __builtin_amdgcn_rcpf(gt0); PIN(gt0);
SB(); M32A(Y[0], AF(2), xb[0][6]); SB();
afl[1] = TR(lbA, 18432);
{ float v = T[0] * gt0; PIN(v); T[0] = v; }
gt1 = T[1] * gt1; PIN(gt1);
gt2 = T[2] * T[2]; PIN(gt2);
SB(); M32A(Y[1], AF(2), xb[1][6]); SB();
afh[1] = TR(lbA, 18944);
gt1 = __builtin_amdgcn_exp2f(gt1); PIN(gt1);
gt2 = __builtin_fmaf(gt2, C1, C0); PIN(gt2);
SB(); M32A(Y[0], AF(3), xb[0][7]); SB();
afl[2] = TR(lbA, 20480);
GLDS(lvoffB, lbase, 1024);
gt1 = gt1 + 1.0f; PIN(gt1);
gt2 = T[2] * gt2; PIN(gt2);
SB(); M32A(Y[1], AF(3), xb[1][7]); SB();
afh[2] = TR(lbA, 20992);
lbase += lstep;
gt0 = T[3] * T[3]; PIN(gt0);
SB();
asm volatile("s_waitcnt vmcnt(16)\n\ts_barrier" ::: "memory");
SB(); M32A(Y[0], AF(0), xb[0][8]); SB();
afl[3] = TR(lbA, 22528);
gt1 = __builtin_amdgcn_rcpf(gt1); PIN(gt1);
gt2 = __builtin_amdgcn_exp2f(gt2); PIN(gt2);
SB(); M32A(Y[1], AF(0), xb[1][8]); SB();
afh[3] = TR(lbA, 23040);
TOUCH(trow * 9216u, wgb + 512);
{ float v = T[1] * gt1; PIN(v); T[1] = v; }
gt2 = gt2 + 1.0f; PIN(gt2);
gt0 = __builtin_fmaf(gt0, C1, C0); PIN(gt0);
SB(); M32A(Y[0], AF(1), xb[0][9]); SB();
afl[0] = TR(lbA, 24576);
SETM0(wv2k + 8192);
gt2 = __builtin_amdgcn_rcpf(gt2); PIN(gt2);
gt0 = T[3] * gt0; PIN(gt0);
SB(); M32A(Y[1], AF(1), xb[1][9]); SB();
afh[0] = TR(lbA, 25088);
GLDS(lvoffA, lbase, 0);
{ float v = T[2] * gt2; PIN(v); T[2] = v; }
gt1 = T[4] * T[4]; PIN(gt1);
SB(); M32A(Y[0], AF(2), xb[0][10]); SB();
afl[1] = TR(lbA, 26624);
gt0 = __builtin_amdgcn_exp2f(gt0); PIN(gt0);
gt1 = __builtin_fmaf(gt1, C1, C0); PIN(gt1);
SB(); M32A(Y[1], AF(2), xb[1][10]); SB();
afh[1] = TR(lbA, 27136);
gt0 = gt0 + 1.0f; PIN(gt0);
gt1 = T[4] * gt1; PIN(gt1);
gt2 = T[5] * T[5]; PIN(gt2);
SB(); M32A(Y[0], AF(3), xb[0][11]); SB();
afl[2] = TR(lbA, 28672);
GLDS(lvoffB, lbase, 1024);
gt0 = __builtin_amdgcn_rcpf(gt0); PIN(gt0);
gt2 = __builtin_fmaf(gt2, C1, C0); PIN(gt2);
SB(); M32A(Y[1], AF(3), xb[1][11]); SB();
afh[2] = TR(lbA, 29184);
lbase += lstep;
{ float v = T[3] * gt0; PIN(v); T[3] = v; }
SB();
asm volatile("s_waitcnt vmcnt(8)\n\ts_barrier" ::: "memory");
SIGNAL_SET(fHme, 2, true);
SB(); M32A(Y[0], AF(0), xb[0][12]); SB();
afl[3] = TR(lbA, 30720);
gt1 = __builtin_amdgcn_exp2f(gt1); PIN(gt1);
gt2 = T[5] * gt2; PIN(gt2);
gt0 = T[6] * T[6]; PIN(gt0);
SB(); M32A(Y[1], AF(0), xb[1][12]); SB();
afh[3] = TR(lbA, 31232);
gt1 = gt1 + 1.0f; PIN(gt1);
gt2 = __builtin_amdgcn_exp2f(gt2); PIN(gt2);
gt0 = __builtin_fmaf(gt0, C1, C0); PIN(gt0);
SB(); M32A(Y[0], AF(1), xb[0][13]); SB();
afl[0] = TR(lbA, 32768);
SETM0(wv2k + 16384);
gt1 = __builtin_amdgcn_rcpf(gt1); PIN(gt1);
gt2 = gt2 + 1.0f; PIN(gt2);
SB(); M32A(Y[1], AF(1), xb[1][13]); SB();
afh[0] = TR(lbA, 33280);
GLDS(lvoffA, lbase, 0);
{ float v = T[4] * gt1; PIN(v); T[4] = v; }
gt0 = T[6] * gt0; PIN(gt0);
SB(); M32A(Y[0], AF(2), xb[0][14]); SB();
afl[1] = TR(lbA, 34816);
gt2 = __builtin_amdgcn_rcpf(gt2); PIN(gt2);
gt0 = __builtin_amdgcn_exp2f(gt0); PIN(gt0);
SB(); M32A(Y[1], AF(2), xb[1][14]); SB();
afh[1] = TR(lbA, 35328);
{ float v = T[5] * gt2; PIN(v); T[5] = v; }
gt0 = gt0 + 1.0f; PIN(gt0);
gt1 = T[7] * T[7]; PIN(gt1);
SB(); M32A(Y[0], AF(3), xb[0][15]); SB();
afl[2] = TR(lbA, 36864);
GLDS(lvoffB, lbase, 1024);
gt0 = __builtin_amdgcn_rcpf(gt0); PIN(gt0);
gt1 = __builtin_fmaf(gt1, C1, C0); PIN(gt1);
SB(); M32A(Y[1], AF(3), xb[1][15]); SB();
afh[2] = TR(lbA, 37376);
lbase += lstep;
{ float v = T[6] * gt0; PIN(v); T[6] = v; }
SB();
asm volatile("s_waitcnt vmcnt(16)\n\ts_barrier" ::: "memory");
SB(); M32A(Y[0], AF(0), xb[0][16]); SB();
afl[3] = TR(lbA, 38912);
gt1 = T[7] * gt1; PIN(gt1);
gt2 = T[8] * T[8]; PIN(gt2);
SB(); M32A(Y[1], AF(0), xb[1][16]); SB();
afh[3] = TR(lbA, 39424);
gt1 = __builtin_amdgcn_exp2f(gt1); PIN(gt1);
gt2 = __builtin_fmaf(gt2, C1, C0); PIN(gt2);
gt0 = T[9] * T[9]; PIN(gt0);
SB(); M32A(Y[0], AF(1), xb[0][17]); SB();
afl[0] = TR(lbA, 40960);
SETM0(wv2k + 24576);
gt1 = gt1 + 1.0f; PIN(gt1);
gt2 = T[8] * gt2; PIN(gt2);
gt0 = __builtin_fmaf(gt0, C1, C0); PIN(gt0);
SB(); M32A(Y[1], AF(1), xb[1][17]); SB();
afh[0] = TR(lbA, 41472);
GLDS(lvoffA, lbase, 0);
gt1 = __builtin_amdgcn_rcpf(gt1); PIN(gt1);
SB(); M32A(Y[0], AF(2), xb[0][18]); SB();
afl[1] = TR(lbA, 43008);
{ float v = T[7] * gt1; PIN(v); T[7] = v; }
gt2 = __builtin_amdgcn_exp2f(gt2); PIN(gt2);
gt0 = T[9] * gt0; PIN(gt0);
SB(); M32A(Y[1], AF(2), xb[1][18]); SB();
afh[1] = TR(lbA, 43520);
gt2 = gt2 + 1.0f; PIN(gt2);
gt0 = __builtin_amdgcn_exp2f(gt0); PIN(gt0);
gt1 = T[10] * T[10]; PIN(gt1);
SB(); M32A(Y[0], AF(3), xb[0][19]); SB();
afl[2] = TR(lbA, 45056);
GLDS(lvoffB, lbase, 1024);
gt2 = __builtin_amdgcn_rcpf(gt2); PIN(gt2);
gt0 = gt0 + 1.0f; PIN(gt0);
SB(); M32A(Y[1], AF(3), xb[1][19]); SB();
afh[2] = TR(lbA, 45568);
lbase += lstep;
{ float v = T[8] * gt2; PIN(v); T[8] = v; }
SB();
asm volatile("s_waitcnt vmcnt(14)\n\ts_barrier" ::: "memory");
SB(); M32A(Y[0], AF(0), xb[0][20]); SB();
afl[3] = TR(lbA, 47104);
gt0 = __builtin_amdgcn_rcpf(gt0); PIN(gt0);
gt1 = __builtin_fmaf(gt1, C1, C0); PIN(gt1);
gt2 = T[11] * T[11]; PIN(gt2);
SB(); M32A(Y[1], AF(0), xb[1][20]); SB();
afh[3] = TR(lbA, 47616);
{ float v = T[9] * gt0; PIN(v); T[9] = v; }
gt1 = T[10] * gt1; PIN(gt1);
gt2 = __builtin_fmaf(gt2, C1, C0); PIN(gt2);
SB(); M32A(Y[0], AF(1), xb[0][21]); SB();
afl[0] = TR(lbA, 49152);
SETM0(wv2k + 32768);
gt1 = __builtin_amdgcn_exp2f(gt1); PIN(gt1);
gt2 = T[11] * gt2; PIN(gt2);
SB(); M32A(Y[1], AF(1), xb[1][21]); SB();
afh[0] = TR(lbA, 49664);
GLDS(lvoffA, lbase, 0);
gt1 = gt1 + 1.0f; PIN(gt1);
gt0 = T[12] * T[12]; PIN(gt0);
SB(); M32A(Y[0], AF(2), xb[0][22]); SB();
afl[1] = TR(lbA, 51200);
gt1 = __builtin_amdgcn_rcpf(gt1); PIN(gt1);
gt2 = __builtin_amdgcn_exp2f(gt2); PIN(gt2);
SB(); M32A(Y[1], AF(2), xb[1][22]); SB();
afh[1] = TR(lbA, 51712);
{ float v = T[10] * gt1; PIN(v); T[10] = v; }
gt2 = gt2 + 1.0f; PIN(gt2);
gt0 = __builtin_fmaf(gt0, C1, C0); PIN(gt0);
SB(); M32A(Y[0], AF(3), xb[0][23]); SB();
afl[2] = TR(lbA, 53248);
GLDS(lvoffB, lbase, 1024);
gt2 = __builtin_amdgcn_rcpf(gt2); PIN(gt2);
gt0 = T[12] * gt0; PIN(gt0);
SB(); M32A(Y[1], AF(3), xb[1][23]); SB();
afh[2] = TR(lbA, 53760);
lbase += lstep;
{ float v = T[11] * gt2; PIN(v); T[11] = v; }
SB();
asm volatile("s_waitcnt vmcnt(13)\n\ts_barrier" ::: "memory");
SB(); M32A(Y[0], AF(0), xb[0][24]); SB();
afl[3] = TR(lbA, 55296);
gt0 = __builtin_amdgcn_exp2f(gt0); PIN(gt0);
gt1 = T[13] * T[13]; PIN(gt1);
gt2 = T[14] * T[14]; PIN(gt2);
SB(); M32A(Y[1], AF(0), xb[1][24]); SB();
afh[3] = TR(lbA, 55808);
gt0 = gt0 + 1.0f; PIN(gt0);
gt1 = __builtin_fmaf(gt1, C1, C0); PIN(gt1);
gt2 = __builtin_fmaf(gt2, C1, C0); PIN(gt2);
SB(); M32A(Y[0], AF(1), xb[0][25]); SB();
afl[0] = TR(lbA, 57344);
SETM0(wv2k + 40960);
LDW1(pw0, zvo, fQ);
gt1 = T[13] * gt1; PIN(gt1);
SB(); M32A(Y[1], AF(1), xb[1][25]); SB();
afh[0] = TR(lbA, 57856);
GLDS(lvoffA, lbase, 0);
LDW1(pw1, zvo, fQp);
SB(); M32A(Y[0], AF(2), xb[0][26]); SB();
afl[1] = TR(lbA, 59392);
gt0 = __builtin_amdgcn_rcpf(gt0); PIN(gt0);
gt1 = __builtin_amdgcn_exp2f(gt1); PIN(gt1);
SB(); M32A(Y[1], AF(2), xb[1][26]); SB();
afh[1] = TR(lbA, 59904);
{ float v = T[12] * gt0; PIN(v); T[12] = v; }
gt1 = gt1 + 1.0f; PIN(gt1);
gt2 = T[14] * gt2; PIN(gt2);
SB(); M32A(Y[0], AF(3), xb[0][27]); SB();
afl[2] = TR(lbA, 61440);
GLDS(lvoffB, lbase, 1024);
gt1 = __builtin_amdgcn_rcpf(gt1); PIN(gt1);
gt0 = T[15] * T[15]; PIN(gt0);
SB(); M32A(Y[1], AF(3), xb[1][27]); SB();
afh[2] = TR(lbA, 61952);
lbase += lstep;
{ float v = T[13] * gt1; PIN(v); T[13] = v; }
SB();
asm volatile("s_waitcnt vmcnt(15)\n\ts_barrier" ::: "memory");
SB(); M32A(Y[0], AF(0), xb[0][28]); SB();
afl[3] = TR(lbA, 63488);
gt2 = __builtin_amdgcn_exp2f(gt2); PIN(gt2);
gt0 = __builtin_fmaf(gt0, C1, C0); PIN(gt0);
gt1 = T[16] * T[16]; PIN(gt1);
SB(); M32A(Y[1], AF(0), xb[1][28]); SB();
afh[3] = TR(lbA, 64000);
gt2 = gt2 + 1.0f; PIN(gt2);
gt0 = T[15] * gt0; PIN(gt0);
gt1 = __builtin_fmaf(gt1, C1, C0); PIN(gt1);
SB(); M32A(Y[0], AF(1), xb[0][29]); SB();
afl[0] = TR(lbA, 65536);
SETM0(wv2k + 49152);
gt2 = __builtin_amdgcn_rcpf(gt2); PIN(gt2);
gt1 = T[16] * gt1; PIN(gt1);
SB(); M32A(Y[1], AF(1), xb[1][29]); SB();
afh[0] = TR(lbA, 66048);
GLDS(lvoffA, lbase, 0);
{ float v = T[14] * gt2; PIN(v); T[14] = v; }
SB(); M32V(Y[0], AF(2), xb[0][30]); SB();
afl[1] = TR(lbA, 67584);
gt0 = __builtin_amdgcn_exp2f(gt0); PIN(gt0);
gt1 = __builtin_amdgcn_exp2f(gt1); PIN(gt1);
SB(); M32V(Y[1], AF(2), xb[1][30]); SB();
afh[1] = TR(lbA, 68096);
gt0 = gt0 + 1.0f; PIN(gt0);
gt1 = gt1 + 1.0f; PIN(gt1);
gt2 = T[17] * T[17]; PIN(gt2);
SB(); M32V(Y[0], AF(3), xb[0][31]); SB();
afl[2] = TR(lbA, 69632);
GLDS(lvoffB, lbase, 1024);
gt0 = __builtin_amdgcn_rcpf(gt0); PIN(gt0);
gt2 = __builtin_fmaf(gt2, C1, C0); PIN(gt2);
SB(); M32V(Y[1], AF(3), xb[1][31]); SB();
afh[2] = TR(lbA, 70144);
lbase += lstep;
{ float v = T[15] * gt0; PIN(v); T[15] = v; }
SB();
asm volatile("s_waitcnt vmcnt(15)\n\ts_barrier" ::: "memory");
SB(); M32V(Y[0], AF(0), xb[0][32]); SB();
afl[3] = TR(lbA, 71680);
gt1 = __builtin_amdgcn_rcpf(gt1); PIN(gt1);
gt2 = T[17] * gt2; PIN(gt2);
gt0 = T[18] * T[18]; PIN(gt0);
SB(); M32V(Y[1], AF(0), xb[1][32]); SB();
afh[3] = TR(lbA, 72192);
{ float v = T[16] * gt1; PIN(v); T[16] = v; }
gt2 = __builtin_amdgcn_exp2f(gt2); PIN(gt2);
gt0 = __builtin_fmaf(gt0, C1, C0); PIN(gt0);
SB(); M32V(Y[0], AF(1), xb[0][33]); SB();
afl[0] = TR(lbA, 0);
SETM0(wv2k + 57344);
gt2 = gt2 + 1.0f; PIN(gt2);
gt0 = T[18] * gt0; PIN(gt0);
gt1 = T[19] * T[19]; PIN(gt1);
SB(); M32V(Y[1], AF(1), xb[1][33]); SB();
afh[0] = TR(lbA, 512);
GLDS(lvoffA, lbase, 0);
gt2 = __builtin_amdgcn_rcpf(gt2); PIN(gt2);
SB(); M32V(Y[0], AF(2), xb[0][34]); SB();
afl[1] = TR(lbA, 2048);
{ float v = T[17] * gt2; PIN(v); T[17] = v; }
gt0 = __builtin_amdgcn_exp2f(gt0); PIN(gt0);
gt1 = __builtin_fmaf(gt1, C1, C0); PIN(gt1);
SB(); M32V(Y[1], AF(2), xb[1][34]); SB();
afh[1] = TR(lbA, 2560);
gt0 = gt0 + 1.0f; PIN(gt0);
gt1 = T[19] * gt1; PIN(gt1);
gt2 = T[20] * T[20]; PIN(gt2);
SB(); M32V(Y[0], AF(3), xb[0][35]); SB();
afl[2] = TR(lbA, 4096);
GLDS(lvoffB, lbase, 1024);
gt0 = __builtin_amdgcn_rcpf(gt0); PIN(gt0);
gt2 = __builtin_fmaf(gt2, C1, C0); PIN(gt2);
SB(); M32V(Y[1], AF(3), xb[1][35]); SB();
afh[2] = TR(lbA, 4608);
lbase += lstep;
{ float v = T[18] * gt0; PIN(v); T[18] = v; }
}
{
const char* nbase = wub + 384; const unsigned nstride = 9216u;
SB();
asm volatile("s_waitcnt vmcnt(5)\n\ts_barrier" : "+v"(pw0), "+v"(pw1) :: "memory");
CHECK_GE(pw0, fQ, 27);
CHECK_GE(pw1, fQp, 27);
SB(); M32ZA(X[0], AF(0), xb[0][0]); SB();
afl[3] = TR(lbA, 6144);
gt1 = __builtin_amdgcn_exp2f(gt1); PIN(gt1);
gt2 = T[20] * gt2; PIN(gt2);
gt0 = T[21] * T[21]; PIN(gt0);
SB(); M32ZA(X[1], AF(0), xb[1][0]); SB();
afh[3] = TR(lbA, 6656);
ADMA(0);
gt1 = gt1 + 1.0f; PIN(gt1);
SB(); M32A(X[0], AF(1), xb[0][1]); SB();
afl[0] = TR(lbA, 8192);
SETM0(wv2k + 65536);
gt1 = __builtin_amdgcn_rcpf(gt1); PIN(gt1);
gt0 = __builtin_fmaf(gt0, C1, C0); PIN(gt0);
SB(); M32A(X[1], AF(1), xb[1][1]); SB();
afh[0] = TR(lbA, 8704);
GLDS(lvoffA, lbase, 0);
{ float v = T[19] * gt1; PIN(v); T[19] = v; }
gt0 = T[21] * gt0; PIN(gt0);
SB(); M32A(X[0], AF(2), xb[0][2]); SB();
afl[1] = TR(lbA, 10240);
gt2 = __builtin_amdgcn_exp2f(gt2); PIN(gt2);
gt0 = __builtin_amdgcn_exp2f(gt0); PIN(gt0);
SB(); M32A(X[1], AF(2), xb[1][2]); SB();
afh[1] = TR(lbA, 10752);
gt2 = gt2 + 1.0f; PIN(gt2);
gt0 = gt0 + 1.0f; PIN(gt0);
gt1 = T[22] * T[22]; PIN(gt1);
SB(); M32A(X[0], AF(3), xb[0][3]); SB();
afl[2] = TR(lbA, 12288);
GLDS(lvoffB, lbase, 1024);
gt2 = __builtin_amdgcn_rcpf(gt2); PIN(gt2);
gt1 = __builtin_fmaf(gt1, C1, C0); PIN(gt1);
SB(); M32A(X[1], AF(3), xb[1][3]); SB();
afh[2] = TR(lbA, 12800);
lbase = nbase; lstep = 64 * nstride; lvoffA = rowA * nstride + c16P1; lvoffB = (rowA + 8) * nstride + c16P1 - 1024;
ADMA(1);
ADMA(2);
ADMA(3);
SB();
asm volatile("s_waitcnt vmcnt(18)\n\ts_barrier" ::: "memory");
SB(); M32A(X[0], AF(0), xb[0][4]); SB();
afl[3] = TR(lbA, 14336);
ADMA(4);
{ float v = T[20] * gt2; PIN(v); T[20] = v; }
gt1 = T[22] * gt1; PIN(gt1);
SB(); M32A(X[1], AF(0), xb[1][4]); SB();
afh[3] = TR(lbA, 14848);
ADMA(5);
gt2 = T[23] * T[23]; PIN(gt2);
SB(); M32A(X[0], AF(1), xb[0][5]); SB();
afl[0] = TR(lbA, 16384);
SETM0(wv2k + 0);
gt0 = __builtin_amdgcn_rcpf(gt0); PIN(gt0);
gt2 = __builtin_fmaf(gt2, C1, C0); PIN(gt2);
SB(); M32A(X[1], AF(1), xb[1][5]); SB();
afh[0] = TR(lbA, 16896);
GLDS(lvoffA, lbase, 0);
{ float v = T[21] * gt0; PIN(v); T[21] = v; }
gt2 = T[23] * gt2; PIN(gt2);
SB(); M32A(X[0], AF(2), xb[0][6]); SB();
afl[1] = TR(lbA, 18432);
gt1 = __builtin_amdgcn_exp2f(gt1); PIN(gt1);
gt2 = __builtin_amdgcn_exp2f(gt2); PIN(gt2);
SB(); M32A(X[1], AF(2), xb[1][6]); SB();
afh[1] = TR(lbA, 18944);
gt1 = gt1 + 1.0f; PIN(gt1);
gt2 = gt2 + 1.0f; PIN(gt2);
gt0 = T[24] * T[24]; PIN(gt0);
{ float v = T[0] * Y[0][0]; PIN(v); T[0] = v; }
SB(); M32A(X[0], AF(3), xb[0][7]); SB();
afl[2] = TR(lbA, 20480);
GLDS(lvoffB, lbase, 1024);
gt1 = __builtin_amdgcn_rcpf(gt1); PIN(gt1);
gt0 = __builtin_fmaf(gt0, C1, C0); PIN(gt0);
SB(); M32A(X[1], AF(3), xb[1][7]); SB();
afh[2] = TR(lbA, 20992);
lbase += lstep;
ADMA(6);
HDMA(0);
SB();
asm volatile("s_waitcnt vmcnt(22)\n\ts_barrier" ::: "memory");
SB(); M32A(X[0], AF(0), xb[0][8]); SB();
afl[3] = TR(lbA, 22528);
HDMA(1);
{ float v = T[22] * gt1; PIN(v); T[22] = v; }
SB(); M32A(X[1], AF(0), xb[1][8]); SB();
afh[3] = TR(lbA, 23040);
TOUCH(trow * 9216u, wub + 512);
HDMA(2);
SB(); M32A(X[0], AF(1), xb[0][9]); SB();
afl[0] = TR(lbA, 24576);
SETM0(wv2k + 8192);
gt2 = __builtin_amdgcn_rcpf(gt2); PIN(gt2);
gt0 = T[24] * gt0; PIN(gt0);
SB(); M32A(X[1], AF(1), xb[1][9]); SB();
afh[0] = TR(lbA, 25088);
GLDS(lvoffA, lbase, 0);
{ float v = T[23] * gt2; PIN(v); T[23] = v; }
gt1 = T[25] * T[25]; PIN(gt1);
SB(); M32A(X[0], AF(2), xb[0][10]); SB();
afl[1] = TR(lbA, 26624);
gt0 = __builtin_amdgcn_exp2f(gt0); PIN(gt0);
gt1 = __builtin_fmaf(gt1, C1, C0); PIN(gt1);
gt2 = T[26] * T[26]; PIN(gt2);
SB(); M32A(X[1], AF(2), xb[1][10]); SB();
afh[1] = TR(lbA, 27136);
gt0 = gt0 + 1.0f; PIN(gt0);
gt1 = T[25] * gt1; PIN(gt1);
gt2 = __builtin_fmaf(gt2, C1, C0); PIN(gt2);
{ float v = T[1] * Y[0][1]; PIN(v); T[1] = v; }
SB(); M32A(X[0], AF(3), xb[0][11]); SB();
afl[2] = TR(lbA, 28672);
GLDS(lvoffB, lbase, 1024);
gt0 = __builtin_amdgcn_rcpf(gt0); PIN(gt0);
gt2 = T[26] * gt2; PIN(gt2);
SB(); M32A(X[1], AF(3), xb[1][11]); SB();
afh[2] = TR(lbA, 29184);
lbase += lstep;
HDMA(3);
HDMA(4);
SB();
asm volatile("s_waitcnt vmcnt(27)\n\ts_barrier" ::: "memory");
SB(); M32A(X[0], AF(0), xb[0][12]); SB();
afl[3] = TR(lbA, 30720);
{ float v = T[24] * gt0; PIN(v); T[24] = v; }
gt1 = __builtin_amdgcn_exp2f(gt1); PIN(gt1);
{ unsigned u = pk_bf16(T[0], T[1]); PIN(u); hh[0] = u; }
SB(); M32A(X[1], AF(0), xb[1][12]); SB();
afh[3] = TR(lbA, 31232);
gt1 = gt1 + 1.0f; PIN(gt1);
gt2 = __builtin_amdgcn_exp2f(gt2); PIN(gt2);
gt0 = T[27] * T[27]; PIN(gt0);
SB(); M32A(X[0], AF(1), xb[0][13]); SB();
afl[0] = TR(lbA, 32768);
SETM0(wv2k + 16384);
gt1 = __builtin_amdgcn_rcpf(gt1); PIN(gt1);
gt2 = gt2 + 1.0f; PIN(gt2);
SB(); M32A(X[1], AF(1), xb[1][13]); SB();
afh[0] = TR(lbA, 33280);
GLDS(lvoffA, lbase, 0);
{ float v = T[25] * gt1; PIN(v); T[25] = v; }
gt0 = __builtin_fmaf(gt0, C1, C0); PIN(gt0);
SB(); M32A(X[0], AF(2), xb[0][14]); SB();
afl[1] = TR(lbA, 34816);
gt2 = __builtin_amdgcn_rcpf(gt2); PIN(gt2);
gt0 = T[27] * gt0; PIN(gt0);
gt1 = T[28] * T[28]; PIN(gt1);
SB(); M32A(X[1], AF(2), xb[1][14]); SB();
afh[1] = TR(lbA, 35328);
{ float v = T[26] * gt2; PIN(v); T[26] = v; }
gt0 = __builtin_amdgcn_exp2f(gt0); PIN(gt0);
gt1 = __builtin_fmaf(gt1, C1, C0); PIN(gt1);
SB(); M32A(X[0], AF(3), xb[0][15]); SB();
afl[2] = TR(lbA, 36864);
GLDS(lvoffB, lbase, 1024);
gt0 = gt0 + 1.0f; PIN(gt0);
gt1 = T[28] * gt1; PIN(gt1);
gt2 = T[29] * T[29]; PIN(gt2);
SB(); M32A(X[1], AF(3), xb[1][15]); SB();
afh[2] = TR(lbA, 37376);
lbase += lstep;
gt2 = __builtin_fmaf(gt2, C1, C0); PIN(gt2);
SB();
asm volatile("s_waitcnt vmcnt(25)\n\ts_barrier" ::: "memory");
SB(); M32A(X[0], AF(0), xb[0][16]); SB();
afl[3] = TR(lbA, 38912);
gt0 = __builtin_amdgcn_rcpf(gt0); PIN(gt0);
gt1 = __builtin_amdgcn_exp2f(gt1); PIN(gt1);
SB(); M32A(X[1], AF(0), xb[1][16]); SB();
afh[3] = TR(lbA, 39424);
{ float v = T[27] * gt0; PIN(v); T[27] = v; }
gt1 = gt1 + 1.0f; PIN(gt1);
gt2 = T[29] * gt2; PIN(gt2);
{ float v = T[2] * Y[0][2]; PIN(v); T[2] = v; }
SB(); M32A(X[0], AF(1), xb[0][17]); SB();
afl[0] = TR(lbA, 40960);
SETM0(wv2k + 24576);
gt1 = __builtin_amdgcn_rcpf(gt1); PIN(gt1);
gt0 = T[30] * T[30]; PIN(gt0);
SB(); M32A(X[1], AF(1), xb[1][17]); SB();
afh[0] = TR(lbA, 41472);
GLDS(lvoffA, lbase, 0);
{ float v = T[28] * gt1; PIN(v); T[28] = v; }
gt0 = __builtin_fmaf(gt0, C1, C0); PIN(gt0);
SB(); M32A(X[0], AF(2), xb[0][18]); SB();
afl[1] = TR(lbA, 43008);
gt2 = __builtin_amdgcn_exp2f(gt2); PIN(gt2);
gt0 = T[30] * gt0; PIN(gt0);
gt1 = T[31] * T[31]; PIN(gt1);
SB(); M32A(X[1], AF(2), xb[1][18]); SB();
afh[1] = TR(lbA, 43520);
gt2 = gt2 + 1.0f; PIN(gt2);
gt0 = __builtin_amdgcn_exp2f(gt0); PIN(gt0);
gt1 = __builtin_fmaf(gt1, C1, C0); PIN(gt1);
SB(); M32A(X[0], AF(3), xb[0][19]); SB();
afl[2] = TR(lbA, 45056);
GLDS(lvoffB, lbase, 1024);
gt2 = __builtin_amdgcn_rcpf(gt2); PIN(gt2);
gt0 = gt0 + 1.0f; PIN(gt0);
SB(); M32A(X[1], AF(3), xb[1][19]); SB();
afh[2] = TR(lbA, 45568);
lbase += lstep;
{ float v = T[29] * gt2; PIN(v); T[29] = v; }
SB();
asm volatile("s_waitcnt vmcnt(25)\n\ts_barrier" ::: "memory");
SB(); M32A(X[0], AF(0), xb[0][20]); SB();
afl[3] = TR(lbA, 47104);
gt0 = __builtin_amdgcn_rcpf(gt0); PIN(gt0);
gt1 = T[31] * gt1; PIN(gt1);
{ float v = T[3] * Y[0][3]; PIN(v); T[3] = v; }
SB(); M32A(X[1], AF(0), xb[1][20]); SB();
afh[3] = TR(lbA, 47616);
{ float v = T[30] * gt0; PIN(v); T[30] = v; }
gt1 = __builtin_amdgcn_exp2f(gt1); PIN(gt1);
{ unsigned u = pk_bf16(T[2], T[3]); PIN(u); hh[1] = u; }
SB(); M32A(X[0], AF(1), xb[0][21]); SB();
afl[0] = TR(lbA, 49152);
SETM0(wv2k + 32768);
gt1 = gt1 + 1.0f; PIN(gt1);
{ float v = T[4] * Y[0][8]; PIN(v); T[4] = v; }
{ float v = T[5] * Y[0][9]; PIN(v); T[5] = v; }
SB(); M32A(X[1], AF(1), xb[1][21]); SB();
afh[0] = TR(lbA, 49664);
GLDS(lvoffA, lbase, 0);
gt1 = __builtin_amdgcn_rcpf(gt1); PIN(gt1);
SB(); M32A(X[0], AF(2), xb[0][22]); SB();
afl[1] = TR(lbA, 51200);
{ float v = T[31] * gt1; PIN(v); T[31] = v; }
{ unsigned u = pk_bf16(T[4], T[5]); PIN(u); hh[2] = u; }
{ float v = T[6] * Y[0][10]; PIN(v); T[6] = v; }
{ float v = T[7] * Y[0][11]; PIN(v); T[7] = v; }
SB(); M32A(X[1], AF(2), xb[1][22]); SB();
afh[1] = TR(lbA, 51712);
{ unsigned u = pk_bf16(T[6], T[7]); PIN(u); hh[3] = u; }
{ float v = T[8] * Y[0][4]; PIN(v); T[8] = v; }
{ float v = T[9] * Y[0][5]; PIN(v); T[9] = v; }
{ float v = T[10] * Y[0][6]; PIN(v); T[10] = v; }
SB(); M32A(X[0], AF(3), xb[0][23]); SB();
afl[2] = TR(lbA, 53248);
GLDS(lvoffB, lbase, 1024);
*(AS3 u32x4*)(uintptr_t)(hw_ad + 32768) = hh;
{ float v = T[11] * Y[0][7]; PIN(v); T[11] = v; }
SB(); M32A(X[1], AF(3), xb[1][23]); SB();
afh[2] = TR(lbA, 53760);
lbase += lstep;
{ float v = T[12] * Y[0][12]; PIN(v); T[12] = v; }
SB();
asm volatile("s_waitcnt vmcnt(25)\n\ts_barrier" ::: "memory");
SB(); M32A(X[0], AF(0), xb[0][24]); SB();
afl[3] = TR(lbA, 55296);
ST41(hxvo, hh, hxw + 32768);
{ float v = T[13] * Y[0][13]; PIN(v); T[13] = v; }
SB(); M32A(X[1], AF(0), xb[1][24]); SB();
afh[3] = TR(lbA, 55808);
{ unsigned u = pk_bf16(T[8], T[9]); PIN(u); hh[0] = u; }
{ unsigned u = pk_bf16(T[10], T[11]); PIN(u); hh[1] = u; }
{ unsigned u = pk_bf16(T[12], T[13]); PIN(u); hh[2] = u; }
{ float v = T[14] * Y[0][14]; PIN(v); T[14] = v; }
SB(); M32A(X[0], AF(1), xb[0][25]); SB();
afl[0] = TR(lbA, 57344);
SETM0(wv2k + 40960);
{ float v = T[15] * Y[0][15]; PIN(v); T[15] = v; }
{ float v = T[16] * Y[1][0]; PIN(v); T[16] = v; }
{ float v = T[17] * Y[1][1]; PIN(v); T[17] = v; }
SB(); M32A(X[1], AF(1), xb[1][25]); SB();
afh[0] = TR(lbA, 57856);
GLDS(lvoffA, lbase, 0);
{ unsigned u = pk_bf16(T[14], T[15]); PIN(u); hh[3] = u; }
{ float v = T[18] * Y[1][2]; PIN(v); T[18] = v; }
SB(); M32A(X[0], AF(2), xb[0][26]); SB();
afl[1] = TR(lbA, 59392);
*(AS3 u32x4*)(uintptr_t)(hw_ad + 33280) = hh;
{ float v = T[19] * Y[1][3]; PIN(v); T[19] = v; }
{ float v = T[20] * Y[1][8]; PIN(v); T[20] = v; }
SB(); M32A(X[1], AF(2), xb[1][26]); SB();
afh[1] = TR(lbA, 59904);
ST41(hxvo, hh, hxw + 33280);
{ float v = T[21] * Y[1][9]; PIN(v); T[21] = v; }
SB(); M32A(X[0], AF(3), xb[0][27]); SB();
afl[2] = TR(lbA, 61440);
GLDS(lvoffB, lbase, 1024);
{ unsigned u = pk_bf16(T[16], T[17]); PIN(u); hh[0] = u; }
{ unsigned u = pk_bf16(T[18], T[19]); PIN(u); hh[1] = u; }
{ unsigned u = pk_bf16(T[20], T[21]); PIN(u); hh[2] = u; }
SB(); M32A(X[1], AF(3), xb[1][27]); SB();
afh[2] = TR(lbA, 61952);
lbase += lstep;
{ float v = T[22] * Y[1][10]; PIN(v); T[22] = v; }
SB();
asm volatile("s_waitcnt vmcnt(26)\n\ts_barrier" ::: "memory");
SB(); M32A(X[0], AF(0), xb[0][28]); SB();
afl[3] = TR(lbA, 63488);
{ float v = T[23] * Y[1][11]; PIN(v); T[23] = v; }
{ float v = T[24] * Y[1][4]; PIN(v); T[24] = v; }
{ float v = T[25] * Y[1][5]; PIN(v); T[25] = v; }
{ float v = T[26] * Y[1][6]; PIN(v); T[26] = v; }
SB(); M32A(X[1], AF(0), xb[1][28]); SB();
afh[3] = TR(lbA, 64000);
{ unsigned u = pk_bf16(T[22], T[23]); PIN(u); hh[3] = u; }
{ float v = T[27] * Y[1][7]; PIN(v); T[27] = v; }
{ float v = T[28] * Y[1][12]; PIN(v); T[28] = v; }
{ float v = T[29] * Y[1][13]; PIN(v); T[29] = v; }
SB(); M32A(X[0], AF(1), xb[0][29]); SB();
afl[0] = TR(lbA, 65536);
SETM0(wv2k + 49152);
*(AS3 u32x4*)(uintptr_t)(hw_ad + 34816) = hh;
{ float v = T[30] * Y[1][14]; PIN(v); T[30] = v; }
SB(); M32A(X[1], AF(1), xb[1][29]); SB();
afh[0] = TR(lbA, 66048);
GLDS(lvoffA, lbase, 0);
{ float v = T[31] * Y[1][15]; PIN(v); T[31] = v; }
SB(); M32V(X[0], AF(2), xb[0][30]); SB();
afl[1] = TR(lbA, 67584);
ST41(hxvo, hh, hxw + 34816);
SB(); M32V(X[1], AF(2), xb[1][30]); SB();
afh[1] = TR(lbA, 68096);
{ unsigned u = pk_bf16(T[24], T[25]); PIN(u); hh[0] = u; }
{ unsigned u = pk_bf16(T[26], T[27]); PIN(u); hh[1] = u; }
{ unsigned u = pk_bf16(T[28], T[29]); PIN(u); hh[2] = u; }
{ unsigned u = pk_bf16(T[30], T[31]); PIN(u); hh[3] = u; }
SB(); M32V(X[0], AF(3), xb[0][31]); SB();
afl[2] = TR(lbA, 69632);
GLDS(lvoffB, lbase, 1024);
*(AS3 u32x4*)(uintptr_t)(hw_ad + 35328) = hh;
SB(); M32V(X[1], AF(3), xb[1][31]); SB();
afh[2] = TR(lbA, 70144);
lbase += lstep;
SB();
asm volatile("s_waitcnt vmcnt(22)\n\ts_barrier" ::: "memory");
SB(); M32V(X[0], AF(0), xb[0][32]); SB();
afl[3] = TR(lbA, 71680);
ST41(hxvo, hh, hxw + 35328);
SB(); M32V(X[1], AF(0), xb[1][32]); SB();
afh[3] = TR(lbA, 72192);
SB(); M32V(X[0], AF(1), xb[0][33]); SB();
afl[0] = TR(lbA, 0);
SETM0(wv2k + 57344);
SB(); M32V(X[1], AF(1), xb[1][33]); SB();
afh[0] = TR(lbA, 512);
GLDS(lvoffA, lbase, 0);
SB(); M32V(X[0], AF(2), xb[0][34]); SB();
afl[1] = TR(lbA, 2048);
SB(); M32V(X[1], AF(2), xb[1][34]); SB();
afh[1] = TR(lbA, 2560);
SB(); M32V(X[0], AF(3), xb[0][35]); SB();
afl[2] = TR(lbA, 4096);
GLDS(lvoffB, lbase, 1024);
SB(); M32V(X[1], AF(3), xb[1][35]); SB();
afh[2] = TR(lbA, 4608);
lbase += lstep;
}
STAMP(4); JIT(4);
TOUCH(trow * 1152u, wdb + 0);
TOUCH(trow * 1152u, wdb + 128);
TOUCH(trow * 1152u, wdb + 256);
TOUCH(trow * 1152u, wdb + 384);
SB(); asm volatile("s_waitcnt vmcnt(20)\n\ts_barrier" ::: "memory");
  {
    const unsigned ab4 = ATT_OFF + th * 5 * 1152 + (2 + atok) * 8;
    const unsigned ab1 = ATT_OFF + 4 * 1152 + (2 + atok) * 8 + th * 4;
    unsigned aq[9], ak0[9], ak1[9], ap[4], au;
    float lg[8], ao[18], am, as_;
    const unsigned ovo = (unsigned)((atok >> 5) * 36864 + (atok & 31) * 16);
    const char* odst = (a.ws + WS_OF) + (size_t)m * 4 * 36864;
    auto ochunk = [&](int gc) { return (gc >> 2) * 1024 + ((gc >> 1) & 1) * 512 + (gc & 1) * 8; };
    #define osoff(c) ochunk(9 * s + th * 5 + (c))
    const int osoff1 = ochunk(9 * s + 4) + th * 4;
    #define AROW(dst, arr, w) { _Pragma("unroll") for (int cc = 0; cc < 4; ++cc) { u32x2 t_ = *(AS3 u32x2*)(uintptr_t)(ab4 + (arr) * 10368 + (14 - 2 * (w)) * 8 + cc * 1152); dst[2 * cc] = t_[0]; dst[2 * cc + 1] = t_[1]; } \
                                dst[8] = *(AS3 unsigned*)(uintptr_t)(ab1 + (arr) * 10368 + (14 - 2 * (w)) * 8); }
    unsigned bk0[9], bk1[9];
    #define QK(dst, w) { float acc_ = 0.f; _Pragma("unroll") for (int d = 0; d < 9; ++d) acc_ = dot2bf(aq[d], dst[d], acc_); lg[w] = acc_; }
    #define PV(r0, r1, p) { _Pragma("unroll") for (int d = 0; d < 9; ++d) { \
        ao[2 * d] = dot2bf(__builtin_amdgcn_perm(r1[d], r0[d], 0x05040100u), ap[p], (p) == 0 ? 0.f : ao[2 * d]); \
        ao[2 * d + 1] = dot2bf(__builtin_amdgcn_perm(r1[d], r0[d], 0x07060302u), ap[p], (p) == 0 ? 0.f : ao[2 * d + 1]); } }
    AROW(aq, 0, 0); AROW(ak0, 1, 0); AROW(ak1, 1, 1); AROW(bk0, 1, 2); AROW(bk1, 1, 3); SB();
    QK(ak0, 0); QK(ak1, 1); SB();
    AROW(ak0, 1, 4); AROW(ak1, 1, 5); SB();
    QK(bk0, 2); QK(bk1, 3); SB();
    AROW(bk0, 1, 6); AROW(bk1, 1, 7); SB();
    QK(ak0, 4); QK(ak1, 5); SB();
    AROW(ak0, 2, 0); AROW(ak1, 2, 1); SB();
    QK(bk0, 6); QK(bk1, 7); SB();
    AROW(bk0, 2, 2); AROW(bk1, 2, 3); SB();
    #pragma unroll
    for (int p = 0; p < 4; ++p) {
      unsigned u = pk_bf16(lg[2 * p], lg[2 * p + 1]);
      u = pk_bf16(bflo(u) * ASC, bfhi(u) * ASC);
      lg[2 * p] = (atg >= 4 * p) ? bflo(u) : -1e9f;
      lg[2 * p + 1] = (atg >= 4 * p + 2) ? bfhi(u) : -1e9f;
    }
    am = fmaxf(fmaxf(fmaxf(lg[0], lg[1]), fmaxf(lg[2], lg[3])), fmaxf(fmaxf(lg[4], lg[5]), fmaxf(lg[6], lg[7]))) * -1.4426950408889634f;
    as_ = 0.f;
    #pragma unroll
    for (int w = 0; w < 8; ++w) { lg[w] = __builtin_amdgcn_exp2f(__builtin_fmaf(lg[w], 1.4426950408889634f, am)); as_ += lg[w]; }
    as_ = __builtin_amdgcn_rcpf(as_);
    #pragma unroll
    for (int p = 0; p < 4; ++p) ap[p] = pk_bf16(lg[2 * p] * as_, lg[2 * p + 1] * as_);
    SB();
    PV(ak0, ak1, 0); SB();
    AROW(ak0, 2, 4); AROW(ak1, 2, 5); SB();
    PV(bk0, bk1, 1); SB();
    AROW(bk0, 2, 6); AROW(bk1, 2, 7); SB();
    PV(ak0, ak1, 2); SB();
    PV(bk0, bk1, 3); SB();
    #pragma unroll
    for (int cc = 0; cc < 4; ++cc) {
      u32x2 o2; o2[0] = pk_bf16(ao[4 * cc], ao[4 * cc + 1]); o2[1] = pk_bf16(ao[4 * cc + 2], ao[4 * cc + 3]);
      ST21(ovo, o2, odst + osoff(cc));
    }
    au = pk_bf16(ao[16], ao[17]);
    ST11(ovo, au, odst + osoff1);
  }

SB();
STAMP(5); JIT(5);
{
const char* nbase = wgb + 512; const unsigned nstride = 9216u;
SB();
asm volatile("s_waitcnt vmcnt(27)\n\ts_barrier" ::: "memory");
SB(); M32ZA(Y[0], AF(0), xb[0][0]); SB();
afl[3] = TR(lbA, 6144);
T[0] = X[0][0];
T[1] = X[0][1];
T[2] = X[0][2];
T[3] = X[0][3];
T[4] = X[0][8];
T[5] = X[0][9];
T[6] = X[0][10];
T[7] = X[0][11];
T[8] = X[0][4];
T[9] = X[0][5];
T[10] = X[0][6];
T[11] = X[0][7];
T[12] = X[0][12];
T[13] = X[0][13];
T[14] = X[0][14];
T[15] = X[0][15];
T[16] = X[1][0];
T[17] = X[1][1];
T[18] = X[1][2];
T[19] = X[1][3];
T[20] = X[1][8];
T[21] = X[1][9];
T[22] = X[1][10];
T[23] = X[1][11];
T[24] = X[1][4];
T[25] = X[1][5];
T[26] = X[1][6];
T[27] = X[1][7];
T[28] = X[1][12];
T[29] = X[1][13];
T[30] = X[1][14];
T[31] = X[1][15];
SB(); M32ZA(Y[1], AF(0), xb[1][0]); SB();
afh[3] = TR(lbA, 6656);
SB(); M32A(Y[0], AF(1), xb[0][1]); SB();
afl[0] = TR(lbA, 8192);
SETM0(wv2k + 65536);
SB(); M32A(Y[1], AF(1), xb[1][1]); SB();
afh[0] = TR(lbA, 8704);
GLDS(lvoffA, lbase, 0);
SB(); M32A(Y[0], AF(2), xb[0][2]); SB();
afl[1] = TR(lbA, 10240);
SB(); M32A(Y[1], AF(2), xb[1][2]); SB();
afh[1] = TR(lbA, 10752);
SB(); M32A(Y[0], AF(3), xb[0][3]); SB();
afl[2] = TR(lbA, 12288);
GLDS(lvoffB, lbase, 1024);
gt0 = T[0] * T[0]; PIN(gt0);
SB(); M32A(Y[1], AF(3), xb[1][3]); SB();
afh[2] = TR(lbA, 12800);
lbase = nbase; lstep = 64 * nstride; lvoffA = rowA * nstride + c16P1; lvoffB = (rowA + 8) * nstride + c16P1 - 1024;
SB();
asm volatile("s_waitcnt vmcnt(25)\n\ts_barrier" ::: "memory");
SB(); M32A(Y[0], AF(0), xb[0][4]); SB();
afl[3] = TR(lbA, 14336);
gt0 = __builtin_fmaf(gt0, C1, C0); PIN(gt0);
SB(); M32A(Y[1], AF(0), xb[1][4]); SB();
afh[3] = TR(lbA, 14848);
gt0 = T[0] * gt0; PIN(gt0);
gt1 = T[1] * T[1]; PIN(gt1);
SB(); M32A(Y[0], AF(1), xb[0][5]); SB();
afl[0] = TR(lbA, 16384);
SETM0(wv2k + 0);
gt0 = __builtin_amdgcn_exp2f(gt0); PIN(gt0);
gt1 = __builtin_fmaf(gt1, C1, C0); PIN(gt1);
SB(); M32A(Y[1], AF(1), xb[1][5]); SB();
afh[0] = TR(lbA, 16896);
GLDS(lvoffA, lbase, 0);
gt0 = gt0 + 1.0f; PIN(gt0);
gt1 = T[1] * gt1; PIN(gt1);
SB(); M32A(Y[0], AF(2), xb[0][6]); SB();
afl[1] = TR(lbA, 18432);
gt0 = __builtin_amdgcn_rcpf(gt0); PIN(gt0);
gt1 = __builtin_amdgcn_exp2f(gt1); PIN(gt1);
SB(); M32A(Y[1], AF(2), xb[1][6]); SB();
afh[1] = TR(lbA, 18944);
{ float v = T[0] * gt0; PIN(v); T[0] = v; }
gt1 = gt1 + 1.0f; PIN(gt1);
gt2 = T[2] * T[2]; PIN(gt2);
SB(); M32A(Y[0], AF(3), xb[0][7]); SB();
afl[2] = TR(lbA, 20480);
GLDS(lvoffB, lbase, 1024);
gt1 = __builtin_amdgcn_rcpf(gt1); PIN(gt1);
gt2 = __builtin_fmaf(gt2, C1, C0); PIN(gt2);
SB(); M32A(Y[1], AF(3), xb[1][7]); SB();
afh[2] = TR(lbA, 20992);
lbase += lstep;
SB();
asm volatile("s_waitcnt vmcnt(25)\n\ts_barrier" ::: "memory");
SB(); M32A(Y[0], AF(0), xb[0][8]); SB();
afl[3] = TR(lbA, 22528);
{ float v = T[1] * gt1; PIN(v); T[1] = v; }
gt2 = T[2] * gt2; PIN(gt2);
gt0 = T[3] * T[3]; PIN(gt0);
SB(); M32A(Y[1], AF(0), xb[1][8]); SB();
afh[3] = TR(lbA, 23040);
gt2 = __builtin_amdgcn_exp2f(gt2); PIN(gt2);
gt0 = __builtin_fmaf(gt0, C1, C0); PIN(gt0);
SB(); M32A(Y[0], AF(1), xb[0][9]); SB();
afl[0] = TR(lbA, 24576);
SETM0(wv2k + 8192);
gt2 = gt2 + 1.0f; PIN(gt2);
gt0 = T[3] * gt0; PIN(gt0);
gt1 = T[4] * T[4]; PIN(gt1);
SB(); M32A(Y[1], AF(1), xb[1][9]); SB();
afh[0] = TR(lbA, 25088);
GLDS(lvoffA, lbase, 0);
gt2 = __builtin_amdgcn_rcpf(gt2); PIN(gt2);
SB(); M32A(Y[0], AF(2), xb[0][10]); SB();
afl[1] = TR(lbA, 26624);
{ float v = T[2] * gt2; PIN(v); T[2] = v; }
gt0 = __builtin_amdgcn_exp2f(gt0); PIN(gt0);
gt1 = __builtin_fmaf(gt1, C1, C0); PIN(gt1);
SB(); M32A(Y[1], AF(2), xb[1][10]); SB();
afh[1] = TR(lbA, 27136);
gt0 = gt0 + 1.0f; PIN(gt0);
gt1 = T[4] * gt1; PIN(gt1);
gt2 = T[5] * T[5]; PIN(gt2);
SB(); M32A(Y[0], AF(3), xb[0][11]); SB();
afl[2] = TR(lbA, 28672);
GLDS(lvoffB, lbase, 1024);
gt0 = __builtin_amdgcn_rcpf(gt0); PIN(gt0);
gt2 = __builtin_fmaf(gt2, C1, C0); PIN(gt2);
SB(); M32A(Y[1], AF(3), xb[1][11]); SB();
afh[2] = TR(lbA, 29184);
lbase += lstep;
SB();
asm volatile("s_waitcnt vmcnt(6)\n\ts_barrier" ::: "memory");
SIGNAL_SET(fHme, 3, true);
SIGNAL_ADD(fO, 1, true);
SB(); M32A(Y[0], AF(0), xb[0][12]); SB();
afl[3] = TR(lbA, 30720);
{ float v = T[3] * gt0; PIN(v); T[3] = v; }
gt1 = __builtin_amdgcn_exp2f(gt1); PIN(gt1);
gt2 = T[5] * gt2; PIN(gt2);
SB(); M32A(Y[1], AF(0), xb[1][12]); SB();
afh[3] = TR(lbA, 31232);
gt1 = gt1 + 1.0f; PIN(gt1);
gt2 = __builtin_amdgcn_exp2f(gt2); PIN(gt2);
SB(); M32A(Y[0], AF(1), xb[0][13]); SB();
afl[0] = TR(lbA, 32768);
SETM0(wv2k + 16384);
gt1 = __builtin_amdgcn_rcpf(gt1); PIN(gt1);
gt2 = gt2 + 1.0f; PIN(gt2);
SB(); M32A(Y[1], AF(1), xb[1][13]); SB();
afh[0] = TR(lbA, 33280);
GLDS(lvoffA, lbase, 0);
{ float v = T[4] * gt1; PIN(v); T[4] = v; }
gt0 = T[6] * T[6]; PIN(gt0);
SB(); M32A(Y[0], AF(2), xb[0][14]); SB();
afl[1] = TR(lbA, 34816);
gt2 = __builtin_amdgcn_rcpf(gt2); PIN(gt2);
gt0 = __builtin_fmaf(gt0, C1, C0); PIN(gt0);
gt1 = T[7] * T[7]; PIN(gt1);
SB(); M32A(Y[1], AF(2), xb[1][14]); SB();
afh[1] = TR(lbA, 35328);
{ float v = T[5] * gt2; PIN(v); T[5] = v; }
gt0 = T[6] * gt0; PIN(gt0);
gt1 = __builtin_fmaf(gt1, C1, C0); PIN(gt1);
SB(); M32A(Y[0], AF(3), xb[0][15]); SB();
afl[2] = TR(lbA, 36864);
GLDS(lvoffB, lbase, 1024);
gt0 = __builtin_amdgcn_exp2f(gt0); PIN(gt0);
gt1 = T[7] * gt1; PIN(gt1);
SB(); M32A(Y[1], AF(3), xb[1][15]); SB();
afh[2] = TR(lbA, 37376);
lbase += lstep;
SB();
asm volatile("s_waitcnt vmcnt(23)\n\ts_barrier" ::: "memory");
SB(); M32A(Y[0], AF(0), xb[0][16]); SB();
afl[3] = TR(lbA, 38912);
gt0 = gt0 + 1.0f; PIN(gt0);
gt1 = __builtin_amdgcn_exp2f(gt1); PIN(gt1);
gt2 = T[8] * T[8]; PIN(gt2);
SB(); M32A(Y[1], AF(0), xb[1][16]); SB();
afh[3] = TR(lbA, 39424);
gt0 = __builtin_amdgcn_rcpf(gt0); PIN(gt0);
gt1 = gt1 + 1.0f; PIN(gt1);
SB(); M32A(Y[0], AF(1), xb[0][17]); SB();
afl[0] = TR(lbA, 40960);
SETM0(wv2k + 24576);
{ float v = T[6] * gt0; PIN(v); T[6] = v; }
gt1 = __builtin_amdgcn_rcpf(gt1); PIN(gt1);
SB(); M32A(Y[1], AF(1), xb[1][17]); SB();
afh[0] = TR(lbA, 41472);
GLDS(lvoffA, lbase, 0);
{ float v = T[7] * gt1; PIN(v); T[7] = v; }
gt2 = __builtin_fmaf(gt2, C1, C0); PIN(gt2);
SB(); M32A(Y[0], AF(2), xb[0][18]); SB();
afl[1] = TR(lbA, 43008);
gt2 = T[8] * gt2; PIN(gt2);
gt0 = T[9] * T[9]; PIN(gt0);
gt1 = T[10] * T[10]; PIN(gt1);
SB(); M32A(Y[1], AF(2), xb[1][18]); SB();
afh[1] = TR(lbA, 43520);
gt2 = __builtin_amdgcn_exp2f(gt2); PIN(gt2);
gt0 = __builtin_fmaf(gt0, C1, C0); PIN(gt0);
SB(); M32A(Y[0], AF(3), xb[0][19]); SB();
afl[2] = TR(lbA, 45056);
GLDS(lvoffB, lbase, 1024);
gt2 = gt2 + 1.0f; PIN(gt2);
gt0 = T[9] * gt0; PIN(gt0);
gt1 = __builtin_fmaf(gt1, C1, C0); PIN(gt1);
SB(); M32A(Y[1], AF(3), xb[1][19]); SB();
afh[2] = TR(lbA, 45568);
lbase += lstep;
SB();
asm volatile("s_waitcnt vmcnt(22)\n\ts_barrier" ::: "memory");
SB(); M32A(Y[0], AF(0), xb[0][20]); SB();
afl[3] = TR(lbA, 47104);
gt2 = __builtin_amdgcn_rcpf(gt2); PIN(gt2);
gt0 = __builtin_amdgcn_exp2f(gt0); PIN(gt0);
SB(); M32A(Y[1], AF(0), xb[1][20]); SB();
afh[3] = TR(lbA, 47616);
{ float v = T[8] * gt2; PIN(v); T[8] = v; }
gt0 = gt0 + 1.0f; PIN(gt0);
gt1 = T[10] * gt1; PIN(gt1);
SB(); M32A(Y[0], AF(1), xb[0][21]); SB();
afl[0] = TR(lbA, 49152);
SETM0(wv2k + 32768);
gt0 = __builtin_amdgcn_rcpf(gt0); PIN(gt0);
gt2 = T[11] * T[11]; PIN(gt2);
SB(); M32A(Y[1], AF(1), xb[1][21]); SB();
afh[0] = TR(lbA, 49664);
GLDS(lvoffA, lbase, 0);
{ float v = T[9] * gt0; PIN(v); T[9] = v; }
gt2 = __builtin_fmaf(gt2, C1, C0); PIN(gt2);
SB(); M32A(Y[0], AF(2), xb[0][22]); SB();
afl[1] = TR(lbA, 51200);
gt1 = __builtin_amdgcn_exp2f(gt1); PIN(gt1);
gt2 = T[11] * gt2; PIN(gt2);
gt0 = T[12] * T[12]; PIN(gt0);
SB(); M32A(Y[1], AF(2), xb[1][22]); SB();
afh[1] = TR(lbA, 51712);
gt1 = gt1 + 1.0f; PIN(gt1);
gt2 = __builtin_amdgcn_exp2f(gt2); PIN(gt2);
SB(); M32A(Y[0], AF(3), xb[0][23]); SB();
afl[2] = TR(lbA, 53248);
GLDS(lvoffB, lbase, 1024);
gt1 = __builtin_amdgcn_rcpf(gt1); PIN(gt1);
gt2 = gt2 + 1.0f; PIN(gt2);
SB(); M32A(Y[1], AF(3), xb[1][23]); SB();
afh[2] = TR(lbA, 53760);
lbase += lstep;
SB();
asm volatile("s_waitcnt vmcnt(21)\n\ts_barrier" ::: "memory");
SB(); M32A(Y[0], AF(0), xb[0][24]); SB();
afl[3] = TR(lbA, 55296);
{ float v = T[10] * gt1; PIN(v); T[10] = v; }
gt2 = __builtin_amdgcn_rcpf(gt2); PIN(gt2);
gt0 = __builtin_fmaf(gt0, C1, C0); PIN(gt0);
SB(); M32A(Y[1], AF(0), xb[1][24]); SB();
afh[3] = TR(lbA, 55808);
{ float v = T[11] * gt2; PIN(v); T[11] = v; }
gt0 = T[12] * gt0; PIN(gt0);
gt1 = T[13] * T[13]; PIN(gt1);
SB(); M32A(Y[0], AF(1), xb[0][25]); SB();
afl[0] = TR(lbA, 57344);
SETM0(wv2k + 40960);
gt0 = __builtin_amdgcn_exp2f(gt0); PIN(gt0);
gt1 = __builtin_fmaf(gt1, C1, C0); PIN(gt1);
SB(); M32A(Y[1], AF(1), xb[1][25]); SB();
afh[0] = TR(lbA, 57856);
GLDS(lvoffA, lbase, 0);
gt0 = gt0 + 1.0f; PIN(gt0);
gt1 = T[13] * gt1; PIN(gt1);
SB(); M32A(Y[0], AF(2), xb[0][26]); SB();
afl[1] = TR(lbA, 59392);
gt0 = __builtin_amdgcn_rcpf(gt0); PIN(gt0);
gt1 = __builtin_amdgcn_exp2f(gt1); PIN(gt1);
SB(); M32A(Y[1], AF(2), xb[1][26]); SB();
afh[1] = TR(lbA, 59904);
{ float v = T[12] * gt0; PIN(v); T[12] = v; }
gt1 = gt1 + 1.0f; PIN(gt1);
gt2 = T[14] * T[14]; PIN(gt2);
SB(); M32A(Y[0], AF(3), xb[0][27]); SB();
afl[2] = TR(lbA, 61440);
GLDS(lvoffB, lbase, 1024);
gt1 = __builtin_amdgcn_rcpf(gt1); PIN(gt1);
gt2 = __builtin_fmaf(gt2, C1, C0); PIN(gt2);
SB(); M32A(Y[1], AF(3), xb[1][27]); SB();
afh[2] = TR(lbA, 61952);
lbase += lstep;
SB();
asm volatile("s_waitcnt vmcnt(12)\n\ts_barrier" ::: "memory");
SB(); M32A(Y[0], AF(0), xb[0][28]); SB();
afl[3] = TR(lbA, 63488);
{ float v = T[13] * gt1; PIN(v); T[13] = v; }
gt2 = T[14] * gt2; PIN(gt2);
gt0 = T[15] * T[15]; PIN(gt0);
SB(); M32A(Y[1], AF(0), xb[1][28]); SB();
afh[3] = TR(lbA, 64000);
gt2 = __builtin_amdgcn_exp2f(gt2); PIN(gt2);
gt0 = __builtin_fmaf(gt0, C1, C0); PIN(gt0);
SB(); M32A(Y[0], AF(1), xb[0][29]); SB();
afl[0] = TR(lbA, 65536);
SETM0(wv2k + 49152);
gt2 = gt2 + 1.0f; PIN(gt2);
gt0 = T[15] * gt0; PIN(gt0);
gt1 = T[16] * T[16]; PIN(gt1);
SB(); M32A(Y[1], AF(1), xb[1][29]); SB();
afh[0] = TR(lbA, 66048);
GLDS(lvoffA, lbase, 0);
gt2 = __builtin_amdgcn_rcpf(gt2); PIN(gt2);
SB(); M32V(Y[0], AF(2), xb[0][30]); SB();
afl[1] = TR(lbA, 67584);
{ float v = T[14] * gt2; PIN(v); T[14] = v; }
gt0 = __builtin_amdgcn_exp2f(gt0); PIN(gt0);
gt1 = __builtin_fmaf(gt1, C1, C0); PIN(gt1);
SB(); M32V(Y[1], AF(2), xb[1][30]); SB();
afh[1] = TR(lbA, 68096);
gt0 = gt0 + 1.0f; PIN(gt0);
gt1 = T[16] * gt1; PIN(gt1);
gt2 = T[17] * T[17]; PIN(gt2);
SB(); M32V(Y[0], AF(3), xb[0][31]); SB();
afl[2] = TR(lbA, 69632);
GLDS(lvoffB, lbase, 1024);
gt0 = __builtin_amdgcn_rcpf(gt0); PIN(gt0);
gt2 = __builtin_fmaf(gt2, C1, C0); PIN(gt2);
SB(); M32V(Y[1], AF(3), xb[1][31]); SB();
afh[2] = TR(lbA, 70144);
lbase += lstep;
SB();
asm volatile("s_waitcnt vmcnt(12)\n\ts_barrier" ::: "memory");
SB(); M32V(Y[0], AF(0), xb[0][32]); SB();
afl[3] = TR(lbA, 71680);
{ float v = T[15] * gt0; PIN(v); T[15] = v; }
gt1 = __builtin_amdgcn_exp2f(gt1); PIN(gt1);
gt2 = T[17] * gt2; PIN(gt2);
SB(); M32V(Y[1], AF(0), xb[1][32]); SB();
afh[3] = TR(lbA, 72192);
gt1 = gt1 + 1.0f; PIN(gt1);
gt2 = __builtin_amdgcn_exp2f(gt2); PIN(gt2);
SB(); M32V(Y[0], AF(1), xb[0][33]); SB();
afl[0] = TR(lbA, 0);
SETM0(wv2k + 57344);
gt1 = __builtin_amdgcn_rcpf(gt1); PIN(gt1);
gt2 = gt2 + 1.0f; PIN(gt2);
SB(); M32V(Y[1], AF(1), xb[1][33]); SB();
afh[0] = TR(lbA, 512);
GLDS(lvoffA, lbase, 0);
{ float v = T[16] * gt1; PIN(v); T[16] = v; }
gt0 = T[18] * T[18]; PIN(gt0);
SB(); M32V(Y[0], AF(2), xb[0][34]); SB();
afl[1] = TR(lbA, 2048);
gt2 = __builtin_amdgcn_rcpf(gt2); PIN(gt2);
gt0 = __builtin_fmaf(gt0, C1, C0); PIN(gt0);
gt1 = T[19] * T[19]; PIN(gt1);
SB(); M32V(Y[1], AF(2), xb[1][34]); SB();
afh[1] = TR(lbA, 2560);
{ float v = T[17] * gt2; PIN(v); T[17] = v; }
gt0 = T[18] * gt0; PIN(gt0);
gt1 = __builtin_fmaf(gt1, C1, C0); PIN(gt1);
SB(); M32V(Y[0], AF(3), xb[0][35]); SB();
afl[2] = TR(lbA, 4096);
GLDS(lvoffB, lbase, 1024);
gt0 = __builtin_amdgcn_exp2f(gt0); PIN(gt0);
gt1 = T[19] * gt1; PIN(gt1);
SB(); M32V(Y[1], AF(3), xb[1][35]); SB();
afh[2] = TR(lbA, 4608);
lbase += lstep;
}
{
const char* nbase = wub + 512; const unsigned nstride = 9216u;
SB();
asm volatile("s_waitcnt vmcnt(12)\n\ts_barrier" ::: "memory");
SB(); M32ZA(X[0], AF(0), xb[0][0]); SB();
afl[3] = TR(lbA, 6144);
gt0 = gt0 + 1.0f; PIN(gt0);
gt1 = __builtin_amdgcn_exp2f(gt1); PIN(gt1);
gt2 = T[20] * T[20]; PIN(gt2);
SB(); M32ZA(X[1], AF(0), xb[1][0]); SB();
afh[3] = TR(lbA, 6656);
gt0 = __builtin_amdgcn_rcpf(gt0); PIN(gt0);
gt1 = gt1 + 1.0f; PIN(gt1);
SB(); M32A(X[0], AF(1), xb[0][1]); SB();
afl[0] = TR(lbA, 8192);
SETM0(wv2k + 65536);
{ float v = T[18] * gt0; PIN(v); T[18] = v; }
gt1 = __builtin_amdgcn_rcpf(gt1); PIN(gt1);
SB(); M32A(X[1], AF(1), xb[1][1]); SB();
afh[0] = TR(lbA, 8704);
GLDS(lvoffA, lbase, 0);
{ float v = T[19] * gt1; PIN(v); T[19] = v; }
gt2 = __builtin_fmaf(gt2, C1, C0); PIN(gt2);
SB(); M32A(X[0], AF(2), xb[0][2]); SB();
afl[1] = TR(lbA, 10240);
gt2 = T[20] * gt2; PIN(gt2);
gt0 = T[21] * T[21]; PIN(gt0);
gt1 = T[22] * T[22]; PIN(gt1);
SB(); M32A(X[1], AF(2), xb[1][2]); SB();
afh[1] = TR(lbA, 10752);
gt2 = __builtin_amdgcn_exp2f(gt2); PIN(gt2);
gt0 = __builtin_fmaf(gt0, C1, C0); PIN(gt0);
SB(); M32A(X[0], AF(3), xb[0][3]); SB();
afl[2] = TR(lbA, 12288);
GLDS(lvoffB, lbase, 1024);
gt2 = gt2 + 1.0f; PIN(gt2);
gt0 = T[21] * gt0; PIN(gt0);
gt1 = __builtin_fmaf(gt1, C1, C0); PIN(gt1);
SB(); M32A(X[1], AF(3), xb[1][3]); SB();
afh[2] = TR(lbA, 12800);
lbase = nbase; lstep = 64 * nstride; lvoffA = rowA * nstride + c16P1; lvoffB = (rowA + 8) * nstride + c16P1 - 1024;
SB();
asm volatile("s_waitcnt vmcnt(12)\n\ts_barrier" ::: "memory");
SB(); M32A(X[0], AF(0), xb[0][4]); SB();
afl[3] = TR(lbA, 14336);
gt2 = __builtin_amdgcn_rcpf(gt2); PIN(gt2);
gt0 = __builtin_amdgcn_exp2f(gt0); PIN(gt0);
SB(); M32A(X[1], AF(0), xb[1][4]); SB();
afh[3] = TR(lbA, 14848);
{ float v = T[20] * gt2; PIN(v); T[20] = v; }
gt0 = gt0 + 1.0f; PIN(gt0);
gt1 = T[22] * gt1; PIN(gt1);
SB(); M32A(X[0], AF(1), xb[0][5]); SB();
afl[0] = TR(lbA, 16384);
SETM0(wv2k + 0);
gt0 = __builtin_amdgcn_rcpf(gt0); PIN(gt0);
gt2 = T[23] * T[23]; PIN(gt2);
SB(); M32A(X[1], AF(1), xb[1][5]); SB();
afh[0] = TR(lbA, 16896);
GLDS(lvoffA, lbase, 0);
{ float v = T[21] * gt0; PIN(v); T[21] = v; }
gt2 = __builtin_fmaf(gt2, C1, C0); PIN(gt2);
SB(); M32A(X[0], AF(2), xb[0][6]); SB();
afl[1] = TR(lbA, 18432);
gt1 = __builtin_amdgcn_exp2f(gt1); PIN(gt1);
gt2 = T[23] * gt2; PIN(gt2);
gt0 = T[24] * T[24]; PIN(gt0);
SB(); M32A(X[1], AF(2), xb[1][6]); SB();
afh[1] = TR(lbA, 18944);
gt1 = gt1 + 1.0f; PIN(gt1);
gt2 = __builtin_amdgcn_exp2f(gt2); PIN(gt2);
SB(); M32A(X[0], AF(3), xb[0][7]); SB();
afl[2] = TR(lbA, 20480);
GLDS(lvoffB, lbase, 1024);
gt1 = __builtin_amdgcn_rcpf(gt1); PIN(gt1);
gt2 = gt2 + 1.0f; PIN(gt2);
SB(); M32A(X[1], AF(3), xb[1][7]); SB();
afh[2] = TR(lbA, 20992);
lbase += lstep;
SB();
asm volatile("s_waitcnt vmcnt(12)\n\ts_barrier" ::: "memory");
SB(); M32A(X[0], AF(0), xb[0][8]); SB();
afl[3] = TR(lbA, 22528);
{ float v = T[22] * gt1; PIN(v); T[22] = v; }
gt2 = __builtin_amdgcn_rcpf(gt2); PIN(gt2);
gt0 = __builtin_fmaf(gt0, C1, C0); PIN(gt0);
SB(); M32A(X[1], AF(0), xb[1][8]); SB();
afh[3] = TR(lbA, 23040);
{ float v = T[23] * gt2; PIN(v); T[23] = v; }
gt0 = T[24] * gt0; PIN(gt0);
gt1 = T[25] * T[25]; PIN(gt1);
SB(); M32A(X[0], AF(1), xb[0][9]); SB();
afl[0] = TR(lbA, 24576);
SETM0(wv2k + 8192);
gt0 = __builtin_amdgcn_exp2f(gt0); PIN(gt0);
gt1 = __builtin_fmaf(gt1, C1, C0); PIN(gt1);
SB(); M32A(X[1], AF(1), xb[1][9]); SB();
afh[0] = TR(lbA, 25088);
GLDS(lvoffA, lbase, 0);
gt0 = gt0 + 1.0f; PIN(gt0);
gt1 = T[25] * gt1; PIN(gt1);
SB(); M32A(X[0], AF(2), xb[0][10]); SB();
afl[1] = TR(lbA, 26624);
gt0 = __builtin_amdgcn_rcpf(gt0); PIN(gt0);
gt1 = __builtin_amdgcn_exp2f(gt1); PIN(gt1);
SB(); M32A(X[1], AF(2), xb[1][10]); SB();
afh[1] = TR(lbA, 27136);
{ float v = T[24] * gt0; PIN(v); T[24] = v; }
gt1 = gt1 + 1.0f; PIN(gt1);
gt2 = T[26] * T[26]; PIN(gt2);
SB(); M32A(X[0], AF(3), xb[0][11]); SB();
afl[2] = TR(lbA, 28672);
GLDS(lvoffB, lbase, 1024);
gt1 = __builtin_amdgcn_rcpf(gt1); PIN(gt1);
gt2 = __builtin_fmaf(gt2, C1, C0); PIN(gt2);
SB(); M32A(X[1], AF(3), xb[1][11]); SB();
afh[2] = TR(lbA, 29184);
lbase += lstep;
SB();
asm volatile("s_waitcnt vmcnt(12)\n\ts_barrier" ::: "memory");
SB(); M32A(X[0], AF(0), xb[0][12]); SB();
afl[3] = TR(lbA, 30720);
{ float v = T[25] * gt1; PIN(v); T[25] = v; }
gt2 = T[26] * gt2; PIN(gt2);
gt0 = T[27] * T[27]; PIN(gt0);
{ float v = T[0] * Y[0][0]; PIN(v); T[0] = v; }
SB(); M32A(X[1], AF(0), xb[1][12]); SB();
afh[3] = TR(lbA, 31232);
gt2 = __builtin_amdgcn_exp2f(gt2); PIN(gt2);
gt0 = __builtin_fmaf(gt0, C1, C0); PIN(gt0);
SB(); M32A(X[0], AF(1), xb[0][13]); SB();
afl[0] = TR(lbA, 32768);
SETM0(wv2k + 16384);
gt2 = gt2 + 1.0f; PIN(gt2);
gt0 = T[27] * gt0; PIN(gt0);
gt1 = T[28] * T[28]; PIN(gt1);
SB(); M32A(X[1], AF(1), xb[1][13]); SB();
afh[0] = TR(lbA, 33280);
GLDS(lvoffA, lbase, 0);
gt2 = __builtin_amdgcn_rcpf(gt2); PIN(gt2);
SB(); M32A(X[0], AF(2), xb[0][14]); SB();
afl[1] = TR(lbA, 34816);
{ float v = T[26] * gt2; PIN(v); T[26] = v; }
gt0 = __builtin_amdgcn_exp2f(gt0); PIN(gt0);
gt1 = __builtin_fmaf(gt1, C1, C0); PIN(gt1);
SB(); M32A(X[1], AF(2), xb[1][14]); SB();
afh[1] = TR(lbA, 35328);
gt0 = gt0 + 1.0f; PIN(gt0);
gt1 = T[28] * gt1; PIN(gt1);
gt2 = T[29] * T[29]; PIN(gt2);
SB(); M32A(X[0], AF(3), xb[0][15]); SB();
afl[2] = TR(lbA, 36864);
GLDS(lvoffB, lbase, 1024);
gt0 = __builtin_amdgcn_rcpf(gt0); PIN(gt0);
gt2 = __builtin_fmaf(gt2, C1, C0); PIN(gt2);
SB(); M32A(X[1], AF(3), xb[1][15]); SB();
afh[2] = TR(lbA, 37376);
lbase += lstep;
SB();
asm volatile("s_waitcnt vmcnt(12)\n\ts_barrier" ::: "memory");
SB(); M32A(X[0], AF(0), xb[0][16]); SB();
afl[3] = TR(lbA, 38912);
{ float v = T[27] * gt0; PIN(v); T[27] = v; }
gt1 = __builtin_amdgcn_exp2f(gt1); PIN(gt1);
gt2 = T[29] * gt2; PIN(gt2);
SB(); M32A(X[1], AF(0), xb[1][16]); SB();
afh[3] = TR(lbA, 39424);
gt1 = gt1 + 1.0f; PIN(gt1);
gt2 = __builtin_amdgcn_exp2f(gt2); PIN(gt2);
SB(); M32A(X[0], AF(1), xb[0][17]); SB();
afl[0] = TR(lbA, 40960);
SETM0(wv2k + 24576);
gt1 = __builtin_amdgcn_rcpf(gt1); PIN(gt1);
gt2 = gt2 + 1.0f; PIN(gt2);
SB(); M32A(X[1], AF(1), xb[1][17]); SB();
afh[0] = TR(lbA, 41472);
GLDS(lvoffA, lbase, 0);
{ float v = T[28] * gt1; PIN(v); T[28] = v; }
gt0 = T[30] * T[30]; PIN(gt0);
SB(); M32A(X[0], AF(2), xb[0][18]); SB();
afl[1] = TR(lbA, 43008);
gt2 = __builtin_amdgcn_rcpf(gt2); PIN(gt2);
gt0 = __builtin_fmaf(gt0, C1, C0); PIN(gt0);
gt1 = T[31] * T[31]; PIN(gt1);
SB(); M32A(X[1], AF(2), xb[1][18]); SB();
afh[1] = TR(lbA, 43520);
{ float v = T[29] * gt2; PIN(v); T[29] = v; }
gt0 = T[30] * gt0; PIN(gt0);
gt1 = __builtin_fmaf(gt1, C1, C0); PIN(gt1);
SB(); M32A(X[0], AF(3), xb[0][19]); SB();
afl[2] = TR(lbA, 45056);
GLDS(lvoffB, lbase, 1024);
gt0 = __builtin_amdgcn_exp2f(gt0); PIN(gt0);
gt1 = T[31] * gt1; PIN(gt1);
SB(); M32A(X[1], AF(3), xb[1][19]); SB();
afh[2] = TR(lbA, 45568);
lbase += lstep;
SB();
asm volatile("s_waitcnt vmcnt(12)\n\ts_barrier" ::: "memory");
SB(); M32A(X[0], AF(0), xb[0][20]); SB();
afl[3] = TR(lbA, 47104);
gt0 = gt0 + 1.0f; PIN(gt0);
gt1 = __builtin_amdgcn_exp2f(gt1); PIN(gt1);
{ float v = T[1] * Y[0][1]; PIN(v); T[1] = v; }
SB(); M32A(X[1], AF(0), xb[1][20]); SB();
afh[3] = TR(lbA, 47616);
gt0 = __builtin_amdgcn_rcpf(gt0); PIN(gt0);
gt1 = gt1 + 1.0f; PIN(gt1);
SB(); M32A(X[0], AF(1), xb[0][21]); SB();
afl[0] = TR(lbA, 49152);
SETM0(wv2k + 32768);
LDW1(pw0, zvo, fOw);
{ float v = T[30] * gt0; PIN(v); T[30] = v; }
SB(); M32A(X[1], AF(1), xb[1][21]); SB();
afh[0] = TR(lbA, 49664);
GLDS(lvoffA, lbase, 0);
gt1 = __builtin_amdgcn_rcpf(gt1); PIN(gt1);
SB(); M32A(X[0], AF(2), xb[0][22]); SB();
afl[1] = TR(lbA, 51200);
{ float v = T[31] * gt1; PIN(v); T[31] = v; }
{ unsigned u = pk_bf16(T[0], T[1]); PIN(u); hh[0] = u; }
{ float v = T[2] * Y[0][2]; PIN(v); T[2] = v; }
{ float v = T[3] * Y[0][3]; PIN(v); T[3] = v; }
SB(); M32A(X[1], AF(2), xb[1][22]); SB();
afh[1] = TR(lbA, 51712);
{ unsigned u = pk_bf16(T[2], T[3]); PIN(u); hh[1] = u; }
{ float v = T[4] * Y[0][8]; PIN(v); T[4] = v; }
{ float v = T[5] * Y[0][9]; PIN(v); T[5] = v; }
SB(); M32A(X[0], AF(3), xb[0][23]); SB();
afl[2] = TR(lbA, 53248);
GLDS(lvoffB, lbase, 1024);
{ unsigned u = pk_bf16(T[4], T[5]); PIN(u); hh[2] = u; }
{ float v = T[6] * Y[0][10]; PIN(v); T[6] = v; }
{ float v = T[7] * Y[0][11]; PIN(v); T[7] = v; }
SB(); M32A(X[1], AF(3), xb[1][23]); SB();
afh[2] = TR(lbA, 53760);
lbase += lstep;
SB();
asm volatile("s_waitcnt vmcnt(13)\n\ts_barrier" ::: "memory");
SB(); M32A(X[0], AF(0), xb[0][24]); SB();
afl[3] = TR(lbA, 55296);
{ unsigned u = pk_bf16(T[6], T[7]); PIN(u); hh[3] = u; }
{ float v = T[8] * Y[0][4]; PIN(v); T[8] = v; }
{ float v = T[9] * Y[0][5]; PIN(v); T[9] = v; }
{ float v = T[10] * Y[0][6]; PIN(v); T[10] = v; }
SB(); M32A(X[1], AF(0), xb[1][24]); SB();
afh[3] = TR(lbA, 55808);
*(AS3 u32x4*)(uintptr_t)(hw_ad + 49152) = hh;
{ float v = T[11] * Y[0][7]; PIN(v); T[11] = v; }
SB(); M32A(X[0], AF(1), xb[0][25]); SB();
afl[0] = TR(lbA, 57344);
SETM0(wv2k + 40960);
ST41(hxvo, hh, hxw + 49152);
SB(); M32A(X[1], AF(1), xb[1][25]); SB();
afh[0] = TR(lbA, 57856);
GLDS(lvoffA, lbase, 0);
{ unsigned u = pk_bf16(T[8], T[9]); PIN(u); hh[0] = u; }
{ unsigned u = pk_bf16(T[10], T[11]); PIN(u); hh[1] = u; }
SB(); M32A(X[0], AF(2), xb[0][26]); SB();
afl[1] = TR(lbA, 59392);
{ float v = T[12] * Y[0][12]; PIN(v); T[12] = v; }
{ float v = T[13] * Y[0][13]; PIN(v); T[13] = v; }
{ float v = T[14] * Y[0][14]; PIN(v); T[14] = v; }
{ float v = T[15] * Y[0][15]; PIN(v); T[15] = v; }
SB(); M32A(X[1], AF(2), xb[1][26]); SB();
afh[1] = TR(lbA, 59904);
{ unsigned u = pk_bf16(T[12], T[13]); PIN(u); hh[2] = u; }
{ unsigned u = pk_bf16(T[14], T[15]); PIN(u); hh[3] = u; }
{ float v = T[16] * Y[1][0]; PIN(v); T[16] = v; }
SB(); M32A(X[0], AF(3), xb[0][27]); SB();
afl[2] = TR(lbA, 61440);
GLDS(lvoffB, lbase, 1024);
*(AS3 u32x4*)(uintptr_t)(hw_ad + 49664) = hh;
{ float v = T[17] * Y[1][1]; PIN(v); T[17] = v; }
SB(); M32A(X[1], AF(3), xb[1][27]); SB();
afh[2] = TR(lbA, 61952);
lbase += lstep;
SB();
asm volatile("s_waitcnt vmcnt(14)\n\ts_barrier" ::: "memory");
SB(); M32A(X[0], AF(0), xb[0][28]); SB();
afl[3] = TR(lbA, 63488);
ST41(hxvo, hh, hxw + 49664);
{ float v = T[18] * Y[1][2]; PIN(v); T[18] = v; }
SB(); M32A(X[1], AF(0), xb[1][28]); SB();
afh[3] = TR(lbA, 64000);
{ unsigned u = pk_bf16(T[16], T[17]); PIN(u); hh[0] = u; }
{ float v = T[19] * Y[1][3]; PIN(v); T[19] = v; }
{ float v = T[20] * Y[1][8]; PIN(v); T[20] = v; }
SB(); M32A(X[0], AF(1), xb[0][29]); SB();
afl[0] = TR(lbA, 65536);
SETM0(wv2k + 49152);
{ unsigned u = pk_bf16(T[18], T[19]); PIN(u); hh[1] = u; }
{ float v = T[21] * Y[1][9]; PIN(v); T[21] = v; }
{ float v = T[22] * Y[1][10]; PIN(v); T[22] = v; }
SB(); M32A(X[1], AF(1), xb[1][29]); SB();
afh[0] = TR(lbA, 66048);
GLDS(lvoffA, lbase, 0);
{ unsigned u = pk_bf16(T[20], T[21]); PIN(u); hh[2] = u; }
{ float v = T[23] * Y[1][11]; PIN(v); T[23] = v; }
SB(); M32V(X[0], AF(2), xb[0][30]); SB();
afl[1] = TR(lbA, 67584);
{ unsigned u = pk_bf16(T[22], T[23]); PIN(u); hh[3] = u; }
{ float v = T[24] * Y[1][4]; PIN(v); T[24] = v; }
{ float v = T[25] * Y[1][5]; PIN(v); T[25] = v; }
{ float v = T[26] * Y[1][6]; PIN(v); T[26] = v; }
SB(); M32V(X[1], AF(2), xb[1][30]); SB();
afh[1] = TR(lbA, 68096);
*(AS3 u32x4*)(uintptr_t)(hw_ad + 51200) = hh;
{ float v = T[27] * Y[1][7]; PIN(v); T[27] = v; }
SB(); M32V(X[0], AF(3), xb[0][31]); SB();
afl[2] = TR(lbA, 69632);
GLDS(lvoffB, lbase, 1024);
ST41(hxvo, hh, hxw + 51200);
SB(); M32V(X[1], AF(3), xb[1][31]); SB();
afh[2] = TR(lbA, 70144);
lbase += lstep;
SB();
asm volatile("s_waitcnt vmcnt(9)\n\ts_barrier" : "+v"(pw0) :: "memory");
SB(); M32V(X[0], AF(0), xb[0][32]); SB();
afl[3] = TR(lbA, 71680);
{ unsigned u = pk_bf16(T[24], T[25]); PIN(u); hh[0] = u; }
{ unsigned u = pk_bf16(T[26], T[27]); PIN(u); hh[1] = u; }
{ float v = T[28] * Y[1][12]; PIN(v); T[28] = v; }
{ float v = T[29] * Y[1][13]; PIN(v); T[29] = v; }
SB(); M32V(X[1], AF(0), xb[1][32]); SB();
afh[3] = TR(lbA, 72192);
{ unsigned u = pk_bf16(T[28], T[29]); PIN(u); hh[2] = u; }
{ float v = T[30] * Y[1][14]; PIN(v); T[30] = v; }
{ float v = T[31] * Y[1][15]; PIN(v); T[31] = v; }
SB(); M32V(X[0], AF(1), xb[0][33]); SB();
afl[0] = TR(lbA, 0);
SETM0(wv2k + 57344);
{ unsigned u = pk_bf16(T[30], T[31]); PIN(u); hh[3] = u; }
SB(); M32V(X[1], AF(1), xb[1][33]); SB();
afh[0] = TR(lbA, 512);
GLDS(lvoffA, lbase, 0);
*(AS3 u32x4*)(uintptr_t)(hw_ad + 51712) = hh;
SB(); M32V(X[0], AF(2), xb[0][34]); SB();
afl[1] = TR(lbA, 2048);
ST41(hxvo, hh, hxw + 51712);
SB(); M32V(X[1], AF(2), xb[1][34]); SB();
afh[1] = TR(lbA, 2560);
SB(); M32V(X[0], AF(3), xb[0][35]); SB();
afl[2] = TR(lbA, 4096);
GLDS(lvoffB, lbase, 1024);
SB(); M32V(X[1], AF(3), xb[1][35]); SB();
afh[2] = TR(lbA, 4608);
lbase += lstep;
}
STAMP(6); JIT(6);
  const unsigned ln2 = lane_id();
  const unsigned c16P2 = ((ln2 & 7) ^ (((ln2 >> 4) & 3) << 1)) * 16;
  const unsigned swl = ((ln2 >> 3) & 1) | (((ln2 >> 4) & 1) << 1);
  const unsigned lbB0 = (4 * (ln2 >> 4) + ((ln2 & 15) >> 2)) * 128 + (((unsigned)(2 * cg) ^ swl) * 32) + 8 * (ln2 & 3), lbB1 = lbB0 ^ 32;
  const unsigned pvo = ln2 * 16;
  const unsigned hr_ad = H_OFF + (th * 4) * 1024 + pvo;
  const unsigned svo = ((ln2 >> 4) & 1) * (18 * NSL * 1024) + ((ln2 >> 5) * 16 + (ln2 & 15)) * 16;
  const unsigned lbW0 = lbA ^ (unsigned)(64 * cg), lbW1 = lbW0 ^ 64u;

u32x4 ob[36];
{
const char* nbase = wob; const unsigned nstride = 1152u;
SB();
asm volatile("s_waitcnt vmcnt(17)\n\ts_barrier" ::: "memory");
CHECK_GE(pw0, fOw, 16);
SB(); M32ZA(Y[0], AF(0), xb[0][0]); SB();
afl[3] = TR(lbA, 6144);
T[0] = X[0][0];
T[1] = X[0][1];
T[2] = X[0][2];
T[3] = X[0][3];
T[4] = X[0][8];
T[5] = X[0][9];
T[6] = X[0][10];
T[7] = X[0][11];
T[8] = X[0][4];
T[9] = X[0][5];
T[10] = X[0][6];
T[11] = X[0][7];
T[12] = X[0][12];
T[13] = X[0][13];
T[14] = X[0][14];
T[15] = X[0][15];
T[16] = X[1][0];
T[17] = X[1][1];
T[18] = X[1][2];
T[19] = X[1][3];
T[20] = X[1][8];
T[21] = X[1][9];
T[22] = X[1][10];
T[23] = X[1][11];
T[24] = X[1][4];
T[25] = X[1][5];
T[26] = X[1][6];
T[27] = X[1][7];
T[28] = X[1][12];
T[29] = X[1][13];
T[30] = X[1][14];
T[31] = X[1][15];
SB(); M32ZA(Y[1], AF(0), xb[1][0]); SB();
afh[3] = TR(lbA, 6656);
SB(); M32A(Y[0], AF(1), xb[0][1]); SB();
afl[0] = TR(lbA, 8192);
SETM0(wv2k + 65536);
LDA1(ob[0], xo0, osrc + 0);
SB(); M32A(Y[1], AF(1), xb[1][1]); SB();
afh[0] = TR(lbA, 8704);
GLDS(lvoffA, lbase, 0);
LDA1(ob[1], xo0, osrc + 1024);
SB(); M32A(Y[0], AF(2), xb[0][2]); SB();
afl[1] = TR(lbA, 10240);
SB(); M32A(Y[1], AF(2), xb[1][2]); SB();
afh[1] = TR(lbA, 10752);
LDA1(ob[2], xo0, osrc + 2048);
SB(); M32A(Y[0], AF(3), xb[0][3]); SB();
afl[2] = TR(lbA, 12288);
GLDS(lvoffB, lbase, 1024);
LDA1(ob[3], xo0, osrc + 3072);
gt0 = T[0] * T[0]; PIN(gt0);
SB(); M32A(Y[1], AF(3), xb[1][3]); SB();
afh[2] = TR(lbA, 12800);
lbase = nbase; lstep = 64 * nstride; lvoffA = rowA * nstride + c16P1; lvoffB = (rowA + 8) * nstride + c16P1 - 1024;
gt0 = __builtin_fmaf(gt0, C1, C0); PIN(gt0);
SB();
asm volatile("s_waitcnt vmcnt(21)\n\ts_barrier" ::: "memory");
SB(); M32A(Y[0], AF(0), xb[0][4]); SB();
afl[3] = TR(lbA, 14336);
LDA1(ob[4], xo0, osrc + 4096);
gt0 = T[0] * gt0; PIN(gt0);
gt1 = T[1] * T[1]; PIN(gt1);
SB(); M32A(Y[1], AF(0), xb[1][4]); SB();
afh[3] = TR(lbA, 14848);
LDA1(ob[5], xo0, osrc + 5120);
gt0 = __builtin_amdgcn_exp2f(gt0); PIN(gt0);
gt1 = __builtin_fmaf(gt1, C1, C0); PIN(gt1);
SB(); M32A(Y[0], AF(1), xb[0][5]); SB();
afl[0] = TR(lbA, 16384);
SETM0(wv2k + 0);
gt0 = gt0 + 1.0f; PIN(gt0);
gt1 = T[1] * gt1; PIN(gt1);
gt2 = T[2] * T[2]; PIN(gt2);
SB(); M32A(Y[1], AF(1), xb[1][5]); SB();
afh[0] = TR(lbA, 16896);
GLDS(lvoffA, lbase, 0);
LDA1(ob[6], xo0, osrc + 6144);
gt0 = __builtin_amdgcn_rcpf(gt0); PIN(gt0);
SB(); M32A(Y[0], AF(2), xb[0][6]); SB();
afl[1] = TR(lbA, 18432);
LDA1(ob[7], xo0, osrc + 7168);
{ float v = T[0] * gt0; PIN(v); T[0] = v; }
gt1 = __builtin_amdgcn_exp2f(gt1); PIN(gt1);
gt2 = __builtin_fmaf(gt2, C1, C0); PIN(gt2);
SB(); M32A(Y[1], AF(2), xb[1][6]); SB();
afh[1] = TR(lbA, 18944);
gt1 = gt1 + 1.0f; PIN(gt1);
gt2 = T[2] * gt2; PIN(gt2);
gt0 = T[3] * T[3]; PIN(gt0);
SB(); M32A(Y[0], AF(3), xb[0][7]); SB();
afl[2] = TR(lbA, 20480);
GLDS(lvoffB, lbase, 1024);
LDA1(ob[8], xo0, osrc + 8192);
gt1 = __builtin_amdgcn_rcpf(gt1); PIN(gt1);
gt0 = __builtin_fmaf(gt0, C1, C0); PIN(gt0);
SB(); M32A(Y[1], AF(3), xb[1][7]); SB();
afh[2] = TR(lbA, 20992);
lbase += lstep;
LDA1(ob[9], xo0, osrc + 9216);
{ float v = T[1] * gt1; PIN(v); T[1] = v; }
SB();
asm volatile("s_waitcnt vmcnt(27)\n\ts_barrier" ::: "memory");
SB(); M32A(Y[0], AF(0), xb[0][8]); SB();
afl[3] = TR(lbA, 22528);
gt2 = __builtin_amdgcn_exp2f(gt2); PIN(gt2);
gt0 = T[3] * gt0; PIN(gt0);
gt1 = T[4] * T[4]; PIN(gt1);
SB(); M32A(Y[1], AF(0), xb[1][8]); SB();
afh[3] = TR(lbA, 23040);
LDA1(ob[10], xo0, osrc + 10240);
gt2 = gt2 + 1.0f; PIN(gt2);
gt0 = __builtin_amdgcn_exp2f(gt0); PIN(gt0);
gt1 = __builtin_fmaf(gt1, C1, C0); PIN(gt1);
SB(); M32A(Y[0], AF(1), xb[0][9]); SB();
afl[0] = TR(lbA, 24576);
SETM0(wv2k + 8192);
LDA1(ob[11], xo0, osrc + 11264);
gt2 = __builtin_amdgcn_rcpf(gt2); PIN(gt2);
gt0 = gt0 + 1.0f; PIN(gt0);
SB(); M32A(Y[1], AF(1), xb[1][9]); SB();
afh[0] = TR(lbA, 25088);
GLDS(lvoffA, lbase, 0);
{ float v = T[2] * gt2; PIN(v); T[2] = v; }
gt0 = __builtin_amdgcn_rcpf(gt0); PIN(gt0);
gt1 = T[4] * gt1; PIN(gt1);
SB(); M32A(Y[0], AF(2), xb[0][10]); SB();
afl[1] = TR(lbA, 26624);
LDA1(ob[12], xo0, osrc + 12288);
{ float v = T[3] * gt0; PIN(v); T[3] = v; }
gt1 = __builtin_amdgcn_exp2f(gt1); PIN(gt1);
gt2 = T[5] * T[5]; PIN(gt2);
SB(); M32A(Y[1], AF(2), xb[1][10]); SB();
afh[1] = TR(lbA, 27136);
LDA1(ob[13], xo0, osrc + 13312);
gt1 = gt1 + 1.0f; PIN(gt1);
gt2 = __builtin_fmaf(gt2, C1, C0); PIN(gt2);
gt0 = T[6] * T[6]; PIN(gt0);
SB(); M32A(Y[0], AF(3), xb[0][11]); SB();
afl[2] = TR(lbA, 28672);
GLDS(lvoffB, lbase, 1024);
gt1 = __builtin_amdgcn_rcpf(gt1); PIN(gt1);
gt2 = T[5] * gt2; PIN(gt2);
gt0 = __builtin_fmaf(gt0, C1, C0); PIN(gt0);
SB(); M32A(Y[1], AF(3), xb[1][11]); SB();
afh[2] = TR(lbA, 29184);
lbase += lstep;
LDA1(ob[14], xo0, osrc + 14336);
{ float v = T[4] * gt1; PIN(v); T[4] = v; }
SB();
asm volatile("s_waitcnt vmcnt(22)\n\ts_barrier" ::: "memory");
SIGNAL_SET(fHme, 4, true);
SB(); M32A(Y[0], AF(0), xb[0][12]); SB();
afl[3] = TR(lbA, 30720);
LDA1(ob[15], xo0, osrc + 15360);
gt2 = __builtin_amdgcn_exp2f(gt2); PIN(gt2);
gt0 = T[6] * gt0; PIN(gt0);
gt1 = T[7] * T[7]; PIN(gt1);
SB(); M32A(Y[1], AF(0), xb[1][12]); SB();
afh[3] = TR(lbA, 31232);
gt2 = gt2 + 1.0f; PIN(gt2);
gt0 = __builtin_amdgcn_exp2f(gt0); PIN(gt0);
gt1 = __builtin_fmaf(gt1, C1, C0); PIN(gt1);
SB(); M32A(Y[0], AF(1), xb[0][13]); SB();
afl[0] = TR(lbA, 32768);
SETM0(wv2k + 16384);
LDA1(ob[16], xo0, osrc + 16384);
gt2 = __builtin_amdgcn_rcpf(gt2); PIN(gt2);
gt0 = gt0 + 1.0f; PIN(gt0);
SB(); M32A(Y[1], AF(1), xb[1][13]); SB();
afh[0] = TR(lbA, 33280);
GLDS(lvoffA, lbase, 0);
LDA1(ob[17], xo0, osrc + 17408);
{ float v = T[5] * gt2; PIN(v); T[5] = v; }
gt1 = T[7] * gt1; PIN(gt1);
SB(); M32A(Y[0], AF(2), xb[0][14]); SB();
afl[1] = TR(lbA, 34816);
gt0 = __builtin_amdgcn_rcpf(gt0); PIN(gt0);
gt1 = __builtin_amdgcn_exp2f(gt1); PIN(gt1);
gt2 = T[8] * T[8]; PIN(gt2);
SB(); M32A(Y[1], AF(2), xb[1][14]); SB();
afh[1] = TR(lbA, 35328);
LDA1(ob[18], xo0, osrc + 18432);
{ float v = T[6] * gt0; PIN(v); T[6] = v; }
gt1 = gt1 + 1.0f; PIN(gt1);
gt2 = __builtin_fmaf(gt2, C1, C0); PIN(gt2);
SB(); M32A(Y[0], AF(3), xb[0][15]); SB();
afl[2] = TR(lbA, 36864);
GLDS(lvoffB, lbase, 1024);
LDA1(ob[19], xo0, osrc + 19456);
gt1 = __builtin_amdgcn_rcpf(gt1); PIN(gt1);
gt2 = T[8] * gt2; PIN(gt2);
SB(); M32A(Y[1], AF(3), xb[1][15]); SB();
afh[2] = TR(lbA, 37376);
lbase += lstep;
{ float v = T[7] * gt1; PIN(v); T[7] = v; }
gt0 = T[9] * T[9]; PIN(gt0);
SB();
asm volatile("s_waitcnt vmcnt(35)\n\ts_barrier" ::: "memory");
SB(); M32A(Y[0], AF(0), xb[0][16]); SB();
afl[3] = TR(lbA, 38912);
LDA1(ob[20], xo0, osrc + 20480);
gt2 = __builtin_amdgcn_exp2f(gt2); PIN(gt2);
gt0 = __builtin_fmaf(gt0, C1, C0); PIN(gt0);
gt1 = T[10] * T[10]; PIN(gt1);
SB(); M32A(Y[1], AF(0), xb[1][16]); SB();
afh[3] = TR(lbA, 39424);
LDA1(ob[21], xo0, osrc + 21504);
gt2 = gt2 + 1.0f; PIN(gt2);
gt0 = T[9] * gt0; PIN(gt0);
gt1 = __builtin_fmaf(gt1, C1, C0); PIN(gt1);
SB(); M32A(Y[0], AF(1), xb[0][17]); SB();
afl[0] = TR(lbA, 40960);
SETM0(wv2k + 24576);
gt2 = __builtin_amdgcn_rcpf(gt2); PIN(gt2);
gt0 = __builtin_amdgcn_exp2f(gt0); PIN(gt0);
gt1 = T[10] * gt1; PIN(gt1);
SB(); M32A(Y[1], AF(1), xb[1][17]); SB();
afh[0] = TR(lbA, 41472);
GLDS(lvoffA, lbase, 0);
LDA1(ob[22], xo0, osrc + 22528);
{ float v = T[8] * gt2; PIN(v); T[8] = v; }
gt0 = gt0 + 1.0f; PIN(gt0);
SB(); M32A(Y[0], AF(2), xb[0][18]); SB();
afl[1] = TR(lbA, 43008);
LDA1(ob[23], xo0, osrc + 23552);
gt0 = __builtin_amdgcn_rcpf(gt0); PIN(gt0);
gt1 = __builtin_amdgcn_exp2f(gt1); PIN(gt1);
SB(); M32A(Y[1], AF(2), xb[1][18]); SB();
afh[1] = TR(lbA, 43520);
{ float v = T[9] * gt0; PIN(v); T[9] = v; }
gt1 = gt1 + 1.0f; PIN(gt1);
gt2 = T[11] * T[11]; PIN(gt2);
SB(); M32A(Y[0], AF(3), xb[0][19]); SB();
afl[2] = TR(lbA, 45056);
GLDS(lvoffB, lbase, 1024);
LDA1(ob[24], xo0, osrc + 24576);
gt1 = __builtin_amdgcn_rcpf(gt1); PIN(gt1);
gt2 = __builtin_fmaf(gt2, C1, C0); PIN(gt2);
SB(); M32A(Y[1], AF(3), xb[1][19]); SB();
afh[2] = TR(lbA, 45568);
lbase += lstep;
LDA1(ob[25], xo0, osrc + 25600);
{ float v = T[10] * gt1; PIN(v); T[10] = v; }
SB();
asm volatile("s_waitcnt vmcnt(40)\n\ts_barrier" ::: "memory");
SB(); M32A(Y[0], AF(0), xb[0][20]); SB();
afl[3] = TR(lbA, 47104);
gt2 = T[11] * gt2; PIN(gt2);
gt0 = T[12] * T[12]; PIN(gt0);
gt1 = T[13] * T[13]; PIN(gt1);
SB(); M32A(Y[1], AF(0), xb[1][20]); SB();
afh[3] = TR(lbA, 47616);
LDA1(ob[26], xo0, osrc + 26624);
gt2 = __builtin_amdgcn_exp2f(gt2); PIN(gt2);
gt0 = __builtin_fmaf(gt0, C1, C0); PIN(gt0);
gt1 = __builtin_fmaf(gt1, C1, C0); PIN(gt1);
SB(); M32A(Y[0], AF(1), xb[0][21]); SB();
afl[0] = TR(lbA, 49152);
SETM0(wv2k + 32768);
LDA1(ob[27], xo0, osrc + 27648);
gt2 = gt2 + 1.0f; PIN(gt2);
gt0 = T[12] * gt0; PIN(gt0);
gt1 = T[13] * gt1; PIN(gt1);
SB(); M32A(Y[1], AF(1), xb[1][21]); SB();
afh[0] = TR(lbA, 49664);
GLDS(lvoffA, lbase, 0);
gt2 = __builtin_amdgcn_rcpf(gt2); PIN(gt2);
gt0 = __builtin_amdgcn_exp2f(gt0); PIN(gt0);
SB(); M32A(Y[0], AF(2), xb[0][22]); SB();
afl[1] = TR(lbA, 51200);
LDA1(ob[28], xo0, osrc + 28672);
{ float v = T[11] * gt2; PIN(v); T[11] = v; }
gt0 = gt0 + 1.0f; PIN(gt0);
gt1 = __builtin_amdgcn_exp2f(gt1); PIN(gt1);
SB(); M32A(Y[1], AF(2), xb[1][22]); SB();
afh[1] = TR(lbA, 51712);
LDA1(ob[29], xo0, osrc + 29696);
gt0 = __builtin_amdgcn_rcpf(gt0); PIN(gt0);
gt1 = gt1 + 1.0f; PIN(gt1);
gt2 = T[14] * T[14]; PIN(gt2);
SB(); M32A(Y[0], AF(3), xb[0][23]); SB();
afl[2] = TR(lbA, 53248);
GLDS(lvoffB, lbase, 1024);
{ float v = T[12] * gt0; PIN(v); T[12] = v; }
gt1 = __builtin_amdgcn_rcpf(gt1); PIN(gt1);
gt2 = __builtin_fmaf(gt2, C1, C0); PIN(gt2);
SB(); M32A(Y[1], AF(3), xb[1][23]); SB();
afh[2] = TR(lbA, 53760);
lbase += lstep;
LDA1(ob[30], xo0, osrc + 30720);
{ float v = T[13] * gt1; PIN(v); T[13] = v; }
SB();
asm volatile("s_waitcnt vmcnt(43)\n\ts_barrier" ::: "memory");
SB(); M32A(Y[0], AF(0), xb[0][24]); SB();
afl[3] = TR(lbA, 55296);
LDA1(ob[31], xo0, osrc + 31744);
gt2 = T[14] * gt2; PIN(gt2);
gt0 = T[15] * T[15]; PIN(gt0);
gt1 = T[16] * T[16]; PIN(gt1);
SB(); M32A(Y[1], AF(0), xb[1][24]); SB();
afh[3] = TR(lbA, 55808);
gt2 = __builtin_amdgcn_exp2f(gt2); PIN(gt2);
gt0 = __builtin_fmaf(gt0, C1, C0); PIN(gt0);
gt1 = __builtin_fmaf(gt1, C1, C0); PIN(gt1);
SB(); M32A(Y[0], AF(1), xb[0][25]); SB();
afl[0] = TR(lbA, 57344);
SETM0(wv2k + 40960);
LDA1(ob[32], xo0, osrc + 32768);
gt2 = gt2 + 1.0f; PIN(gt2);
gt0 = T[15] * gt0; PIN(gt0);
gt1 = T[16] * gt1; PIN(gt1);
SB(); M32A(Y[1], AF(1), xb[1][25]); SB();
afh[0] = TR(lbA, 57856);
GLDS(lvoffA, lbase, 0);
LDA1(ob[33], xo0, osrc + 33792);
gt2 = __builtin_amdgcn_rcpf(gt2); PIN(gt2);
SB(); M32A(Y[0], AF(2), xb[0][26]); SB();
afl[1] = TR(lbA, 59392);
{ float v = T[14] * gt2; PIN(v); T[14] = v; }
gt0 = __builtin_amdgcn_exp2f(gt0); PIN(gt0);
gt1 = __builtin_amdgcn_exp2f(gt1); PIN(gt1);
SB(); M32A(Y[1], AF(2), xb[1][26]); SB();
afh[1] = TR(lbA, 59904);
LDA1(ob[34], xo0, osrc + 34816);
gt0 = gt0 + 1.0f; PIN(gt0);
gt1 = gt1 + 1.0f; PIN(gt1);
gt2 = T[17] * T[17]; PIN(gt2);
SB(); M32A(Y[0], AF(3), xb[0][27]); SB();
afl[2] = TR(lbA, 61440);
GLDS(lvoffB, lbase, 1024);
LDA1(ob[35], xo0, osrc + 35840);
gt0 = __builtin_amdgcn_rcpf(gt0); PIN(gt0);
gt2 = __builtin_fmaf(gt2, C1, C0); PIN(gt2);
SB(); M32A(Y[1], AF(3), xb[1][27]); SB();
afh[2] = TR(lbA, 61952);
lbase += lstep;
{ float v = T[15] * gt0; PIN(v); T[15] = v; }
gt2 = T[17] * gt2; PIN(gt2);
SB();
asm volatile("s_waitcnt vmcnt(45)\n\ts_barrier" ::: "memory");
SB(); M32A(Y[0], AF(0), xb[0][28]); SB();
afl[3] = TR(lbA, 63488);
gt1 = __builtin_amdgcn_rcpf(gt1); PIN(gt1);
gt2 = __builtin_amdgcn_exp2f(gt2); PIN(gt2);
gt0 = T[18] * T[18]; PIN(gt0);
SB(); M32A(Y[1], AF(0), xb[1][28]); SB();
afh[3] = TR(lbA, 64000);
{ float v = T[16] * gt1; PIN(v); T[16] = v; }
gt2 = gt2 + 1.0f; PIN(gt2);
gt0 = __builtin_fmaf(gt0, C1, C0); PIN(gt0);
SB(); M32A(Y[0], AF(1), xb[0][29]); SB();
afl[0] = TR(lbA, 65536);
SETM0(wv2k + 49152);
gt2 = __builtin_amdgcn_rcpf(gt2); PIN(gt2);
gt0 = T[18] * gt0; PIN(gt0);
gt1 = T[19] * T[19]; PIN(gt1);
SB(); M32A(Y[1], AF(1), xb[1][29]); SB();
afh[0] = TR(lbA, 66048);
GLDS(lvoffA, lbase, 0);
{ float v = T[17] * gt2; PIN(v); T[17] = v; }
gt0 = __builtin_amdgcn_exp2f(gt0); PIN(gt0);
gt1 = __builtin_fmaf(gt1, C1, C0); PIN(gt1);
SB(); M32V(Y[0], AF(2), xb[0][30]); SB();
afl[1] = TR(lbA, 67584);
gt0 = gt0 + 1.0f; PIN(gt0);
gt1 = T[19] * gt1; PIN(gt1);
gt2 = T[20] * T[20]; PIN(gt2);
SB(); M32V(Y[1], AF(2), xb[1][30]); SB();
afh[1] = TR(lbA, 68096);
gt0 = __builtin_amdgcn_rcpf(gt0); PIN(gt0);
gt1 = __builtin_amdgcn_exp2f(gt1); PIN(gt1);
gt2 = __builtin_fmaf(gt2, C1, C0); PIN(gt2);
SB(); M32V(Y[0], AF(3), xb[0][31]); SB();
afl[2] = TR(lbA, 69632);
GLDS(lvoffB, lbase, 1024);
{ float v = T[18] * gt0; PIN(v); T[18] = v; }
gt1 = gt1 + 1.0f; PIN(gt1);
gt2 = T[20] * gt2; PIN(gt2);
SB(); M32V(Y[1], AF(3), xb[1][31]); SB();
afh[2] = TR(lbA, 70144);
lbase += lstep;
gt1 = __builtin_amdgcn_rcpf(gt1); PIN(gt1);
SB();
asm volatile("s_waitcnt vmcnt(40)\n\ts_barrier" ::: "memory");
SB(); M32V(Y[0], AF(0), xb[0][32]); SB();
afl[3] = TR(lbA, 71680);
{ float v = T[19] * gt1; PIN(v); T[19] = v; }
gt2 = __builtin_amdgcn_exp2f(gt2); PIN(gt2);
gt0 = T[21] * T[21]; PIN(gt0);
SB(); M32V(Y[1], AF(0), xb[1][32]); SB();
afh[3] = TR(lbA, 72192);
gt2 = gt2 + 1.0f; PIN(gt2);
gt0 = __builtin_fmaf(gt0, C1, C0); PIN(gt0);
gt1 = T[22] * T[22]; PIN(gt1);
SB(); M32V(Y[0], AF(1), xb[0][33]); SB();
afl[0] = TR(lbW0, 0);
SETM0(wv2k + 57344);
gt2 = __builtin_amdgcn_rcpf(gt2); PIN(gt2);
gt0 = T[21] * gt0; PIN(gt0);
gt1 = __builtin_fmaf(gt1, C1, C0); PIN(gt1);
SB(); M32V(Y[1], AF(1), xb[1][33]); SB();
afh[0] = TR(lbW0, 512);
GLDS(lvoffA, lbase, 0);
{ float v = T[20] * gt2; PIN(v); T[20] = v; }
gt0 = __builtin_amdgcn_exp2f(gt0); PIN(gt0);
gt1 = T[22] * gt1; PIN(gt1);
SB(); M32V(Y[0], AF(2), xb[0][34]); SB();
afl[1] = TR(lbW1, 0);
gt0 = gt0 + 1.0f; PIN(gt0);
gt1 = __builtin_amdgcn_exp2f(gt1); PIN(gt1);
gt2 = T[23] * T[23]; PIN(gt2);
SB(); M32V(Y[1], AF(2), xb[1][34]); SB();
afh[1] = TR(lbW1, 512);
gt0 = __builtin_amdgcn_rcpf(gt0); PIN(gt0);
gt1 = gt1 + 1.0f; PIN(gt1);
gt2 = __builtin_fmaf(gt2, C1, C0); PIN(gt2);
SB(); M32V(Y[0], AF(3), xb[0][35]); SB();
afl[2] = TR(lbW0, 2048);
GLDS(lvoffB, lbase, 1024);
{ float v = T[21] * gt0; PIN(v); T[21] = v; }
gt1 = __builtin_amdgcn_rcpf(gt1); PIN(gt1);
gt2 = T[23] * gt2; PIN(gt2);
SB(); M32V(Y[1], AF(3), xb[1][35]); SB();
afh[2] = TR(lbW0, 2560);
lbase += lstep;
{ float v = T[22] * gt1; PIN(v); T[22] = v; }
gt0 = T[24] * T[24]; PIN(gt0);
}
STAMP(7);
{
SB();
asm volatile("s_waitcnt vmcnt(34)\n\ts_barrier" : "+a"(ob[0]), "+a"(ob[1]), "+a"(ob[2]), "+a"(ob[3]) :: "memory");
SB(); M32ZA(X[0], AF(0), ob[0]); SB();
afl[3] = TR(lbW1, 2048);
afh[3] = TR(lbW1, 2560);
gt2 = __builtin_amdgcn_exp2f(gt2); PIN(gt2);
gt0 = __builtin_fmaf(gt0, C1, C0); PIN(gt0);
SB(); M32ZA(X[1], AF(1), ob[0]); SB();
afl[0] = TR(lbW0, 4096);
afh[0] = TR(lbW0, 4608);
gt2 = gt2 + 1.0f; PIN(gt2);
gt0 = T[24] * gt0; PIN(gt0);
gt1 = T[25] * T[25]; PIN(gt1);
SB(); M32A(X[0], AF(2), ob[1]); SB();
afl[1] = TR(lbW1, 4096);
afh[1] = TR(lbW1, 4608);
SETM0(wv2k + 65536);
gt2 = __builtin_amdgcn_rcpf(gt2); PIN(gt2);
SB(); M32A(X[1], AF(3), ob[1]); SB();
afl[2] = TR(lbW0, 6144);
afh[2] = TR(lbW0, 6656);
GLDS(lvoffA, lbase, 0);
{ float v = T[23] * gt2; PIN(v); T[23] = v; }
gt1 = __builtin_fmaf(gt1, C1, C0); PIN(gt1);
SB(); M32A(X[0], AF(0), ob[2]); SB();
afl[3] = TR(lbW1, 6144);
afh[3] = TR(lbW1, 6656);
gt0 = __builtin_amdgcn_exp2f(gt0); PIN(gt0);
gt1 = T[25] * gt1; PIN(gt1);
SB(); M32A(X[1], AF(1), ob[2]); SB();
afl[0] = TR(lbW0, 8192);
afh[0] = TR(lbW0, 8704);
gt0 = gt0 + 1.0f; PIN(gt0);
gt1 = __builtin_amdgcn_exp2f(gt1); PIN(gt1);
SB(); M32A(X[0], AF(2), ob[3]); SB();
afl[1] = TR(lbW1, 8192);
afh[1] = TR(lbW1, 8704);
GLDS(lvoffB, lbase, 1024);
gt0 = __builtin_amdgcn_rcpf(gt0); PIN(gt0);
SB(); M32A(X[1], AF(3), ob[3]); SB();
afl[2] = TR(lbW0, 10240);
afh[2] = TR(lbW0, 10752);
lbase = wdb + 0; lvoffA = rowA * 1152 + c16P2; lvoffB = (rowA + 8) * 1152 + c16P2 - 1024;
SB();
asm volatile("s_waitcnt vmcnt(29)\n\ts_barrier" : "+a"(ob[4]), "+a"(ob[5]), "+a"(ob[6]), "+a"(ob[7]) :: "memory");
SB(); M32A(X[0], AF(0), ob[4]); SB();
afl[3] = TR(lbW1, 10240);
afh[3] = TR(lbW1, 10752);
{ float v = T[24] * gt0; PIN(v); T[24] = v; }
gt1 = gt1 + 1.0f; PIN(gt1);
gt2 = T[26] * T[26]; PIN(gt2);
SB(); M32A(X[1], AF(1), ob[4]); SB();
afl[0] = TR(lbW0, 12288);
afh[0] = TR(lbW0, 12800);
gt1 = __builtin_amdgcn_rcpf(gt1); PIN(gt1);
gt2 = __builtin_fmaf(gt2, C1, C0); PIN(gt2);
SB(); M32A(X[0], AF(2), ob[5]); SB();
afl[1] = TR(lbW1, 12288);
afh[1] = TR(lbW1, 12800);
SETM0(wv2k + 0);
{ float v = T[25] * gt1; PIN(v); T[25] = v; }
gt2 = T[26] * gt2; PIN(gt2);
SB(); M32A(X[1], AF(3), ob[5]); SB();
afl[2] = TR(lbW0, 14336);
afh[2] = TR(lbW0, 14848);
GLDS(lvoffA, lbase, 0);
gt2 = __builtin_amdgcn_exp2f(gt2); PIN(gt2);
SB(); M32A(X[0], AF(0), ob[6]); SB();
afl[3] = TR(lbW1, 14336);
afh[3] = TR(lbW1, 14848);
gt2 = gt2 + 1.0f; PIN(gt2);
gt0 = T[27] * T[27]; PIN(gt0);
gt1 = T[28] * T[28]; PIN(gt1);
SB(); M32A(X[1], AF(1), ob[6]); SB();
afl[0] = TR(lbW0, 16384);
afh[0] = TR(lbW0, 16896);
gt2 = __builtin_amdgcn_rcpf(gt2); PIN(gt2);
gt0 = __builtin_fmaf(gt0, C1, C0); PIN(gt0);
SB(); M32A(X[0], AF(2), ob[7]); SB();
afl[1] = TR(lbW1, 16384);
afh[1] = TR(lbW1, 16896);
GLDS(lvoffB, lbase, 1024);
{ float v = T[26] * gt2; PIN(v); T[26] = v; }
gt0 = T[27] * gt0; PIN(gt0);
SB(); M32A(X[1], AF(3), ob[7]); SB();
afl[2] = TR(lbW0, 18432);
afh[2] = TR(lbW0, 18944);
lbase = wdb + 73728;
SB();
asm volatile("s_waitcnt vmcnt(24)\n\ts_barrier" : "+a"(ob[8]), "+a"(ob[9]), "+a"(ob[10]), "+a"(ob[11]) :: "memory");
SB(); M32A(X[0], AF(0), ob[8]); SB();
afl[3] = TR(lbW1, 18432);
afh[3] = TR(lbW1, 18944);
gt0 = __builtin_amdgcn_exp2f(gt0); PIN(gt0);
gt1 = __builtin_fmaf(gt1, C1, C0); PIN(gt1);
SB(); M32A(X[1], AF(1), ob[8]); SB();
afl[0] = TR(lbW0, 20480);
afh[0] = TR(lbW0, 20992);
gt0 = gt0 + 1.0f; PIN(gt0);
gt1 = T[28] * gt1; PIN(gt1);
gt2 = T[29] * T[29]; PIN(gt2);
SB(); M32A(X[0], AF(2), ob[9]); SB();
afl[1] = TR(lbW1, 20480);
afh[1] = TR(lbW1, 20992);
SETM0(wv2k + 8192);
gt0 = __builtin_amdgcn_rcpf(gt0); PIN(gt0);
SB(); M32A(X[1], AF(3), ob[9]); SB();
afl[2] = TR(lbW0, 22528);
afh[2] = TR(lbW0, 23040);
GLDS(lvoffA, lbase, 0);
{ float v = T[27] * gt0; PIN(v); T[27] = v; }
gt2 = __builtin_fmaf(gt2, C1, C0); PIN(gt2);
SB(); M32A(X[0], AF(0), ob[10]); SB();
afl[3] = TR(lbW1, 22528);
afh[3] = TR(lbW1, 23040);
gt1 = __builtin_amdgcn_exp2f(gt1); PIN(gt1);
gt2 = T[29] * gt2; PIN(gt2);
SB(); M32A(X[1], AF(1), ob[10]); SB();
afl[0] = TR(lbW0, 24576);
afh[0] = TR(lbW0, 25088);
gt1 = gt1 + 1.0f; PIN(gt1);
gt2 = __builtin_amdgcn_exp2f(gt2); PIN(gt2);
SB(); M32A(X[0], AF(2), ob[11]); SB();
afl[1] = TR(lbW1, 24576);
afh[1] = TR(lbW1, 25088);
GLDS(lvoffB, lbase, 1024);
gt1 = __builtin_amdgcn_rcpf(gt1); PIN(gt1);
SB(); M32A(X[1], AF(3), ob[11]); SB();
afl[2] = TR(lbW0, 26624);
afh[2] = TR(lbW0, 27136);
lbase = wdb + 147456;
SB();
asm volatile("s_waitcnt vmcnt(18)\n\ts_barrier" : "+a"(ob[12]), "+a"(ob[13]), "+a"(ob[14]), "+a"(ob[15]) :: "memory");
SB(); M32A(X[0], AF(0), ob[12]); SB();
afl[3] = TR(lbW1, 26624);
afh[3] = TR(lbW1, 27136);
{ float v = T[28] * gt1; PIN(v); T[28] = v; }
gt2 = gt2 + 1.0f; PIN(gt2);
gt0 = T[30] * T[30]; PIN(gt0);
SB(); M32A(X[1], AF(1), ob[12]); SB();
afl[0] = TR(lbW0, 28672);
afh[0] = TR(lbW0, 29184);
gt2 = __builtin_amdgcn_rcpf(gt2); PIN(gt2);
gt0 = __builtin_fmaf(gt0, C1, C0); PIN(gt0);
SB(); M32A(X[0], AF(2), ob[13]); SB();
afl[1] = TR(lbW1, 28672);
afh[1] = TR(lbW1, 29184);
SETM0(wv2k + 16384);
{ float v = T[29] * gt2; PIN(v); T[29] = v; }
gt0 = T[30] * gt0; PIN(gt0);
SB(); M32A(X[1], AF(3), ob[13]); SB();
afl[2] = TR(lbW0, 30720);
afh[2] = TR(lbW0, 31232);
GLDS(lvoffA, lbase, 0);
gt0 = __builtin_amdgcn_exp2f(gt0); PIN(gt0);
SB(); M32A(X[0], AF(0), ob[14]); SB();
afl[3] = TR(lbW1, 30720);
afh[3] = TR(lbW1, 31232);
gt0 = gt0 + 1.0f; PIN(gt0);
gt1 = T[31] * T[31]; PIN(gt1);
{ float v = T[0] * Y[0][0]; PIN(v); T[0] = v; }
SB(); M32A(X[1], AF(1), ob[14]); SB();
afl[0] = TR(lbW0, 32768);
afh[0] = TR(lbW0, 33280);
gt0 = __builtin_amdgcn_rcpf(gt0); PIN(gt0);
gt1 = __builtin_fmaf(gt1, C1, C0); PIN(gt1);
SB(); M32A(X[0], AF(2), ob[15]); SB();
afl[1] = TR(lbW1, 32768);
afh[1] = TR(lbW1, 33280);
GLDS(lvoffB, lbase, 1024);
{ float v = T[30] * gt0; PIN(v); T[30] = v; }
gt1 = T[31] * gt1; PIN(gt1);
SB(); M32A(X[1], AF(3), ob[15]); SB();
afl[2] = TR(lbW0, 34816);
afh[2] = TR(lbW0, 35328);
lbase = wdb + 221184;
SB();
asm volatile("s_waitcnt vmcnt(13)\n\ts_barrier" : "+a"(ob[16]), "+a"(ob[17]), "+a"(ob[18]), "+a"(ob[19]) :: "memory");
SB(); M32A(X[0], AF(0), ob[16]); SB();
afl[3] = TR(lbW1, 34816);
afh[3] = TR(lbW1, 35328);
gt1 = __builtin_amdgcn_exp2f(gt1); PIN(gt1);
{ float v = T[1] * Y[0][1]; PIN(v); T[1] = v; }
SB(); M32A(X[1], AF(1), ob[16]); SB();
afl[0] = TR(lbW0, 36864);
afh[0] = TR(lbW0, 37376);
gt1 = gt1 + 1.0f; PIN(gt1);
{ unsigned u = pk_bf16(T[0], T[1]); PIN(u); hh[0] = u; }
{ float v = T[2] * Y[0][2]; PIN(v); T[2] = v; }
SB(); M32A(X[0], AF(2), ob[17]); SB();
afl[1] = TR(lbW1, 36864);
afh[1] = TR(lbW1, 37376);
SETM0(wv2k + 24576);
gt1 = __builtin_amdgcn_rcpf(gt1); PIN(gt1);
SB(); M32A(X[1], AF(3), ob[17]); SB();
afl[2] = TR(lbW0, 38912);
afh[2] = TR(lbW0, 39424);
GLDS(lvoffA, lbase, 0);
{ float v = T[31] * gt1; PIN(v); T[31] = v; }
{ float v = T[3] * Y[0][3]; PIN(v); T[3] = v; }
SB(); M32A(X[0], AF(0), ob[18]); SB();
afl[3] = TR(lbW1, 38912);
afh[3] = TR(lbW1, 39424);
{ unsigned u = pk_bf16(T[2], T[3]); PIN(u); hh[1] = u; }
{ float v = T[4] * Y[0][8]; PIN(v); T[4] = v; }
{ float v = T[5] * Y[0][9]; PIN(v); T[5] = v; }
SB(); M32A(X[1], AF(1), ob[18]); SB();
afl[0] = TR(lbW0, 40960);
afh[0] = TR(lbW0, 41472);
{ unsigned u = pk_bf16(T[4], T[5]); PIN(u); hh[2] = u; }
{ float v = T[6] * Y[0][10]; PIN(v); T[6] = v; }
{ float v = T[7] * Y[0][11]; PIN(v); T[7] = v; }
SB(); M32A(X[0], AF(2), ob[19]); SB();
afl[1] = TR(lbW1, 40960);
afh[1] = TR(lbW1, 41472);
GLDS(lvoffB, lbase, 1024);
{ unsigned u = pk_bf16(T[6], T[7]); PIN(u); hh[3] = u; }
{ float v = T[8] * Y[0][4]; PIN(v); T[8] = v; }
SB(); M32A(X[1], AF(3), ob[19]); SB();
afl[2] = TR(lbW0, 43008);
afh[2] = TR(lbW0, 43520);
lbase = wdb + 294912;
SB();
asm volatile("s_waitcnt vmcnt(12)\n\ts_barrier" : "+a"(ob[20]), "+a"(ob[21]), "+a"(ob[22]), "+a"(ob[23]) :: "memory");
SB(); M32A(X[0], AF(0), ob[20]); SB();
afl[3] = TR(lbW1, 43008);
afh[3] = TR(lbW1, 43520);
*(AS3 u32x4*)(uintptr_t)(hw_ad + 65536) = hh;
{ float v = T[9] * Y[0][5]; PIN(v); T[9] = v; }
SB(); M32A(X[1], AF(1), ob[20]); SB();
afl[0] = TR(lbW0, 45056);
afh[0] = TR(lbW0, 45568);
ST41(hxvo, hh, hxw + 65536);
SB(); M32A(X[0], AF(2), ob[21]); SB();
afl[1] = TR(lbW1, 45056);
afh[1] = TR(lbW1, 45568);
SETM0(wv2k + 32768);
{ unsigned u = pk_bf16(T[8], T[9]); PIN(u); hh[0] = u; }
{ float v = T[10] * Y[0][6]; PIN(v); T[10] = v; }
SB(); M32A(X[1], AF(3), ob[21]); SB();
afl[2] = TR(lbW0, 47104);
afh[2] = TR(lbW0, 47616);
GLDS(lvoffA, lbase, 0);
{ float v = T[11] * Y[0][7]; PIN(v); T[11] = v; }
{ float v = T[12] * Y[0][12]; PIN(v); T[12] = v; }
SB(); M32A(X[0], AF(0), ob[22]); SB();
afl[3] = TR(lbW1, 47104);
afh[3] = TR(lbW1, 47616);
{ unsigned u = pk_bf16(T[10], T[11]); PIN(u); hh[1] = u; }
{ float v = T[13] * Y[0][13]; PIN(v); T[13] = v; }
{ float v = T[14] * Y[0][14]; PIN(v); T[14] = v; }
SB(); M32A(X[1], AF(1), ob[22]); SB();
afl[0] = TR(lbW0, 49152);
afh[0] = TR(lbW0, 49664);
{ unsigned u = pk_bf16(T[12], T[13]); PIN(u); hh[2] = u; }
{ float v = T[15] * Y[0][15]; PIN(v); T[15] = v; }
{ float v = T[16] * Y[1][0]; PIN(v); T[16] = v; }
SB(); M32A(X[0], AF(2), ob[23]); SB();
afl[1] = TR(lbW1, 49152);
afh[1] = TR(lbW1, 49664);
GLDS(lvoffB, lbase, 1024);
{ unsigned u = pk_bf16(T[14], T[15]); PIN(u); hh[3] = u; }
{ float v = T[17] * Y[1][1]; PIN(v); T[17] = v; }
SB(); M32A(X[1], AF(3), ob[23]); SB();
afl[2] = TR(lbW0, 51200);
afh[2] = TR(lbW0, 51712);
lbase = wdb + 128;
SB();
asm volatile("s_waitcnt vmcnt(13)\n\ts_barrier" : "+a"(ob[24]), "+a"(ob[25]), "+a"(ob[26]), "+a"(ob[27]) :: "memory");
SB(); M32A(X[0], AF(0), ob[24]); SB();
afl[3] = TR(lbW1, 51200);
afh[3] = TR(lbW1, 51712);
*(AS3 u32x4*)(uintptr_t)(hw_ad + 66048) = hh;
{ float v = T[18] * Y[1][2]; PIN(v); T[18] = v; }
SB(); M32A(X[1], AF(1), ob[24]); SB();
afl[0] = TR(lbW0, 53248);
afh[0] = TR(lbW0, 53760);
ST41(hxvo, hh, hxw + 66048);
SB(); M32A(X[0], AF(2), ob[25]); SB();
afl[1] = TR(lbW1, 53248);
afh[1] = TR(lbW1, 53760);
SETM0(wv2k + 40960);
{ unsigned u = pk_bf16(T[16], T[17]); PIN(u); hh[0] = u; }
{ float v = T[19] * Y[1][3]; PIN(v); T[19] = v; }
SB(); M32A(X[1], AF(3), ob[25]); SB();
afl[2] = TR(lbW0, 55296);
afh[2] = TR(lbW0, 55808);
GLDS(lvoffA, lbase, 0);
{ unsigned u = pk_bf16(T[18], T[19]); PIN(u); hh[1] = u; }
{ float v = T[20] * Y[1][8]; PIN(v); T[20] = v; }
SB(); M32A(X[0], AF(0), ob[26]); SB();
afl[3] = TR(lbW1, 55296);
afh[3] = TR(lbW1, 55808);
{ float v = T[21] * Y[1][9]; PIN(v); T[21] = v; }
{ float v = T[22] * Y[1][10]; PIN(v); T[22] = v; }
{ float v = T[23] * Y[1][11]; PIN(v); T[23] = v; }
SB(); M32A(X[1], AF(1), ob[26]); SB();
afl[0] = TR(lbW0, 57344);
afh[0] = TR(lbW0, 57856);
{ unsigned u = pk_bf16(T[20], T[21]); PIN(u); hh[2] = u; }
{ unsigned u = pk_bf16(T[22], T[23]); PIN(u); hh[3] = u; }
{ float v = T[24] * Y[1][4]; PIN(v); T[24] = v; }
SB(); M32A(X[0], AF(2), ob[27]); SB();
afl[1] = TR(lbW1, 57344);
afh[1] = TR(lbW1, 57856);
GLDS(lvoffB, lbase, 1024);
*(AS3 u32x4*)(uintptr_t)(hw_ad + 67584) = hh;
SB(); M32A(X[1], AF(3), ob[27]); SB();
afl[2] = TR(lbW0, 59392);
afh[2] = TR(lbW0, 59904);
lbase = wdb + 73856;
SB();
asm volatile("s_waitcnt vmcnt(14)\n\ts_barrier" : "+a"(ob[28]), "+a"(ob[29]), "+a"(ob[30]), "+a"(ob[31]) :: "memory");
SB(); M32A(X[0], AF(0), ob[28]); SB();
afl[3] = TR(lbW1, 59392);
afh[3] = TR(lbW1, 59904);
ST41(hxvo, hh, hxw + 67584);
SB(); M32A(X[1], AF(1), ob[28]); SB();
afl[0] = TR(lbW0, 61440);
afh[0] = TR(lbW0, 61952);
{ float v = T[25] * Y[1][5]; PIN(v); T[25] = v; }
{ float v = T[26] * Y[1][6]; PIN(v); T[26] = v; }
{ float v = T[27] * Y[1][7]; PIN(v); T[27] = v; }
SB(); M32A(X[0], AF(2), ob[29]); SB();
afl[1] = TR(lbW1, 61440);
afh[1] = TR(lbW1, 61952);
SETM0(wv2k + 49152);
{ unsigned u = pk_bf16(T[24], T[25]); PIN(u); hh[0] = u; }
{ unsigned u = pk_bf16(T[26], T[27]); PIN(u); hh[1] = u; }
SB(); M32A(X[1], AF(3), ob[29]); SB();
afl[2] = TR(lbW0, 63488);
afh[2] = TR(lbW0, 64000);
GLDS(lvoffA, lbase, 0);
{ float v = T[28] * Y[1][12]; PIN(v); T[28] = v; }
{ float v = T[29] * Y[1][13]; PIN(v); T[29] = v; }
SB(); M32A(X[0], AF(0), ob[30]); SB();
afl[3] = TR(lbW1, 63488);
afh[3] = TR(lbW1, 64000);
{ unsigned u = pk_bf16(T[28], T[29]); PIN(u); hh[2] = u; }
{ float v = T[30] * Y[1][14]; PIN(v); T[30] = v; }
{ float v = T[31] * Y[1][15]; PIN(v); T[31] = v; }
SB(); M32A(X[1], AF(1), ob[30]); SB();
afl[0] = TR(lbW0, 65536);
afh[0] = TR(lbW0, 66048);
{ unsigned u = pk_bf16(T[30], T[31]); PIN(u); hh[3] = u; }
SB(); M32A(X[0], AF(2), ob[31]); SB();
afl[1] = TR(lbW1, 65536);
afh[1] = TR(lbW1, 66048);
GLDS(lvoffB, lbase, 1024);
*(AS3 u32x4*)(uintptr_t)(hw_ad + 68096) = hh;
SB(); M32A(X[1], AF(3), ob[31]); SB();
afl[2] = TR(lbW0, 67584);
afh[2] = TR(lbW0, 68096);
lbase = wdb + 147584;
SB();
asm volatile("s_waitcnt vmcnt(15)\n\ts_barrier" : "+a"(ob[32]), "+a"(ob[33]), "+a"(ob[34]), "+a"(ob[35]) :: "memory");
SB(); M32A(X[0], AF(0), ob[32]); SB();
afl[3] = TR(lbW1, 67584);
afh[3] = TR(lbW1, 68096);
ST41(hxvo, hh, hxw + 68096);
SB(); M32A(X[1], AF(1), ob[32]); SB();
afl[0] = TR(lbW0, 69632);
afh[0] = TR(lbW0, 70144);
SB(); M32A(X[0], AF(2), ob[33]); SB();
afl[1] = TR(lbW1, 69632);
afh[1] = TR(lbW1, 70144);
SETM0(wv2k + 57344);
SB(); M32A(X[1], AF(3), ob[33]); SB();
afl[2] = TR(lbW0, 71680);
afh[2] = TR(lbW0, 72192);
GLDS(lvoffA, lbase, 0);
SB(); M32A(X[0], AF(0), ob[34]); SB();
afl[3] = TR(lbW1, 71680);
afh[3] = TR(lbW1, 72192);
SB(); M32A(X[1], AF(1), ob[34]); SB();
afl[0] = TR(lbB0, 0);
afh[0] = TR(lbB0, 2048);
SB(); M32A(X[0], AF(2), ob[35]); SB();
afl[1] = TR(lbB1, 0);
afh[1] = TR(lbB1, 2048);
GLDS(lvoffB, lbase, 1024);
SB(); M32A(X[1], AF(3), ob[35]); SB();
afl[2] = TR(lbB0, 4096);
afh[2] = TR(lbB0, 6144);
lbase = wdb + 221312;
}
asm volatile("s_waitcnt lgkmcnt(0)" ::: "memory");
STAMP(8); JIT(8);
u32x4 hb[4][18];
f32x4 P[2][4], R[2][4], Q[2][4];
hb[0][0] = *(AS3 u32x4*)(uintptr_t)(hr_ad + 0);
hb[1][0] = *(AS3 u32x4*)(uintptr_t)(hr_ad + 1024);
hb[2][0] = *(AS3 u32x4*)(uintptr_t)(hr_ad + 2048);
hb[3][0] = *(AS3 u32x4*)(uintptr_t)(hr_ad + 3072);
hb[0][1] = *(AS3 u32x4*)(uintptr_t)(hr_ad + 8192);
hb[1][1] = *(AS3 u32x4*)(uintptr_t)(hr_ad + 9216);
hb[2][1] = *(AS3 u32x4*)(uintptr_t)(hr_ad + 10240);
hb[3][1] = *(AS3 u32x4*)(uintptr_t)(hr_ad + 11264);
SB();
asm volatile("s_waitcnt vmcnt(16)\n\ts_barrier" ::: "memory");
SB(); M16ZA(P[0][0], AF(0), hb[0][0]); SB();
afl[3] = TR(lbB1, 4096);
SB(); M16ZA(P[0][1], AF(0), hb[1][0]); SB();
afh[3] = TR(lbB1, 6144);
SB(); M16ZA(P[0][2], AF(0), hb[2][0]); SB();
LDW1(pw0, zvo, fHpa);
SB(); M16ZA(P[0][3], AF(0), hb[3][0]); SB();
hb[0][2] = *(AS3 u32x4*)(uintptr_t)(hr_ad + 16384);
SB(); M16ZA(P[1][0], AF(1), hb[0][0]); SB();
afl[0] = TR(lbB0, 8192);
SB(); M16ZA(P[1][1], AF(1), hb[1][0]); SB();
afh[0] = TR(lbB0, 10240);
SB(); M16ZA(P[1][2], AF(1), hb[2][0]); SB();
SETM0(wv2k + 65536);
hb[1][2] = *(AS3 u32x4*)(uintptr_t)(hr_ad + 17408);
SB(); M16ZA(P[1][3], AF(1), hb[3][0]); SB();
GLDS(lvoffA, lbase, 0);
SB(); M16A(P[0][0], AF(2), hb[0][1]); SB();
afl[1] = TR(lbB1, 8192);
hb[2][2] = *(AS3 u32x4*)(uintptr_t)(hr_ad + 18432);
SB(); M16A(P[0][1], AF(2), hb[1][1]); SB();
afh[1] = TR(lbB1, 10240);
hb[3][2] = *(AS3 u32x4*)(uintptr_t)(hr_ad + 19456);
SB(); M16A(P[0][2], AF(2), hb[2][1]); SB();
hb[0][3] = *(AS3 u32x4*)(uintptr_t)(hr_ad + 24576);
SB(); M16A(P[0][3], AF(2), hb[3][1]); SB();
hb[1][3] = *(AS3 u32x4*)(uintptr_t)(hr_ad + 25600);
SB(); M16A(P[1][0], AF(3), hb[0][1]); SB();
afl[2] = TR(lbB0, 12288);
hb[2][3] = *(AS3 u32x4*)(uintptr_t)(hr_ad + 26624);
SB(); M16A(P[1][1], AF(3), hb[1][1]); SB();
afh[2] = TR(lbB0, 14336);
hb[3][3] = *(AS3 u32x4*)(uintptr_t)(hr_ad + 27648);
SB(); M16A(P[1][2], AF(3), hb[2][1]); SB();
GLDS(lvoffB, lbase, 1024);
SB(); M16A(P[1][3], AF(3), hb[3][1]); SB();
SB();
asm volatile("s_waitcnt vmcnt(17)\n\ts_barrier" ::: "memory");
SB(); M16A(P[0][0], AF(0), hb[0][2]); SB();
afl[3] = TR(lbB1, 12288);
SB(); M16A(P[0][1], AF(0), hb[1][2]); SB();
afh[3] = TR(lbB1, 14336);
SB(); M16A(P[0][2], AF(0), hb[2][2]); SB();
lbase = wdb + 295040;
SB(); M16A(P[0][3], AF(0), hb[3][2]); SB();
hb[0][4] = *(AS3 u32x4*)(uintptr_t)(hr_ad + 32768);
SB(); M16A(P[1][0], AF(1), hb[0][2]); SB();
afl[0] = TR(lbB0, 16384);
SB(); M16A(P[1][1], AF(1), hb[1][2]); SB();
afh[0] = TR(lbB0, 18432);
SB(); M16A(P[1][2], AF(1), hb[2][2]); SB();
SETM0(wv2k + 0);
hb[1][4] = *(AS3 u32x4*)(uintptr_t)(hr_ad + 33792);
SB(); M16A(P[1][3], AF(1), hb[3][2]); SB();
GLDS(lvoffA, lbase, 0);
SB(); M16A(P[0][0], AF(2), hb[0][3]); SB();
afl[1] = TR(lbB1, 16384);
hb[2][4] = *(AS3 u32x4*)(uintptr_t)(hr_ad + 34816);
SB(); M16A(P[0][1], AF(2), hb[1][3]); SB();
afh[1] = TR(lbB1, 18432);
hb[3][4] = *(AS3 u32x4*)(uintptr_t)(hr_ad + 35840);
SB(); M16A(P[0][2], AF(2), hb[2][3]); SB();
hb[0][5] = *(AS3 u32x4*)(uintptr_t)(hr_ad + 40960);
SB(); M16A(P[0][3], AF(2), hb[3][3]); SB();
hb[1][5] = *(AS3 u32x4*)(uintptr_t)(hr_ad + 41984);
SB(); M16A(P[1][0], AF(3), hb[0][3]); SB();
afl[2] = TR(lbB0, 20480);
hb[2][5] = *(AS3 u32x4*)(uintptr_t)(hr_ad + 43008);
SB(); M16A(P[1][1], AF(3), hb[1][3]); SB();
afh[2] = TR(lbB0, 22528);
hb[3][5] = *(AS3 u32x4*)(uintptr_t)(hr_ad + 44032);
SB(); M16A(P[1][2], AF(3), hb[2][3]); SB();
GLDS(lvoffB, lbase, 1024);
SB(); M16A(P[1][3], AF(3), hb[3][3]); SB();
SB();
asm volatile("s_waitcnt vmcnt(4)\n\ts_barrier" : "+v"(pw0) :: "memory");
SIGNAL_SET(fHme, 5, nw == 1);
CHECK_GE(pw0, fHpa, 3);
SB(); M16A(P[0][0], AF(0), hb[0][4]); SB();
afl[3] = TR(lbB1, 20480);
SB(); M16A(P[0][1], AF(0), hb[1][4]); SB();
afh[3] = TR(lbB1, 22528);
SB(); M16A(P[0][2], AF(0), hb[2][4]); SB();
lbase = wdb + 368640;
SB(); M16A(P[0][3], AF(0), hb[3][4]); SB();
hb[0][6] = *(AS3 u32x4*)(uintptr_t)(hr_ad + 49152);
asm volatile("s_mov_b32 m0, %0\n\ts_nop 0" :: "s"(wv4k + 73728) : "memory"); GLDS1(pvo, hxr4 + 81920, 0); GLDS1(pvo, hxr4 + 81920, 1024); GLDS1(pvo, hxr4 + 81920, 2048); GLDS1(pvo, hxr4 + 81920, 3072);
SB(); M16A(P[1][0], AF(1), hb[0][4]); SB();
afl[0] = TR(lbB0, 24576);
SB(); M16A(P[1][1], AF(1), hb[1][4]); SB();
afh[0] = TR(lbB0, 26624);
SB(); M16A(P[1][2], AF(1), hb[2][4]); SB();
SETM0(wv2k + 8192);
hb[1][6] = *(AS3 u32x4*)(uintptr_t)(hr_ad + 50176);
SB(); M16A(P[1][3], AF(1), hb[3][4]); SB();
GLDS(lvoffA, lbase, 0);
SB(); M16A(P[0][0], AF(2), hb[0][5]); SB();
afl[1] = TR(lbB1, 24576);
hb[2][6] = *(AS3 u32x4*)(uintptr_t)(hr_ad + 51200);
SB(); M16A(P[0][1], AF(2), hb[1][5]); SB();
afh[1] = TR(lbB1, 26624);
hb[3][6] = *(AS3 u32x4*)(uintptr_t)(hr_ad + 52224);
SB(); M16A(P[0][2], AF(2), hb[2][5]); SB();
hb[0][7] = *(AS3 u32x4*)(uintptr_t)(hr_ad + 57344);
SB(); M16A(P[0][3], AF(2), hb[3][5]); SB();
hb[1][7] = *(AS3 u32x4*)(uintptr_t)(hr_ad + 58368);
SB(); M16A(P[1][0], AF(3), hb[0][5]); SB();
afl[2] = TR(lbB0, 28672);
hb[2][7] = *(AS3 u32x4*)(uintptr_t)(hr_ad + 59392);
SB(); M16A(P[1][1], AF(3), hb[1][5]); SB();
afh[2] = TR(lbB0, 30720);
hb[3][7] = *(AS3 u32x4*)(uintptr_t)(hr_ad + 60416);
SB(); M16A(P[1][2], AF(3), hb[2][5]); SB();
GLDS(lvoffB, lbase, 1024);
SB(); M16A(P[1][3], AF(3), hb[3][5]); SB();
SB();
asm volatile("s_waitcnt vmcnt(20)\n\ts_barrier" ::: "memory");
SB(); M16A(P[0][0], AF(0), hb[0][6]); SB();
afl[3] = TR(lbB1, 28672);
SB(); M16A(P[0][1], AF(0), hb[1][6]); SB();
afh[3] = TR(lbB1, 30720);
SB(); M16A(P[0][2], AF(0), hb[2][6]); SB();
lbase = wdb + 442368;
SB(); M16A(P[0][3], AF(0), hb[3][6]); SB();
hb[0][8] = *(AS3 u32x4*)(uintptr_t)(hr_ad + 65536);
asm volatile("s_mov_b32 m0, %0\n\ts_nop 0" :: "s"(wv4k + 90112) : "memory"); GLDS1(pvo, hxr4 + 98304, 0); GLDS1(pvo, hxr4 + 98304, 1024); GLDS1(pvo, hxr4 + 98304, 2048); GLDS1(pvo, hxr4 + 98304, 3072);
SB(); M16A(P[1][0], AF(1), hb[0][6]); SB();
afl[0] = TR(lbB0, 32768);
SB(); M16A(P[1][1], AF(1), hb[1][6]); SB();
afh[0] = TR(lbB0, 34816);
SB(); M16A(P[1][2], AF(1), hb[2][6]); SB();
SETM0(wv2k + 16384);
hb[1][8] = *(AS3 u32x4*)(uintptr_t)(hr_ad + 66560);
SB(); M16A(P[1][3], AF(1), hb[3][6]); SB();
GLDS(lvoffA, lbase, 0);
SB(); M16A(P[0][0], AF(2), hb[0][7]); SB();
afl[1] = TR(lbB1, 32768);
hb[2][8] = *(AS3 u32x4*)(uintptr_t)(hr_ad + 67584);
SB(); M16A(P[0][1], AF(2), hb[1][7]); SB();
afh[1] = TR(lbB1, 34816);
hb[3][8] = *(AS3 u32x4*)(uintptr_t)(hr_ad + 68608);
SB(); M16A(P[0][2], AF(2), hb[2][7]); SB();
hb[0][9] = *(AS3 u32x4*)(uintptr_t)(hr_ad + 73728);
SB(); M16A(P[0][3], AF(2), hb[3][7]); SB();
hb[1][9] = *(AS3 u32x4*)(uintptr_t)(hr_ad + 74752);
SB(); M16A(P[1][0], AF(3), hb[0][7]); SB();
afl[2] = TR(lbB0, 36864);
hb[2][9] = *(AS3 u32x4*)(uintptr_t)(hr_ad + 75776);
SB(); M16A(P[1][1], AF(3), hb[1][7]); SB();
afh[2] = TR(lbB0, 38912);
hb[3][9] = *(AS3 u32x4*)(uintptr_t)(hr_ad + 76800);
SB(); M16A(P[1][2], AF(3), hb[2][7]); SB();
GLDS(lvoffB, lbase, 1024);
SB(); M16A(P[1][3], AF(3), hb[3][7]); SB();
SB();
asm volatile("s_waitcnt vmcnt(23)\n\ts_barrier" ::: "memory");
SB(); M16A(P[0][0], AF(0), hb[0][8]); SB();
afl[3] = TR(lbB1, 36864);
SB(); M16A(P[0][1], AF(0), hb[1][8]); SB();
afh[3] = TR(lbB1, 38912);
SB(); M16A(P[0][2], AF(0), hb[2][8]); SB();
lbase = wdb + 516096;
SB(); M16A(P[0][3], AF(0), hb[3][8]); SB();
asm volatile("s_mov_b32 m0, %0\n\ts_nop 0" :: "s"(wv4k + 106496) : "memory"); GLDS1(pvo, hxr4 + 114688, 0); GLDS1(pvo, hxr4 + 114688, 1024); GLDS1(pvo, hxr4 + 114688, 2048); GLDS1(pvo, hxr4 + 114688, 3072);
SB(); M16A(P[1][0], AF(1), hb[0][8]); SB();
afl[0] = TR(lbB0, 40960);
SB(); M16A(P[1][1], AF(1), hb[1][8]); SB();
afh[0] = TR(lbB0, 43008);
SB(); M16A(P[1][2], AF(1), hb[2][8]); SB();
SETM0(wv2k + 24576);
SB(); M16A(P[1][3], AF(1), hb[3][8]); SB();
GLDS(lvoffA, lbase, 0);
SB(); M16A(P[0][0], AF(2), hb[0][9]); SB();
afl[1] = TR(lbB1, 40960);
SB(); M16A(P[0][1], AF(2), hb[1][9]); SB();
afh[1] = TR(lbB1, 43008);
SB(); M16A(P[0][2], AF(2), hb[2][9]); SB();
SB(); M16A(P[0][3], AF(2), hb[3][9]); SB();
SB(); M16A(P[1][0], AF(3), hb[0][9]); SB();
afl[2] = TR(lbB0, 45056);
SB(); M16A(P[1][1], AF(3), hb[1][9]); SB();
afh[2] = TR(lbB0, 47104);
SB(); M16A(P[1][2], AF(3), hb[2][9]); SB();
GLDS(lvoffB, lbase, 1024);
SB(); M16A(P[1][3], AF(3), hb[3][9]); SB();
SB();
asm volatile("s_waitcnt vmcnt(26)\n\ts_barrier" ::: "memory");
SB(); M16ZA(R[0][0], AF(0), hb[0][0]); SB();
afl[3] = TR(lbB1, 45056);
SB(); M16ZA(R[0][1], AF(0), hb[1][0]); SB();
afh[3] = TR(lbB1, 47104);
SB(); M16ZA(R[0][2], AF(0), hb[2][0]); SB();
lbase = wdb + 368768;
SB(); M16ZA(R[0][3], AF(0), hb[3][0]); SB();
SB(); M16ZA(R[1][0], AF(1), hb[0][0]); SB();
afl[0] = TR(lbB0, 49152);
{ unsigned u = pk_bf16(X[0][0], X[0][1]); PIN(u); hh[0] = u; }
SB(); M16ZA(R[1][1], AF(1), hb[1][0]); SB();
afh[0] = TR(lbB0, 51200);
{ unsigned u = pk_bf16(X[0][2], X[0][3]); PIN(u); hh[1] = u; }
SB(); M16ZA(R[1][2], AF(1), hb[2][0]); SB();
SETM0(wv2k + 32768);
{ unsigned u = pk_bf16(X[0][8], X[0][9]); PIN(u); hh[2] = u; }
{ unsigned u = pk_bf16(X[0][10], X[0][11]); PIN(u); hh[3] = u; }
SB(); M16ZA(R[1][3], AF(1), hb[3][0]); SB();
GLDS(lvoffA, lbase, 0);
SB(); M16A(R[0][0], AF(2), hb[0][1]); SB();
afl[1] = TR(lbB1, 49152);
SB(); M16A(R[0][1], AF(2), hb[1][1]); SB();
afh[1] = TR(lbB1, 51200);
SB(); M16A(R[0][2], AF(2), hb[2][1]); SB();
ST41(svo, hh, sbase0 + (wp0) * 18432 + 0);
SB(); M16A(R[0][3], AF(2), hb[3][1]); SB();
{ unsigned u = pk_bf16(X[0][4], X[0][5]); PIN(u); hh[0] = u; }
{ unsigned u = pk_bf16(X[0][6], X[0][7]); PIN(u); hh[1] = u; }
{ unsigned u = pk_bf16(X[0][12], X[0][13]); PIN(u); hh[2] = u; }
SB(); M16A(R[1][0], AF(3), hb[0][1]); SB();
afl[2] = TR(lbB0, 53248);
{ unsigned u = pk_bf16(X[0][14], X[0][15]); PIN(u); hh[3] = u; }
SB(); M16A(R[1][1], AF(3), hb[1][1]); SB();
afh[2] = TR(lbB0, 55296);
SB(); M16A(R[1][2], AF(3), hb[2][1]); SB();
GLDS(lvoffB, lbase, 1024);
SB(); M16A(R[1][3], AF(3), hb[3][1]); SB();
SB();
asm volatile("s_waitcnt vmcnt(26)\n\ts_barrier" ::: "memory");
SB(); M16A(R[0][0], AF(0), hb[0][2]); SB();
afl[3] = TR(lbB1, 53248);
SB(); M16A(R[0][1], AF(0), hb[1][2]); SB();
afh[3] = TR(lbB1, 55296);
SB(); M16A(R[0][2], AF(0), hb[2][2]); SB();
lbase = wdb + 442496;
SB(); M16A(R[0][3], AF(0), hb[3][2]); SB();
ST41(svo, hh, sbase0 + (wp0) * 18432 + 512);
SB(); M16A(R[1][0], AF(1), hb[0][2]); SB();
afl[0] = TR(lbB0, 57344);
{ unsigned u = pk_bf16(X[1][0], X[1][1]); PIN(u); hh[0] = u; }
SB(); M16A(R[1][1], AF(1), hb[1][2]); SB();
afh[0] = TR(lbB0, 59392);
{ unsigned u = pk_bf16(X[1][2], X[1][3]); PIN(u); hh[1] = u; }
SB(); M16A(R[1][2], AF(1), hb[2][2]); SB();
SETM0(wv2k + 40960);
{ unsigned u = pk_bf16(X[1][8], X[1][9]); PIN(u); hh[2] = u; }
{ unsigned u = pk_bf16(X[1][10], X[1][11]); PIN(u); hh[3] = u; }
SB(); M16A(R[1][3], AF(1), hb[3][2]); SB();
GLDS(lvoffA, lbase, 0);
SB(); M16A(R[0][0], AF(2), hb[0][3]); SB();
afl[1] = TR(lbB1, 57344);
SB(); M16A(R[0][1], AF(2), hb[1][3]); SB();
afh[1] = TR(lbB1, 59392);
SB(); M16A(R[0][2], AF(2), hb[2][3]); SB();
ST41(svo, hh, sbase0 + (wp0) * 18432 + 9216);
SB(); M16A(R[0][3], AF(2), hb[3][3]); SB();
{ unsigned u = pk_bf16(X[1][4], X[1][5]); PIN(u); hh[0] = u; }
{ unsigned u = pk_bf16(X[1][6], X[1][7]); PIN(u); hh[1] = u; }
{ unsigned u = pk_bf16(X[1][12], X[1][13]); PIN(u); hh[2] = u; }
SB(); M16A(R[1][0], AF(3), hb[0][3]); SB();
afl[2] = TR(lbB0, 61440);
{ unsigned u = pk_bf16(X[1][14], X[1][15]); PIN(u); hh[3] = u; }
SB(); M16A(R[1][1], AF(3), hb[1][3]); SB();
afh[2] = TR(lbB0, 63488);
SB(); M16A(R[1][2], AF(3), hb[2][3]); SB();
GLDS(lvoffB, lbase, 1024);
SB(); M16A(R[1][3], AF(3), hb[3][3]); SB();
SB();
asm volatile("s_waitcnt vmcnt(27)\n\ts_barrier" ::: "memory");
SB(); M16A(R[0][0], AF(0), hb[0][4]); SB();
afl[3] = TR(lbB1, 61440);
SB(); M16A(R[0][1], AF(0), hb[1][4]); SB();
afh[3] = TR(lbB1, 63488);
SB(); M16A(R[0][2], AF(0), hb[2][4]); SB();
lbase = wdb + 516224;
SB(); M16A(R[0][3], AF(0), hb[3][4]); SB();
ST41(svo, hh, sbase0 + (wp0) * 18432 + 9728);
SB(); M16A(R[1][0], AF(1), hb[0][4]); SB();
afl[0] = TR(lbB0, 65536);
SB(); M16A(R[1][1], AF(1), hb[1][4]); SB();
afh[0] = TR(lbB0, 67584);
SB(); M16A(R[1][2], AF(1), hb[2][4]); SB();
SETM0(wv2k + 49152);
SB(); M16A(R[1][3], AF(1), hb[3][4]); SB();
GLDS(lvoffA, lbase, 0);
SB(); M16A(R[0][0], AF(2), hb[0][5]); SB();
afl[1] = TR(lbB1, 65536);
SB(); M16A(R[0][1], AF(2), hb[1][5]); SB();
afh[1] = TR(lbB1, 67584);
SB(); M16A(R[0][2], AF(2), hb[2][5]); SB();
SB(); M16A(R[0][3], AF(2), hb[3][5]); SB();
SB(); M16A(R[1][0], AF(3), hb[0][5]); SB();
afl[2] = TR(lbB0, 69632);
SB(); M16A(R[1][1], AF(3), hb[1][5]); SB();
afh[2] = TR(lbB0, 71680);
SB(); M16A(R[1][2], AF(3), hb[2][5]); SB();
GLDS(lvoffB, lbase, 1024);
SB(); M16A(R[1][3], AF(3), hb[3][5]); SB();
SB();
asm volatile("s_waitcnt vmcnt(28)\n\ts_barrier" ::: "memory");
SB(); M16A(R[0][0], AF(0), hb[0][6]); SB();
afl[3] = TR(lbB1, 69632);
SB(); M16A(R[0][1], AF(0), hb[1][6]); SB();
afh[3] = TR(lbB1, 71680);
SB(); M16A(R[0][2], AF(0), hb[2][6]); SB();
lbase = wdb + 589824;
SB(); M16A(R[0][3], AF(0), hb[3][6]); SB();
SB(); M16A(R[1][0], AF(1), hb[0][6]); SB();
afl[0] = TR(lbB0, 0);
SB(); M16A(R[1][1], AF(1), hb[1][6]); SB();
afh[0] = TR(lbB0, 2048);
SB(); M16A(R[1][2], AF(1), hb[2][6]); SB();
SETM0(wv2k + 57344);
SB(); M16A(R[1][3], AF(1), hb[3][6]); SB();
GLDS(lvoffA, lbase, 0);
SB(); M16A(R[0][0], AF(2), hb[0][7]); SB();
afl[1] = TR(lbB1, 0);
SB(); M16A(R[0][1], AF(2), hb[1][7]); SB();
afh[1] = TR(lbB1, 2048);
SB(); M16A(R[0][2], AF(2), hb[2][7]); SB();
SB(); M16A(R[0][3], AF(2), hb[3][7]); SB();
SB(); M16A(R[1][0], AF(3), hb[0][7]); SB();
afl[2] = TR(lbB0, 4096);
SB(); M16A(R[1][1], AF(3), hb[1][7]); SB();
afh[2] = TR(lbB0, 6144);
SB(); M16A(R[1][2], AF(3), hb[2][7]); SB();
GLDS(lvoffB, lbase, 1024);
SB(); M16A(R[1][3], AF(3), hb[3][7]); SB();
SB();
asm volatile("s_waitcnt vmcnt(24)\n\ts_barrier" ::: "memory");
SB(); M16A(R[0][0], AF(0), hb[0][8]); SB();
afl[3] = TR(lbB1, 4096);
SB(); M16A(R[0][1], AF(0), hb[1][8]); SB();
afh[3] = TR(lbB1, 6144);
SB(); M16A(R[0][2], AF(0), hb[2][8]); SB();
lbase = wdb + 589952;
LDW1(pw1, zvo, fHpa);
SB(); M16A(R[0][3], AF(0), hb[3][8]); SB();
hb[0][10] = *(AS3 u32x4*)(uintptr_t)(hr_ad + 0);
SB(); M16A(R[1][0], AF(1), hb[0][8]); SB();
afl[0] = TR(lbB0, 8192);
SB(); M16A(R[1][1], AF(1), hb[1][8]); SB();
afh[0] = TR(lbB0, 10240);
SB(); M16A(R[1][2], AF(1), hb[2][8]); SB();
SETM0(wv2k + 65536);
hb[1][10] = *(AS3 u32x4*)(uintptr_t)(hr_ad + 1024);
SB(); M16A(R[1][3], AF(1), hb[3][8]); SB();
GLDS(lvoffA, lbase, 0);
SB(); M16A(R[0][0], AF(2), hb[0][9]); SB();
afl[1] = TR(lbB1, 8192);
hb[2][10] = *(AS3 u32x4*)(uintptr_t)(hr_ad + 2048);
SB(); M16A(R[0][1], AF(2), hb[1][9]); SB();
afh[1] = TR(lbB1, 10240);
hb[3][10] = *(AS3 u32x4*)(uintptr_t)(hr_ad + 3072);
SB(); M16A(R[0][2], AF(2), hb[2][9]); SB();
hb[0][11] = *(AS3 u32x4*)(uintptr_t)(hr_ad + 8192);
SB(); M16A(R[0][3], AF(2), hb[3][9]); SB();
hb[1][11] = *(AS3 u32x4*)(uintptr_t)(hr_ad + 9216);
SB(); M16A(R[1][0], AF(3), hb[0][9]); SB();
afl[2] = TR(lbB0, 12288);
hb[2][11] = *(AS3 u32x4*)(uintptr_t)(hr_ad + 10240);
SB(); M16A(R[1][1], AF(3), hb[1][9]); SB();
afh[2] = TR(lbB0, 14336);
hb[3][11] = *(AS3 u32x4*)(uintptr_t)(hr_ad + 11264);
SB(); M16A(R[1][2], AF(3), hb[2][9]); SB();
GLDS(lvoffB, lbase, 1024);
SB(); M16A(R[1][3], AF(3), hb[3][9]); SB();
SB();
asm volatile("s_waitcnt vmcnt(7)\n\ts_barrier" ::: "memory");
SIGNAL_ADD(fPw, 1, true);
SB(); M16A(P[0][0], AF(0), hb[0][10]); SB();
afl[3] = TR(lbB1, 12288);
SB(); M16A(P[0][1], AF(0), hb[1][10]); SB();
afh[3] = TR(lbB1, 14336);
SB(); M16A(P[0][2], AF(0), hb[2][10]); SB();
lbase = wdb + 256;
SB(); M16A(P[0][3], AF(0), hb[3][10]); SB();
hb[0][12] = *(AS3 u32x4*)(uintptr_t)(hr_ad + 16384);
SB(); M16A(P[1][0], AF(1), hb[0][10]); SB();
afl[0] = TR(lbB0, 16384);
SB(); M16A(P[1][1], AF(1), hb[1][10]); SB();
afh[0] = TR(lbB0, 18432);
SB(); M16A(P[1][2], AF(1), hb[2][10]); SB();
SETM0(wv2k + 0);
hb[1][12] = *(AS3 u32x4*)(uintptr_t)(hr_ad + 17408);
SB(); M16A(P[1][3], AF(1), hb[3][10]); SB();
GLDS(lvoffA, lbase, 0);
SB(); M16A(P[0][0], AF(2), hb[0][11]); SB();
afl[1] = TR(lbB1, 16384);
hb[2][12] = *(AS3 u32x4*)(uintptr_t)(hr_ad + 18432);
SB(); M16A(P[0][1], AF(2), hb[1][11]); SB();
afh[1] = TR(lbB1, 18432);
hb[3][12] = *(AS3 u32x4*)(uintptr_t)(hr_ad + 19456);
SB(); M16A(P[0][2], AF(2), hb[2][11]); SB();
hb[0][13] = *(AS3 u32x4*)(uintptr_t)(hr_ad + 24576);
SB(); M16A(P[0][3], AF(2), hb[3][11]); SB();
hb[1][13] = *(AS3 u32x4*)(uintptr_t)(hr_ad + 25600);
SB(); M16A(P[1][0], AF(3), hb[0][11]); SB();
afl[2] = TR(lbB0, 20480);
hb[2][13] = *(AS3 u32x4*)(uintptr_t)(hr_ad + 26624);
SB(); M16A(P[1][1], AF(3), hb[1][11]); SB();
afh[2] = TR(lbB0, 22528);
hb[3][13] = *(AS3 u32x4*)(uintptr_t)(hr_ad + 27648);
SB(); M16A(P[1][2], AF(3), hb[2][11]); SB();
GLDS(lvoffB, lbase, 1024);
SB(); M16A(P[1][3], AF(3), hb[3][11]); SB();
SB();
asm volatile("s_waitcnt vmcnt(4)\n\ts_barrier" : "+v"(pw1) :: "memory");
CHECK_GE(pw1, fHpa, 4);
SB(); M16A(P[0][0], AF(0), hb[0][12]); SB();
afl[3] = TR(lbB1, 20480);
SB(); M16A(P[0][1], AF(0), hb[1][12]); SB();
afh[3] = TR(lbB1, 22528);
SB(); M16A(P[0][2], AF(0), hb[2][12]); SB();
lbase = wdb + 73984;
SB(); M16A(P[0][3], AF(0), hb[3][12]); SB();
hb[0][14] = *(AS3 u32x4*)(uintptr_t)(hr_ad + 32768);
asm volatile("s_mov_b32 m0, %0\n\ts_nop 0" :: "s"(wv4k + 122880) : "memory"); GLDS1(pvo, hxr4 + 131072, 0); GLDS1(pvo, hxr4 + 131072, 1024); GLDS1(pvo, hxr4 + 131072, 2048); GLDS1(pvo, hxr4 + 131072, 3072);
SB(); M16A(P[1][0], AF(1), hb[0][12]); SB();
afl[0] = TR(lbB0, 24576);
SB(); M16A(P[1][1], AF(1), hb[1][12]); SB();
afh[0] = TR(lbB0, 26624);
SB(); M16A(P[1][2], AF(1), hb[2][12]); SB();
SETM0(wv2k + 8192);
hb[1][14] = *(AS3 u32x4*)(uintptr_t)(hr_ad + 33792);
SB(); M16A(P[1][3], AF(1), hb[3][12]); SB();
GLDS(lvoffA, lbase, 0);
SB(); M16A(P[0][0], AF(2), hb[0][13]); SB();
afl[1] = TR(lbB1, 24576);
hb[2][14] = *(AS3 u32x4*)(uintptr_t)(hr_ad + 34816);
SB(); M16A(P[0][1], AF(2), hb[1][13]); SB();
afh[1] = TR(lbB1, 26624);
hb[3][14] = *(AS3 u32x4*)(uintptr_t)(hr_ad + 35840);
SB(); M16A(P[0][2], AF(2), hb[2][13]); SB();
hb[0][15] = *(AS3 u32x4*)(uintptr_t)(hr_ad + 40960);
SB(); M16A(P[0][3], AF(2), hb[3][13]); SB();
hb[1][15] = *(AS3 u32x4*)(uintptr_t)(hr_ad + 41984);
SB(); M16A(P[1][0], AF(3), hb[0][13]); SB();
afl[2] = TR(lbB0, 28672);
hb[2][15] = *(AS3 u32x4*)(uintptr_t)(hr_ad + 43008);
SB(); M16A(P[1][1], AF(3), hb[1][13]); SB();
afh[2] = TR(lbB0, 30720);
hb[3][15] = *(AS3 u32x4*)(uintptr_t)(hr_ad + 44032);
SB(); M16A(P[1][2], AF(3), hb[2][13]); SB();
GLDS(lvoffB, lbase, 1024);
SB(); M16A(P[1][3], AF(3), hb[3][13]); SB();
SB();
asm volatile("s_waitcnt vmcnt(20)\n\ts_barrier" ::: "memory");
SB(); M16A(P[0][0], AF(0), hb[0][14]); SB();
afl[3] = TR(lbB1, 28672);
SB(); M16A(P[0][1], AF(0), hb[1][14]); SB();
afh[3] = TR(lbB1, 30720);
SB(); M16A(P[0][2], AF(0), hb[2][14]); SB();
lbase = wdb + 147712;
SB(); M16A(P[0][3], AF(0), hb[3][14]); SB();
SB(); M16A(P[1][0], AF(1), hb[0][14]); SB();
afl[0] = TR(lbB0, 32768);
SB(); M16A(P[1][1], AF(1), hb[1][14]); SB();
afh[0] = TR(lbB0, 34816);
SB(); M16A(P[1][2], AF(1), hb[2][14]); SB();
SETM0(wv2k + 16384);
SB(); M16A(P[1][3], AF(1), hb[3][14]); SB();
GLDS(lvoffA, lbase, 0);
SB(); M16V(P[0][0], AF(2), hb[0][15]); SB();
afl[1] = TR(lbB1, 32768);
SB(); M16V(P[0][1], AF(2), hb[1][15]); SB();
afh[1] = TR(lbB1, 34816);
SB(); M16V(P[0][2], AF(2), hb[2][15]); SB();
SB(); M16V(P[0][3], AF(2), hb[3][15]); SB();
SB(); M16V(P[1][0], AF(3), hb[0][15]); SB();
afl[2] = TR(lbB0, 36864);
SB(); M16V(P[1][1], AF(3), hb[1][15]); SB();
afh[2] = TR(lbB0, 38912);
SB(); M16V(P[1][2], AF(3), hb[2][15]); SB();
GLDS(lvoffB, lbase, 1024);
SB(); M16V(P[1][3], AF(3), hb[3][15]); SB();
SB();
asm volatile("s_waitcnt vmcnt(18)\n\ts_barrier" ::: "memory");
SB(); M16A(R[0][0], AF(0), hb[0][10]); SB();
afl[3] = TR(lbB1, 36864);
SB(); M16A(R[0][1], AF(0), hb[1][10]); SB();
afh[3] = TR(lbB1, 38912);
SB(); M16A(R[0][2], AF(0), hb[2][10]); SB();
lbase = wdb + 221440;
SB(); M16A(R[0][3], AF(0), hb[3][10]); SB();
SB(); M16A(R[1][0], AF(1), hb[0][10]); SB();
afl[0] = TR(lbB0, 40960);
SB(); M16A(R[1][1], AF(1), hb[1][10]); SB();
afh[0] = TR(lbB0, 43008);
SB(); M16A(R[1][2], AF(1), hb[2][10]); SB();
SETM0(wv2k + 24576);
SB(); M16A(R[1][3], AF(1), hb[3][10]); SB();
GLDS(lvoffA, lbase, 0);
SB(); M16A(R[0][0], AF(2), hb[0][11]); SB();
afl[1] = TR(lbB1, 40960);
SB(); M16A(R[0][1], AF(2), hb[1][11]); SB();
afh[1] = TR(lbB1, 43008);
SB(); M16A(R[0][2], AF(2), hb[2][11]); SB();
SB(); M16A(R[0][3], AF(2), hb[3][11]); SB();
SB(); M16A(R[1][0], AF(3), hb[0][11]); SB();
afl[2] = TR(lbB0, 45056);
SB(); M16A(R[1][1], AF(3), hb[1][11]); SB();
afh[2] = TR(lbB0, 47104);
SB(); M16A(R[1][2], AF(3), hb[2][11]); SB();
GLDS(lvoffB, lbase, 1024);
SB(); M16A(R[1][3], AF(3), hb[3][11]); SB();
SB();
asm volatile("s_waitcnt vmcnt(17)\n\ts_barrier" ::: "memory");
SB(); M16A(R[0][0], AF(0), hb[0][12]); SB();
afl[3] = TR(lbB1, 45056);
SB(); M16A(R[0][1], AF(0), hb[1][12]); SB();
afh[3] = TR(lbB1, 47104);
SB(); M16A(R[0][2], AF(0), hb[2][12]); SB();
lbase = wdb + 295168;
SB(); M16A(R[0][3], AF(0), hb[3][12]); SB();
SB(); M16A(R[1][0], AF(1), hb[0][12]); SB();
afl[0] = TR(lbB0, 49152);
SB(); M16A(R[1][1], AF(1), hb[1][12]); SB();
afh[0] = TR(lbB0, 51200);
SB(); M16A(R[1][2], AF(1), hb[2][12]); SB();
SETM0(wv2k + 32768);
SB(); M16A(R[1][3], AF(1), hb[3][12]); SB();
GLDS(lvoffA, lbase, 0);
SB(); M16A(R[0][0], AF(2), hb[0][13]); SB();
afl[1] = TR(lbB1, 49152);
SB(); M16A(R[0][1], AF(2), hb[1][13]); SB();
afh[1] = TR(lbB1, 51200);
SB(); M16A(R[0][2], AF(2), hb[2][13]); SB();
SB(); M16A(R[0][3], AF(2), hb[3][13]); SB();
SB(); M16A(R[1][0], AF(3), hb[0][13]); SB();
afl[2] = TR(lbB0, 53248);
SB(); M16A(R[1][1], AF(3), hb[1][13]); SB();
afh[2] = TR(lbB0, 55296);
SB(); M16A(R[1][2], AF(3), hb[2][13]); SB();
GLDS(lvoffB, lbase, 1024);
SB(); M16A(R[1][3], AF(3), hb[3][13]); SB();
SB();
asm volatile("s_waitcnt vmcnt(8)\n\ts_barrier" ::: "memory");
SB(); M16A(R[0][0], AF(0), hb[0][14]); SB();
afl[3] = TR(lbB1, 53248);
SB(); M16A(R[0][1], AF(0), hb[1][14]); SB();
afh[3] = TR(lbB1, 55296);
SB(); M16A(R[0][2], AF(0), hb[2][14]); SB();
lbase = wdb + 368896;
SB(); M16A(R[0][3], AF(0), hb[3][14]); SB();
hb[0][16] = *(AS3 u32x4*)(uintptr_t)(hr_ad + 49152);
SB(); M16A(R[1][0], AF(1), hb[0][14]); SB();
afl[0] = TR(lbB0, 57344);
SB(); M16A(R[1][1], AF(1), hb[1][14]); SB();
afh[0] = TR(lbB0, 59392);
SB(); M16A(R[1][2], AF(1), hb[2][14]); SB();
SETM0(wv2k + 40960);
hb[1][16] = *(AS3 u32x4*)(uintptr_t)(hr_ad + 50176);
SB(); M16A(R[1][3], AF(1), hb[3][14]); SB();
GLDS(lvoffA, lbase, 0);
SB(); M16V(R[0][0], AF(2), hb[0][15]); SB();
afl[1] = TR(lbB1, 57344);
hb[2][16] = *(AS3 u32x4*)(uintptr_t)(hr_ad + 51200);
SB(); M16V(R[0][1], AF(2), hb[1][15]); SB();
afh[1] = TR(lbB1, 59392);
hb[3][16] = *(AS3 u32x4*)(uintptr_t)(hr_ad + 52224);
SB(); M16V(R[0][2], AF(2), hb[2][15]); SB();
hb[0][17] = *(AS3 u32x4*)(uintptr_t)(hr_ad + 57344);
SB(); M16V(R[0][3], AF(2), hb[3][15]); SB();
hb[1][17] = *(AS3 u32x4*)(uintptr_t)(hr_ad + 58368);
SB(); M16V(R[1][0], AF(3), hb[0][15]); SB();
afl[2] = TR(lbB0, 61440);
hb[2][17] = *(AS3 u32x4*)(uintptr_t)(hr_ad + 59392);
SB(); M16V(R[1][1], AF(3), hb[1][15]); SB();
afh[2] = TR(lbB0, 63488);
hb[3][17] = *(AS3 u32x4*)(uintptr_t)(hr_ad + 60416);
SB(); M16V(R[1][2], AF(3), hb[2][15]); SB();
GLDS(lvoffB, lbase, 1024);
SB(); M16V(R[1][3], AF(3), hb[3][15]); SB();
SB();
asm volatile("s_waitcnt vmcnt(16)\n\ts_barrier" ::: "memory");
SB(); M16V(P[0][0], AF(0), hb[0][16]); SB();
afl[3] = TR(lbB1, 61440);
SB(); M16V(P[0][1], AF(0), hb[1][16]); SB();
afh[3] = TR(lbB1, 63488);
SB(); M16V(P[0][2], AF(0), hb[2][16]); SB();
lbase = wdb + 442624;
SB(); M16V(P[0][3], AF(0), hb[3][16]); SB();
SB(); M16V(P[1][0], AF(1), hb[0][16]); SB();
afl[0] = TR(lbB0, 65536);
SB(); M16V(P[1][1], AF(1), hb[1][16]); SB();
afh[0] = TR(lbB0, 67584);
SB(); M16V(P[1][2], AF(1), hb[2][16]); SB();
SETM0(wv2k + 49152);
SB(); M16V(P[1][3], AF(1), hb[3][16]); SB();
GLDS(lvoffA, lbase, 0);
SB(); M16V(P[0][0], AF(2), hb[0][17]); SB();
afl[1] = TR(lbB1, 65536);
SB(); M16V(P[0][1], AF(2), hb[1][17]); SB();
afh[1] = TR(lbB1, 67584);
SB(); M16V(P[0][2], AF(2), hb[2][17]); SB();
SB(); M16V(P[0][3], AF(2), hb[3][17]); SB();
SB(); M16V(P[1][0], AF(3), hb[0][17]); SB();
afl[2] = TR(lbB0, 69632);
SB(); M16V(P[1][1], AF(3), hb[1][17]); SB();
afh[2] = TR(lbB0, 71680);
SB(); M16V(P[1][2], AF(3), hb[2][17]); SB();
GLDS(lvoffB, lbase, 1024);
SB(); M16V(P[1][3], AF(3), hb[3][17]); SB();
SB();
asm volatile("s_waitcnt vmcnt(16)\n\ts_barrier" ::: "memory");
SB(); M16V(R[0][0], AF(0), hb[0][16]); SB();
afl[3] = TR(lbB1, 69632);
SB(); M16V(R[0][1], AF(0), hb[1][16]); SB();
afh[3] = TR(lbB1, 71680);
SB(); M16V(R[0][2], AF(0), hb[2][16]); SB();
lbase = wdb + 516352;
SB(); M16V(R[0][3], AF(0), hb[3][16]); SB();
SB(); M16V(R[1][0], AF(1), hb[0][16]); SB();
afl[0] = TR(lbB0, 0);
SB(); M16V(R[1][1], AF(1), hb[1][16]); SB();
afh[0] = TR(lbB0, 2048);
SB(); M16V(R[1][2], AF(1), hb[2][16]); SB();
SETM0(wv2k + 57344);
SB(); M16V(R[1][3], AF(1), hb[3][16]); SB();
GLDS(lvoffA, lbase, 0);
SB(); M16V(R[0][0], AF(2), hb[0][17]); SB();
afl[1] = TR(lbB1, 0);
SB(); M16V(R[0][1], AF(2), hb[1][17]); SB();
afh[1] = TR(lbB1, 2048);
SB(); M16V(R[0][2], AF(2), hb[2][17]); SB();
{ unsigned u = pk_bf16(P[0][0][0], P[0][0][1]); PIN(u); hh[0] = u; }
{ unsigned u = pk_bf16(P[0][0][2], P[0][0][3]); PIN(u); hh[1] = u; }
{ unsigned u = pk_bf16(P[1][0][0], P[1][0][1]); PIN(u); hh[2] = u; }
SB(); M16V(R[0][3], AF(2), hb[3][17]); SB();
{ unsigned u = pk_bf16(P[1][0][2], P[1][0][3]); PIN(u); hh[3] = u; }
SB(); M16V(R[1][0], AF(3), hb[0][17]); SB();
afl[2] = TR(lbB0, 4096);
SB(); M16V(R[1][1], AF(3), hb[1][17]); SB();
afh[2] = TR(lbB0, 6144);
SB(); M16V(R[1][2], AF(3), hb[2][17]); SB();
GLDS(lvoffB, lbase, 1024);
SB(); M16V(R[1][3], AF(3), hb[3][17]); SB();
STAMP(9); JIT(9);
SB();
asm volatile("s_waitcnt vmcnt(12)\n\ts_barrier" ::: "memory");
SB(); M16ZA(Q[0][0], AF(0), hb[0][0]); SB();
afl[3] = TR(lbB1, 4096);
SB(); M16ZA(Q[0][1], AF(0), hb[1][0]); SB();
afh[3] = TR(lbB1, 6144);
SB(); M16ZA(Q[0][2], AF(0), hb[2][0]); SB();
lbase = wdb + 590080;
SB(); M16ZA(Q[0][3], AF(0), hb[3][0]); SB();
PST(pvo, hh, pbase0 + 0);
SB(); M16ZA(Q[1][0], AF(1), hb[0][0]); SB();
afl[0] = TR(lbB0, 8192);
{ unsigned u = pk_bf16(P[0][1][0], P[0][1][1]); PIN(u); hh[0] = u; }
SB(); M16ZA(Q[1][1], AF(1), hb[1][0]); SB();
afh[0] = TR(lbB0, 10240);
{ unsigned u = pk_bf16(P[0][1][2], P[0][1][3]); PIN(u); hh[1] = u; }
SB(); M16ZA(Q[1][2], AF(1), hb[2][0]); SB();
SETM0(wv2k + 65536);
{ unsigned u = pk_bf16(P[1][1][0], P[1][1][1]); PIN(u); hh[2] = u; }
{ unsigned u = pk_bf16(P[1][1][2], P[1][1][3]); PIN(u); hh[3] = u; }
SB(); M16ZA(Q[1][3], AF(1), hb[3][0]); SB();
GLDS(lvoffA, lbase, 0);
SB(); M16A(Q[0][0], AF(2), hb[0][1]); SB();
afl[1] = TR(lbB1, 8192);
SB(); M16A(Q[0][1], AF(2), hb[1][1]); SB();
afh[1] = TR(lbB1, 10240);
SB(); M16A(Q[0][2], AF(2), hb[2][1]); SB();
PST(pvo, hh, pbase0 + 165888);
SB(); M16A(Q[0][3], AF(2), hb[3][1]); SB();
{ unsigned u = pk_bf16(P[0][2][0], P[0][2][1]); PIN(u); hh[0] = u; }
{ unsigned u = pk_bf16(P[0][2][2], P[0][2][3]); PIN(u); hh[1] = u; }
{ unsigned u = pk_bf16(P[1][2][0], P[1][2][1]); PIN(u); hh[2] = u; }
SB(); M16A(Q[1][0], AF(3), hb[0][1]); SB();
afl[2] = TR(lbB0, 12288);
{ unsigned u = pk_bf16(P[1][2][2], P[1][2][3]); PIN(u); hh[3] = u; }
SB(); M16A(Q[1][1], AF(3), hb[1][1]); SB();
afh[2] = TR(lbB0, 14336);
SB(); M16A(Q[1][2], AF(3), hb[2][1]); SB();
GLDS(lvoffB, lbase, 1024);
SB(); M16A(Q[1][3], AF(3), hb[3][1]); SB();
SB();
asm volatile("s_waitcnt vmcnt(14)\n\ts_barrier" ::: "memory");
SB(); M16A(Q[0][0], AF(0), hb[0][2]); SB();
afl[3] = TR(lbB1, 12288);
SB(); M16A(Q[0][1], AF(0), hb[1][2]); SB();
afh[3] = TR(lbB1, 14336);
SB(); M16A(Q[0][2], AF(0), hb[2][2]); SB();
lbase = wdb + 384;
SB(); M16A(Q[0][3], AF(0), hb[3][2]); SB();
PST(pvo, hh, pbase0 + 331776);
SB(); M16A(Q[1][0], AF(1), hb[0][2]); SB();
afl[0] = TR(lbB0, 16384);
{ unsigned u = pk_bf16(P[0][3][0], P[0][3][1]); PIN(u); hh[0] = u; }
SB(); M16A(Q[1][1], AF(1), hb[1][2]); SB();
afh[0] = TR(lbB0, 18432);
{ unsigned u = pk_bf16(P[0][3][2], P[0][3][3]); PIN(u); hh[1] = u; }
SB(); M16A(Q[1][2], AF(1), hb[2][2]); SB();
SETM0(wv2k + 0);
{ unsigned u = pk_bf16(P[1][3][0], P[1][3][1]); PIN(u); hh[2] = u; }
{ unsigned u = pk_bf16(P[1][3][2], P[1][3][3]); PIN(u); hh[3] = u; }
SB(); M16A(Q[1][3], AF(1), hb[3][2]); SB();
GLDS(lvoffA, lbase, 0);
SB(); M16A(Q[0][0], AF(2), hb[0][3]); SB();
afl[1] = TR(lbB1, 16384);
SB(); M16A(Q[0][1], AF(2), hb[1][3]); SB();
afh[1] = TR(lbB1, 18432);
SB(); M16A(Q[0][2], AF(2), hb[2][3]); SB();
PST(pvo, hh, pbase0 + 497664);
SB(); M16A(Q[0][3], AF(2), hb[3][3]); SB();
{ unsigned u = pk_bf16(R[0][0][0], R[0][0][1]); PIN(u); hh[0] = u; }
{ unsigned u = pk_bf16(R[0][0][2], R[0][0][3]); PIN(u); hh[1] = u; }
{ unsigned u = pk_bf16(R[1][0][0], R[1][0][1]); PIN(u); hh[2] = u; }
SB(); M16A(Q[1][0], AF(3), hb[0][3]); SB();
afl[2] = TR(lbB0, 20480);
{ unsigned u = pk_bf16(R[1][0][2], R[1][0][3]); PIN(u); hh[3] = u; }
SB(); M16A(Q[1][1], AF(3), hb[1][3]); SB();
afh[2] = TR(lbB0, 22528);
SB(); M16A(Q[1][2], AF(3), hb[2][3]); SB();
GLDS(lvoffB, lbase, 1024);
SB(); M16A(Q[1][3], AF(3), hb[3][3]); SB();
SB();
asm volatile("s_waitcnt vmcnt(16)\n\ts_barrier" ::: "memory");
SB(); M16A(Q[0][0], AF(0), hb[0][4]); SB();
afl[3] = TR(lbB1, 20480);
SB(); M16A(Q[0][1], AF(0), hb[1][4]); SB();
afh[3] = TR(lbB1, 22528);
SB(); M16A(Q[0][2], AF(0), hb[2][4]); SB();
lbase = wdb + 74112;
SB(); M16A(Q[0][3], AF(0), hb[3][4]); SB();
PST(pvo, hh, pbase0 + 18432);
SB(); M16A(Q[1][0], AF(1), hb[0][4]); SB();
afl[0] = TR(lbB0, 24576);
{ unsigned u = pk_bf16(R[0][1][0], R[0][1][1]); PIN(u); hh[0] = u; }
SB(); M16A(Q[1][1], AF(1), hb[1][4]); SB();
afh[0] = TR(lbB0, 26624);
{ unsigned u = pk_bf16(R[0][1][2], R[0][1][3]); PIN(u); hh[1] = u; }
SB(); M16A(Q[1][2], AF(1), hb[2][4]); SB();
SETM0(wv2k + 8192);
{ unsigned u = pk_bf16(R[1][1][0], R[1][1][1]); PIN(u); hh[2] = u; }
{ unsigned u = pk_bf16(R[1][1][2], R[1][1][3]); PIN(u); hh[3] = u; }
SB(); M16A(Q[1][3], AF(1), hb[3][4]); SB();
GLDS(lvoffA, lbase, 0);
SB(); M16A(Q[0][0], AF(2), hb[0][5]); SB();
afl[1] = TR(lbB1, 24576);
SB(); M16A(Q[0][1], AF(2), hb[1][5]); SB();
afh[1] = TR(lbB1, 26624);
SB(); M16A(Q[0][2], AF(2), hb[2][5]); SB();
PST(pvo, hh, pbase0 + 184320);
SB(); M16A(Q[0][3], AF(2), hb[3][5]); SB();
{ unsigned u = pk_bf16(R[0][2][0], R[0][2][1]); PIN(u); hh[0] = u; }
{ unsigned u = pk_bf16(R[0][2][2], R[0][2][3]); PIN(u); hh[1] = u; }
{ unsigned u = pk_bf16(R[1][2][0], R[1][2][1]); PIN(u); hh[2] = u; }
SB(); M16A(Q[1][0], AF(3), hb[0][5]); SB();
afl[2] = TR(lbB0, 28672);
{ unsigned u = pk_bf16(R[1][2][2], R[1][2][3]); PIN(u); hh[3] = u; }
SB(); M16A(Q[1][1], AF(3), hb[1][5]); SB();
afh[2] = TR(lbB0, 30720);
SB(); M16A(Q[1][2], AF(3), hb[2][5]); SB();
GLDS(lvoffB, lbase, 1024);
SB(); M16A(Q[1][3], AF(3), hb[3][5]); SB();
SB();
asm volatile("s_waitcnt vmcnt(18)\n\ts_barrier" ::: "memory");
SB(); M16A(Q[0][0], AF(0), hb[0][6]); SB();
afl[3] = TR(lbB1, 28672);
SB(); M16A(Q[0][1], AF(0), hb[1][6]); SB();
afh[3] = TR(lbB1, 30720);
SB(); M16A(Q[0][2], AF(0), hb[2][6]); SB();
lbase = wdb + 147840;
SB(); M16A(Q[0][3], AF(0), hb[3][6]); SB();
PST(pvo, hh, pbase0 + 350208);
SB(); M16A(Q[1][0], AF(1), hb[0][6]); SB();
afl[0] = TR(lbB0, 32768);
{ unsigned u = pk_bf16(R[0][3][0], R[0][3][1]); PIN(u); hh[0] = u; }
SB(); M16A(Q[1][1], AF(1), hb[1][6]); SB();
afh[0] = TR(lbB0, 34816);
{ unsigned u = pk_bf16(R[0][3][2], R[0][3][3]); PIN(u); hh[1] = u; }
SB(); M16A(Q[1][2], AF(1), hb[2][6]); SB();
SETM0(wv2k + 16384);
{ unsigned u = pk_bf16(R[1][3][0], R[1][3][1]); PIN(u); hh[2] = u; }
{ unsigned u = pk_bf16(R[1][3][2], R[1][3][3]); PIN(u); hh[3] = u; }
SB(); M16A(Q[1][3], AF(1), hb[3][6]); SB();
GLDS(lvoffA, lbase, 0);
SB(); M16A(Q[0][0], AF(2), hb[0][7]); SB();
afl[1] = TR(lbB1, 32768);
SB(); M16A(Q[0][1], AF(2), hb[1][7]); SB();
afh[1] = TR(lbB1, 34816);
SB(); M16A(Q[0][2], AF(2), hb[2][7]); SB();
PST(pvo, hh, pbase0 + 516096);
SB(); M16A(Q[0][3], AF(2), hb[3][7]); SB();
SB(); M16A(Q[1][0], AF(3), hb[0][7]); SB();
afl[2] = TR(lbB0, 36864);
SB(); M16A(Q[1][1], AF(3), hb[1][7]); SB();
afh[2] = TR(lbB0, 38912);
SB(); M16A(Q[1][2], AF(3), hb[2][7]); SB();
GLDS(lvoffB, lbase, 1024);
SB(); M16A(Q[1][3], AF(3), hb[3][7]); SB();
SB();
asm volatile("s_waitcnt vmcnt(20)\n\ts_barrier" ::: "memory");
SB(); M16A(Q[0][0], AF(0), hb[0][8]); SB();
afl[3] = TR(lbB1, 36864);
SB(); M16A(Q[0][1], AF(0), hb[1][8]); SB();
afh[3] = TR(lbB1, 38912);
SB(); M16A(Q[0][2], AF(0), hb[2][8]); SB();
lbase = wdb + 221568;
SB(); M16A(Q[0][3], AF(0), hb[3][8]); SB();
SB(); M16A(Q[1][0], AF(1), hb[0][8]); SB();
afl[0] = TR(lbB0, 40960);
SB(); M16A(Q[1][1], AF(1), hb[1][8]); SB();
afh[0] = TR(lbB0, 43008);
SB(); M16A(Q[1][2], AF(1), hb[2][8]); SB();
SETM0(wv2k + 24576);
SB(); M16A(Q[1][3], AF(1), hb[3][8]); SB();
GLDS(lvoffA, lbase, 0);
SB(); M16A(Q[0][0], AF(2), hb[0][9]); SB();
afl[1] = TR(lbB1, 40960);
SB(); M16A(Q[0][1], AF(2), hb[1][9]); SB();
afh[1] = TR(lbB1, 43008);
SB(); M16A(Q[0][2], AF(2), hb[2][9]); SB();
SB(); M16A(Q[0][3], AF(2), hb[3][9]); SB();
SB(); M16A(Q[1][0], AF(3), hb[0][9]); SB();
afl[2] = TR(lbB0, 45056);
SB(); M16A(Q[1][1], AF(3), hb[1][9]); SB();
afh[2] = TR(lbB0, 47104);
SB(); M16A(Q[1][2], AF(3), hb[2][9]); SB();
GLDS(lvoffB, lbase, 1024);
SB(); M16A(Q[1][3], AF(3), hb[3][9]); SB();
SB();
asm volatile("s_waitcnt vmcnt(3)\n\ts_barrier" ::: "memory");
SIGNAL_ADD(fP, 1, true);
SB(); M16A(Q[0][0], AF(0), hb[0][10]); SB();
afl[3] = TR(lbB1, 45056);
SB(); M16A(Q[0][1], AF(0), hb[1][10]); SB();
afh[3] = TR(lbB1, 47104);
SB(); M16A(Q[0][2], AF(0), hb[2][10]); SB();
lbase = wdb + 295296;
SB(); M16A(Q[0][3], AF(0), hb[3][10]); SB();
SB(); M16A(Q[1][0], AF(1), hb[0][10]); SB();
afl[0] = TR(lbB0, 49152);
SB(); M16A(Q[1][1], AF(1), hb[1][10]); SB();
afh[0] = TR(lbB0, 51200);
SB(); M16A(Q[1][2], AF(1), hb[2][10]); SB();
SETM0(wv2k + 32768);
SB(); M16A(Q[1][3], AF(1), hb[3][10]); SB();
GLDS(lvoffA, lbase, 0);
SB(); M16A(Q[0][0], AF(2), hb[0][11]); SB();
afl[1] = TR(lbB1, 49152);
SB(); M16A(Q[0][1], AF(2), hb[1][11]); SB();
afh[1] = TR(lbB1, 51200);
SB(); M16A(Q[0][2], AF(2), hb[2][11]); SB();
SB(); M16A(Q[0][3], AF(2), hb[3][11]); SB();
SB(); M16A(Q[1][0], AF(3), hb[0][11]); SB();
afl[2] = TR(lbB0, 53248);
SB(); M16A(Q[1][1], AF(3), hb[1][11]); SB();
afh[2] = TR(lbB0, 55296);
SB(); M16A(Q[1][2], AF(3), hb[2][11]); SB();
GLDS(lvoffB, lbase, 1024);
SB(); M16A(Q[1][3], AF(3), hb[3][11]); SB();
SB();
asm volatile("s_waitcnt vmcnt(20)\n\ts_barrier" ::: "memory");
SB(); M16A(Q[0][0], AF(0), hb[0][12]); SB();
afl[3] = TR(lbB1, 53248);
SB(); M16A(Q[0][1], AF(0), hb[1][12]); SB();
afh[3] = TR(lbB1, 55296);
SB(); M16A(Q[0][2], AF(0), hb[2][12]); SB();
lbase = wdb + 369024;
SB(); M16A(Q[0][3], AF(0), hb[3][12]); SB();
SB(); M16A(Q[1][0], AF(1), hb[0][12]); SB();
afl[0] = TR(lbB0, 57344);
SB(); M16A(Q[1][1], AF(1), hb[1][12]); SB();
afh[0] = TR(lbB0, 59392);
SB(); M16A(Q[1][2], AF(1), hb[2][12]); SB();
SETM0(wv2k + 40960);
SB(); M16A(Q[1][3], AF(1), hb[3][12]); SB();
GLDS(lvoffA, lbase, 0);
SB(); M16A(Q[0][0], AF(2), hb[0][13]); SB();
afl[1] = TR(lbB1, 57344);
SB(); M16A(Q[0][1], AF(2), hb[1][13]); SB();
afh[1] = TR(lbB1, 59392);
SB(); M16A(Q[0][2], AF(2), hb[2][13]); SB();
SB(); M16A(Q[0][3], AF(2), hb[3][13]); SB();
SB(); M16A(Q[1][0], AF(3), hb[0][13]); SB();
afl[2] = TR(lbB0, 61440);
SB(); M16A(Q[1][1], AF(3), hb[1][13]); SB();
afh[2] = TR(lbB0, 63488);
SB(); M16A(Q[1][2], AF(3), hb[2][13]); SB();
GLDS(lvoffB, lbase, 1024);
SB(); M16A(Q[1][3], AF(3), hb[3][13]); SB();
SB();
asm volatile("s_waitcnt vmcnt(18)\n\ts_barrier" ::: "memory");
SB(); M16A(Q[0][0], AF(0), hb[0][14]); SB();
afl[3] = TR(lbB1, 61440);
SB(); M16A(Q[0][1], AF(0), hb[1][14]); SB();
afh[3] = TR(lbB1, 63488);
SB(); M16A(Q[0][2], AF(0), hb[2][14]); SB();
lbase = wdb + 442752;
SB(); M16A(Q[0][3], AF(0), hb[3][14]); SB();
SB(); M16A(Q[1][0], AF(1), hb[0][14]); SB();
afl[0] = TR(lbB0, 65536);
SB(); M16A(Q[1][1], AF(1), hb[1][14]); SB();
afh[0] = TR(lbB0, 67584);
SB(); M16A(Q[1][2], AF(1), hb[2][14]); SB();
SETM0(wv2k + 49152);
SB(); M16A(Q[1][3], AF(1), hb[3][14]); SB();
GLDS(lvoffA, lbase, 0);
SB(); M16V(Q[0][0], AF(2), hb[0][15]); SB();
afl[1] = TR(lbB1, 65536);
SB(); M16V(Q[0][1], AF(2), hb[1][15]); SB();
afh[1] = TR(lbB1, 67584);
SB(); M16V(Q[0][2], AF(2), hb[2][15]); SB();
SB(); M16V(Q[0][3], AF(2), hb[3][15]); SB();
SB(); M16V(Q[1][0], AF(3), hb[0][15]); SB();
afl[2] = TR(lbB0, 69632);
SB(); M16V(Q[1][1], AF(3), hb[1][15]); SB();
afh[2] = TR(lbB0, 71680);
SB(); M16V(Q[1][2], AF(3), hb[2][15]); SB();
GLDS(lvoffB, lbase, 1024);
SB(); M16V(Q[1][3], AF(3), hb[3][15]); SB();
SB();
asm volatile("s_waitcnt vmcnt(16)\n\ts_barrier" ::: "memory");
SB(); M16V(Q[0][0], AF(0), hb[0][16]); SB();
afl[3] = TR(lbB1, 69632);
SB(); M16V(Q[0][1], AF(0), hb[1][16]); SB();
afh[3] = TR(lbB1, 71680);
SB(); M16V(Q[0][2], AF(0), hb[2][16]); SB();
lbase = wdb + 516480;
SB(); M16V(Q[0][3], AF(0), hb[3][16]); SB();
SB(); M16V(Q[1][0], AF(1), hb[0][16]); SB();
afl[0] = TR(lbB0, 0);
SB(); M16V(Q[1][1], AF(1), hb[1][16]); SB();
afh[0] = TR(lbB0, 2048);
SB(); M16V(Q[1][2], AF(1), hb[2][16]); SB();
SETM0(wv2k + 57344);
SB(); M16V(Q[1][3], AF(1), hb[3][16]); SB();
GLDS(lvoffA, lbase, 0);
SB(); M16V(Q[0][0], AF(2), hb[0][17]); SB();
afl[1] = TR(lbB1, 0);
SB(); M16V(Q[0][1], AF(2), hb[1][17]); SB();
afh[1] = TR(lbB1, 2048);
SB(); M16V(Q[0][2], AF(2), hb[2][17]); SB();
SB(); M16V(Q[0][3], AF(2), hb[3][17]); SB();
SB(); M16V(Q[1][0], AF(3), hb[0][17]); SB();
afl[2] = TR(lbB0, 4096);
SB(); M16V(Q[1][1], AF(3), hb[1][17]); SB();
afh[2] = TR(lbB0, 6144);
SB(); M16V(Q[1][2], AF(3), hb[2][17]); SB();
GLDS(lvoffB, lbase, 1024);
SB(); M16V(Q[1][3], AF(3), hb[3][17]); SB();
SB();
asm volatile("s_waitcnt vmcnt(14)\n\ts_barrier" ::: "memory");
SB(); M16ZA(P[0][0], AF(0), hb[0][0]); SB();
afl[3] = TR(lbB1, 4096);
SB(); M16ZA(P[0][1], AF(0), hb[1][0]); SB();
afh[3] = TR(lbB1, 6144);
SB(); M16ZA(P[0][2], AF(0), hb[2][0]); SB();
lbase = wdb + 590208;
SB(); M16ZA(P[0][3], AF(0), hb[3][0]); SB();
SB(); M16ZA(P[1][0], AF(1), hb[0][0]); SB();
afl[0] = TR(lbB0, 8192);
SB(); M16ZA(P[1][1], AF(1), hb[1][0]); SB();
afh[0] = TR(lbB0, 10240);
SB(); M16ZA(P[1][2], AF(1), hb[2][0]); SB();
SETM0(wv2k + 65536);
SB(); M16ZA(P[1][3], AF(1), hb[3][0]); SB();
GLDS(lvoffA, lbase, 0);
SB(); M16A(P[0][0], AF(2), hb[0][1]); SB();
afl[1] = TR(lbB1, 8192);
SB(); M16A(P[0][1], AF(2), hb[1][1]); SB();
afh[1] = TR(lbB1, 10240);
SB(); M16A(P[0][2], AF(2), hb[2][1]); SB();
{ unsigned u = pk_bf16(Q[0][0][0], Q[0][0][1]); PIN(u); hh[0] = u; }
{ unsigned u = pk_bf16(Q[0][0][2], Q[0][0][3]); PIN(u); hh[1] = u; }
{ unsigned u = pk_bf16(Q[1][0][0], Q[1][0][1]); PIN(u); hh[2] = u; }
SB(); M16A(P[0][3], AF(2), hb[3][1]); SB();
{ unsigned u = pk_bf16(Q[1][0][2], Q[1][0][3]); PIN(u); hh[3] = u; }
SB(); M16A(P[1][0], AF(3), hb[0][1]); SB();
afl[2] = TR(lbB0, 12288);
SB(); M16A(P[1][1], AF(3), hb[1][1]); SB();
afh[2] = TR(lbB0, 14336);
SB(); M16A(P[1][2], AF(3), hb[2][1]); SB();
GLDS(lvoffB, lbase, 1024);
SB(); M16A(P[1][3], AF(3), hb[3][1]); SB();
SB();
asm volatile("s_waitcnt vmcnt(12)\n\ts_barrier" ::: "memory");
SB(); M16A(P[0][0], AF(0), hb[0][2]); SB();
afl[3] = TR(lbB1, 12288);
SB(); M16A(P[0][1], AF(0), hb[1][2]); SB();
afh[3] = TR(lbB1, 14336);
SB(); M16A(P[0][2], AF(0), hb[2][2]); SB();
lbase = a.wd; lvoffA = 0; lvoffB = 0;
SB(); M16A(P[0][3], AF(0), hb[3][2]); SB();
PST(pvo, hh, pbase0 + 36864);
SB(); M16A(P[1][0], AF(1), hb[0][2]); SB();
afl[0] = TR(lbB0, 16384);
{ unsigned u = pk_bf16(Q[0][1][0], Q[0][1][1]); PIN(u); hh[0] = u; }
SB(); M16A(P[1][1], AF(1), hb[1][2]); SB();
afh[0] = TR(lbB0, 18432);
{ unsigned u = pk_bf16(Q[0][1][2], Q[0][1][3]); PIN(u); hh[1] = u; }
SB(); M16A(P[1][2], AF(1), hb[2][2]); SB();
SETM0(wv2k + 0);
{ unsigned u = pk_bf16(Q[1][1][0], Q[1][1][1]); PIN(u); hh[2] = u; }
{ unsigned u = pk_bf16(Q[1][1][2], Q[1][1][3]); PIN(u); hh[3] = u; }
SB(); M16A(P[1][3], AF(1), hb[3][2]); SB();
GLDS(lvoffA, lbase, 0);
SB(); M16A(P[0][0], AF(2), hb[0][3]); SB();
afl[1] = TR(lbB1, 16384);
SB(); M16A(P[0][1], AF(2), hb[1][3]); SB();
afh[1] = TR(lbB1, 18432);
SB(); M16A(P[0][2], AF(2), hb[2][3]); SB();
PST(pvo, hh, pbase0 + 202752);
SB(); M16A(P[0][3], AF(2), hb[3][3]); SB();
{ unsigned u = pk_bf16(Q[0][2][0], Q[0][2][1]); PIN(u); hh[0] = u; }
{ unsigned u = pk_bf16(Q[0][2][2], Q[0][2][3]); PIN(u); hh[1] = u; }
{ unsigned u = pk_bf16(Q[1][2][0], Q[1][2][1]); PIN(u); hh[2] = u; }
SB(); M16A(P[1][0], AF(3), hb[0][3]); SB();
afl[2] = TR(lbB0, 20480);
{ unsigned u = pk_bf16(Q[1][2][2], Q[1][2][3]); PIN(u); hh[3] = u; }
SB(); M16A(P[1][1], AF(3), hb[1][3]); SB();
afh[2] = TR(lbB0, 22528);
SB(); M16A(P[1][2], AF(3), hb[2][3]); SB();
GLDS(lvoffB, lbase, 1024);
SB(); M16A(P[1][3], AF(3), hb[3][3]); SB();
SB();
asm volatile("s_waitcnt vmcnt(14)\n\ts_barrier" ::: "memory");
SB(); M16A(P[0][0], AF(0), hb[0][4]); SB();
afl[3] = TR(lbB1, 20480);
SB(); M16A(P[0][1], AF(0), hb[1][4]); SB();
afh[3] = TR(lbB1, 22528);
SB(); M16A(P[0][2], AF(0), hb[2][4]); SB();
SB(); M16A(P[0][3], AF(0), hb[3][4]); SB();
PST(pvo, hh, pbase0 + 368640);
SB(); M16A(P[1][0], AF(1), hb[0][4]); SB();
afl[0] = TR(lbB0, 24576);
{ unsigned u = pk_bf16(Q[0][3][0], Q[0][3][1]); PIN(u); hh[0] = u; }
SB(); M16A(P[1][1], AF(1), hb[1][4]); SB();
afh[0] = TR(lbB0, 26624);
{ unsigned u = pk_bf16(Q[0][3][2], Q[0][3][3]); PIN(u); hh[1] = u; }
SB(); M16A(P[1][2], AF(1), hb[2][4]); SB();
SETM0(wv2k + 8192);
{ unsigned u = pk_bf16(Q[1][3][0], Q[1][3][1]); PIN(u); hh[2] = u; }
{ unsigned u = pk_bf16(Q[1][3][2], Q[1][3][3]); PIN(u); hh[3] = u; }
SB(); M16A(P[1][3], AF(1), hb[3][4]); SB();
GLDS(lvoffA, lbase, 0);
SB(); M16A(P[0][0], AF(2), hb[0][5]); SB();
afl[1] = TR(lbB1, 24576);
SB(); M16A(P[0][1], AF(2), hb[1][5]); SB();
afh[1] = TR(lbB1, 26624);
SB(); M16A(P[0][2], AF(2), hb[2][5]); SB();
PST(pvo, hh, pbase0 + 534528);
SB(); M16A(P[0][3], AF(2), hb[3][5]); SB();
SB(); M16A(P[1][0], AF(3), hb[0][5]); SB();
afl[2] = TR(lbB0, 28672);
SB(); M16A(P[1][1], AF(3), hb[1][5]); SB();
afh[2] = TR(lbB0, 30720);
SB(); M16A(P[1][2], AF(3), hb[2][5]); SB();
GLDS(lvoffB, lbase, 1024);
SB(); M16A(P[1][3], AF(3), hb[3][5]); SB();
SB();
asm volatile("s_waitcnt vmcnt(16)\n\ts_barrier" ::: "memory");
SB(); M16A(P[0][0], AF(0), hb[0][6]); SB();
afl[3] = TR(lbB1, 28672);
SB(); M16A(P[0][1], AF(0), hb[1][6]); SB();
afh[3] = TR(lbB1, 30720);
SB(); M16A(P[0][2], AF(0), hb[2][6]); SB();
SB(); M16A(P[0][3], AF(0), hb[3][6]); SB();
SB(); M16A(P[1][0], AF(1), hb[0][6]); SB();
afl[0] = TR(lbB0, 32768);
SB(); M16A(P[1][1], AF(1), hb[1][6]); SB();
afh[0] = TR(lbB0, 34816);
SB(); M16A(P[1][2], AF(1), hb[2][6]); SB();
SETM0(wv2k + 16384);
SB(); M16A(P[1][3], AF(1), hb[3][6]); SB();
GLDS(lvoffA, lbase, 0);
SB(); M16A(P[0][0], AF(2), hb[0][7]); SB();
afl[1] = TR(lbB1, 32768);
SB(); M16A(P[0][1], AF(2), hb[1][7]); SB();
afh[1] = TR(lbB1, 34816);
SB(); M16A(P[0][2], AF(2), hb[2][7]); SB();
SB(); M16A(P[0][3], AF(2), hb[3][7]); SB();
SB(); M16A(P[1][0], AF(3), hb[0][7]); SB();
afl[2] = TR(lbB0, 36864);
SB(); M16A(P[1][1], AF(3), hb[1][7]); SB();
afh[2] = TR(lbB0, 38912);
SB(); M16A(P[1][2], AF(3), hb[2][7]); SB();
GLDS(lvoffB, lbase, 1024);
SB(); M16A(P[1][3], AF(3), hb[3][7]); SB();
SB();
asm volatile("s_waitcnt vmcnt(3)\n\ts_barrier" ::: "memory");
SIGNAL_ADD(fP + 32, 1, true);
SB(); M16A(P[0][0], AF(0), hb[0][8]); SB();
afl[3] = TR(lbB1, 36864);
SB(); M16A(P[0][1], AF(0), hb[1][8]); SB();
afh[3] = TR(lbB1, 38912);
SB(); M16A(P[0][2], AF(0), hb[2][8]); SB();
SB(); M16A(P[0][3], AF(0), hb[3][8]); SB();
SB(); M16A(P[1][0], AF(1), hb[0][8]); SB();
afl[0] = TR(lbB0, 40960);
SB(); M16A(P[1][1], AF(1), hb[1][8]); SB();
afh[0] = TR(lbB0, 43008);
SB(); M16A(P[1][2], AF(1), hb[2][8]); SB();
SETM0(wv2k + 24576);
SB(); M16A(P[1][3], AF(1), hb[3][8]); SB();
GLDS(lvoffA, lbase, 0);
SB(); M16A(P[0][0], AF(2), hb[0][9]); SB();
afl[1] = TR(lbB1, 40960);
SB(); M16A(P[0][1], AF(2), hb[1][9]); SB();
afh[1] = TR(lbB1, 43008);
SB(); M16A(P[0][2], AF(2), hb[2][9]); SB();
SB(); M16A(P[0][3], AF(2), hb[3][9]); SB();
SB(); M16A(P[1][0], AF(3), hb[0][9]); SB();
afl[2] = TR(lbB0, 45056);
SB(); M16A(P[1][1], AF(3), hb[1][9]); SB();
afh[2] = TR(lbB0, 47104);
SB(); M16A(P[1][2], AF(3), hb[2][9]); SB();
GLDS(lvoffB, lbase, 1024);
SB(); M16A(P[1][3], AF(3), hb[3][9]); SB();
SB();
asm volatile("s_waitcnt vmcnt(16)\n\ts_barrier" ::: "memory");
SB(); M16A(P[0][0], AF(0), hb[0][10]); SB();
afl[3] = TR(lbB1, 45056);
SB(); M16A(P[0][1], AF(0), hb[1][10]); SB();
afh[3] = TR(lbB1, 47104);
SB(); M16A(P[0][2], AF(0), hb[2][10]); SB();
SB(); M16A(P[0][3], AF(0), hb[3][10]); SB();
SB(); M16A(P[1][0], AF(1), hb[0][10]); SB();
afl[0] = TR(lbB0, 49152);
SB(); M16A(P[1][1], AF(1), hb[1][10]); SB();
afh[0] = TR(lbB0, 51200);
SB(); M16A(P[1][2], AF(1), hb[2][10]); SB();
SETM0(wv2k + 32768);
SB(); M16A(P[1][3], AF(1), hb[3][10]); SB();
GLDS(lvoffA, lbase, 0);
SB(); M16A(P[0][0], AF(2), hb[0][11]); SB();
afl[1] = TR(lbB1, 49152);
SB(); M16A(P[0][1], AF(2), hb[1][11]); SB();
afh[1] = TR(lbB1, 51200);
SB(); M16A(P[0][2], AF(2), hb[2][11]); SB();
SB(); M16A(P[0][3], AF(2), hb[3][11]); SB();
SB(); M16A(P[1][0], AF(3), hb[0][11]); SB();
afl[2] = TR(lbB0, 53248);
SB(); M16A(P[1][1], AF(3), hb[1][11]); SB();
afh[2] = TR(lbB0, 55296);
SB(); M16A(P[1][2], AF(3), hb[2][11]); SB();
GLDS(lvoffB, lbase, 1024);
SB(); M16A(P[1][3], AF(3), hb[3][11]); SB();
SB();
asm volatile("s_waitcnt vmcnt(16)\n\ts_barrier" ::: "memory");
SB(); M16A(P[0][0], AF(0), hb[0][12]); SB();
afl[3] = TR(lbB1, 53248);
SB(); M16A(P[0][1], AF(0), hb[1][12]); SB();
afh[3] = TR(lbB1, 55296);
SB(); M16A(P[0][2], AF(0), hb[2][12]); SB();
SB(); M16A(P[0][3], AF(0), hb[3][12]); SB();
SB(); M16A(P[1][0], AF(1), hb[0][12]); SB();
afl[0] = TR(lbB0, 57344);
SB(); M16A(P[1][1], AF(1), hb[1][12]); SB();
afh[0] = TR(lbB0, 59392);
SB(); M16A(P[1][2], AF(1), hb[2][12]); SB();
SETM0(wv2k + 40960);
SB(); M16A(P[1][3], AF(1), hb[3][12]); SB();
GLDS(lvoffA, lbase, 0);
SB(); M16A(P[0][0], AF(2), hb[0][13]); SB();
afl[1] = TR(lbB1, 57344);
SB(); M16A(P[0][1], AF(2), hb[1][13]); SB();
afh[1] = TR(lbB1, 59392);
SB(); M16A(P[0][2], AF(2), hb[2][13]); SB();
SB(); M16A(P[0][3], AF(2), hb[3][13]); SB();
SB(); M16A(P[1][0], AF(3), hb[0][13]); SB();
afl[2] = TR(lbB0, 61440);
SB(); M16A(P[1][1], AF(3), hb[1][13]); SB();
afh[2] = TR(lbB0, 63488);
SB(); M16A(P[1][2], AF(3), hb[2][13]); SB();
GLDS(lvoffB, lbase, 1024);
SB(); M16A(P[1][3], AF(3), hb[3][13]); SB();
SB();
asm volatile("s_waitcnt vmcnt(16)\n\ts_barrier" ::: "memory");
SB(); M16A(P[0][0], AF(0), hb[0][14]); SB();
afl[3] = TR(lbB1, 61440);
SB(); M16A(P[0][1], AF(0), hb[1][14]); SB();
afh[3] = TR(lbB1, 63488);
SB(); M16A(P[0][2], AF(0), hb[2][14]); SB();
SB(); M16A(P[0][3], AF(0), hb[3][14]); SB();
SB(); M16A(P[1][0], AF(1), hb[0][14]); SB();
afl[0] = TR(lbB0, 65536);
SB(); M16A(P[1][1], AF(1), hb[1][14]); SB();
afh[0] = TR(lbB0, 67584);
SB(); M16A(P[1][2], AF(1), hb[2][14]); SB();
SETM0(wv2k + 49152);
SB(); M16A(P[1][3], AF(1), hb[3][14]); SB();
GLDS(lvoffA, lbase, 0);
SB(); M16V(P[0][0], AF(2), hb[0][15]); SB();
afl[1] = TR(lbB1, 65536);
SB(); M16V(P[0][1], AF(2), hb[1][15]); SB();
afh[1] = TR(lbB1, 67584);
SB(); M16V(P[0][2], AF(2), hb[2][15]); SB();
SB(); M16V(P[0][3], AF(2), hb[3][15]); SB();
SB(); M16V(P[1][0], AF(3), hb[0][15]); SB();
afl[2] = TR(lbB0, 69632);
SB(); M16V(P[1][1], AF(3), hb[1][15]); SB();
afh[2] = TR(lbB0, 71680);
SB(); M16V(P[1][2], AF(3), hb[2][15]); SB();
GLDS(lvoffB, lbase, 1024);
SB(); M16V(P[1][3], AF(3), hb[3][15]); SB();
SB();
asm volatile("s_waitcnt vmcnt(14)\n\ts_barrier" ::: "memory");
SB(); M16V(P[0][0], AF(0), hb[0][16]); SB();
afl[3] = TR(lbB1, 69632);
SB(); M16V(P[0][1], AF(0), hb[1][16]); SB();
afh[3] = TR(lbB1, 71680);
SB(); M16V(P[0][2], AF(0), hb[2][16]); SB();
SB(); M16V(P[0][3], AF(0), hb[3][16]); SB();
SB(); M16V(P[1][0], AF(1), hb[0][16]); SB();
afl[0] = TR(lbB0, 0);
SB(); M16V(P[1][1], AF(1), hb[1][16]); SB();
afh[0] = TR(lbB0, 2048);
SB(); M16V(P[1][2], AF(1), hb[2][16]); SB();
SETM0(wv2k + 57344);
SB(); M16V(P[1][3], AF(1), hb[3][16]); SB();
GLDS(lvoffA, lbase, 0);
SB(); M16V(P[0][0], AF(2), hb[0][17]); SB();
afl[1] = TR(lbB1, 0);
SB(); M16V(P[0][1], AF(2), hb[1][17]); SB();
afh[1] = TR(lbB1, 2048);
SB(); M16V(P[0][2], AF(2), hb[2][17]); SB();
SB(); M16V(P[0][3], AF(2), hb[3][17]); SB();
SB(); M16V(P[1][0], AF(3), hb[0][17]); SB();
afl[2] = TR(lbB0, 4096);
SB(); M16V(P[1][1], AF(3), hb[1][17]); SB();
afh[2] = TR(lbB0, 6144);
SB(); M16V(P[1][2], AF(3), hb[2][17]); SB();
GLDS(lvoffB, lbase, 1024);
SB(); M16V(P[1][3], AF(3), hb[3][17]); SB();
STAMP(10); JIT(10);
  const char* rbase = (a.ws + WS_PART) + ((size_t)(((m * 8 + (s >> 1)) * 18 + (s & 1)) * NSL)) * 1024;
  const int rdp0 = (wave & 1) + 4 * (wave >> 1);
  const int rdp1 = wave == 0 ? 2 : (wave == 3 ? 3 : 5 + wave);
  const unsigned ln3 = lane_id();
  const unsigned xoff = (unsigned)((m * 128 + (s >> 1) * 16 + (ln3 & 15)) * 1152 + ((s & 1) * 32 + 4 * (ln3 >> 4)) * 2);
  u32x2 x0[2], x1[2], x2[2];
  unsigned z0, z1, z2;
  #define LDX(dst, dp, q) asm volatile("global_load_dwordx2 %0, %1, %2" : "=v"(dst) : "v"(xoff), "s"(a.x + (dp) * 128 + (q) * 32) : "memory")
  LDW1(z0, zvo, fP); LDW1(z1, zvo, fP + 32); LDW1(z2, zvo, fP + 64);
  LDX(x0[0], rdp0, 0); LDX(x0[1], rdp0, 1); LDX(x1[0], rdp1, 0); LDX(x1[1], rdp1, 1); LDX(x2[0], 8, 0); LDX(x2[1], 8, 1);

MNOP();
{ unsigned u = pk_bf16(P[0][0][0], P[0][0][1]); PIN(u); hh[0] = u; }
{ unsigned u = pk_bf16(P[0][0][2], P[0][0][3]); PIN(u); hh[1] = u; }
{ unsigned u = pk_bf16(P[1][0][0], P[1][0][1]); PIN(u); hh[2] = u; }
{ unsigned u = pk_bf16(P[1][0][2], P[1][0][3]); PIN(u); hh[3] = u; }
PST(pvo, hh, pbase0 + 55296);
{ unsigned u = pk_bf16(P[0][1][0], P[0][1][1]); PIN(u); hh[0] = u; }
{ unsigned u = pk_bf16(P[0][1][2], P[0][1][3]); PIN(u); hh[1] = u; }
{ unsigned u = pk_bf16(P[1][1][0], P[1][1][1]); PIN(u); hh[2] = u; }
{ unsigned u = pk_bf16(P[1][1][2], P[1][1][3]); PIN(u); hh[3] = u; }
PST(pvo, hh, pbase0 + 221184);
{ unsigned u = pk_bf16(P[0][2][0], P[0][2][1]); PIN(u); hh[0] = u; }
{ unsigned u = pk_bf16(P[0][2][2], P[0][2][3]); PIN(u); hh[1] = u; }
{ unsigned u = pk_bf16(P[1][2][0], P[1][2][1]); PIN(u); hh[2] = u; }
{ unsigned u = pk_bf16(P[1][2][2], P[1][2][3]); PIN(u); hh[3] = u; }
PST(pvo, hh, pbase0 + 387072);
{ unsigned u = pk_bf16(P[0][3][0], P[0][3][1]); PIN(u); hh[0] = u; }
{ unsigned u = pk_bf16(P[0][3][2], P[0][3][3]); PIN(u); hh[1] = u; }
{ unsigned u = pk_bf16(P[1][3][0], P[1][3][1]); PIN(u); hh[2] = u; }
{ unsigned u = pk_bf16(P[1][3][2], P[1][3][3]); PIN(u); hh[3] = u; }
PST(pvo, hh, pbase0 + 552960);
  {
    asm volatile("s_waitcnt vmcnt(0)\n\ts_barrier" : "+v"(z0), "+v"(z1), "+v"(z2), "+v"(x0[0]), "+v"(x0[1]), "+v"(x1[0]), "+v"(x1[1]), "+v"(x2[0]), "+v"(x2[1]) :: "memory");
    SIGNAL_ADD(fP + 64, 1, true);
    STAMP(12);
    const __amdgpu_buffer_rsrc_t rrs = __builtin_amdgcn_make_buffer_rsrc((void*)rbase, 0, 18 * NSL * 1024, 0x00020000);
    auto load_unit = [&](u32x4 (&v)[NSL], int dp) {
      #pragma unroll
      for (int i = 0; i < NSL; ++i) v[i] = __builtin_amdgcn_raw_buffer_load_b128(rrs, pvo + i * 1024, dp * (2 * NSL * 1024), 16);
    };
    auto finish_unit = [&](u32x4 (&v)[NSL], const u32x2 (&xv)[2], int dp) {
      float acc[8];
      #pragma unroll
      for (int i = 0; i < 8; ++i) acc[i] = 0.f;
      #pragma unroll
      for (int sl = 0; sl < 8; ++sl)
        #pragma unroll
        for (int i = 0; i < 4; ++i) {
          unsigned u = v[sl][i];
          acc[2 * i] += bflo(u);
          acc[2 * i + 1] += bfhi(u);
        }
      #pragma unroll
      for (int q = 0; q < 2; ++q) {
        float r[4];
        #pragma unroll
        for (int i = 0; i < 4; ++i) {
          float d = rb(acc[4 * q + i]);
          float gg = rb(d * __builtin_amdgcn_rcpf(1.0f + __builtin_amdgcn_exp2f(-1.4426950408889634f * d)));
          unsigned xu = xv[q][i >> 1], su = v[8][2 * q + (i >> 1)];
          float xx = (i & 1) ? bfhi(xu) : bflo(xu);
          float ss = (i & 1) ? bfhi(su) : bflo(su);
          r[i] = xx + rb(ss * gg);
        }
        u32x2 o; o[0] = pk_bf16(r[0], r[1]); o[1] = pk_bf16(r[2], r[3]);
        *(u32x2*)(a.out + xoff + dp * 128 + q * 32) = o;
      }
    };
    u32x4 rv[NSL], rw[NSL], ru[NSL];
    CHECK_GE(z0, fP, 25); load_unit(rv, rdp0);
    if (wave < 3) { CHECK_GE(z1, fP + 32, 16); load_unit(rw, rdp1); }
    STAMP(13);
    const bool last2 = (wave == 1 || wave == 3);
    const int rdp2 = wave == 1 ? 8 : 3;
    z2 += 1;
    const bool rdy2 = __builtin_amdgcn_readfirstlane(z2) >= 16u;
    if (last2 && rdy2) load_unit(ru, rdp2);
    finish_unit(rv, x0, rdp0);
    if (wave < 3) finish_unit(rw, x1, rdp1);
    STAMP(14);
    if (last2 && !rdy2) { z2 = 0; CHECK_GE0(z2, fP + 64, 16); load_unit(ru, rdp2); }
    STAMP(15);
    if (last2) finish_unit(ru, wave == 1 ? x2 : x1, rdp2);
  }

asm volatile(".Lk1_tail0:");
  asm volatile("s_waitcnt vmcnt(0)" ::: "memory");
  STAMP(11);

}
DI void body1(const KArgs& a, int m, int j, int nq, int qp0, int nw, int wp0, int wm, int wave, const char* code_base, unsigned code_len) {
const int lane = (int)lane_id();
constexpr int NP = 4, TYPE = 1;
JIT(0);
  unsigned long long tsv[16], csv[16];
  STAMP(0);
  const int th = wave & 1, cg = wave >> 1;
  const int t16 = lane & 15, g = lane >> 4;
  const int tok = lane & 31, h = lane >> 5;
  const int s = 2 * j + TYPE;
  const int mp = __builtin_amdgcn_readfirstlane(m > 0 ? m - 1 : 0);
  const int f0 = j * 576 + TYPE * 320;
  const unsigned r8 = lane >> 3, c8 = lane & 7;
  const unsigned rowA = 16 * wave + r8;
  const unsigned c16P1 = (c8 ^ (((r8 >> 1) & 1) << 2)) * 16;
  const char* lbase; unsigned lstep = 0; unsigned lvoffA, lvoffB;
  const unsigned wv2k = wave * 2048;
  const char* const wqb = a.wqkv + qp0 * 128;
  const char* const wgb = a.wg + f0 * 2;
  const char* const wub = a.wu + f0 * 2;
  const char* const wob = a.wo + wp0 * 128;
  const char* wdb = a.wd + (size_t)j * 576 * 1152;
  u32x4 xb[2][36];
  const char* xsrc = (a.ws + WS_XN) + (size_t)(m * 4 + wave) * 36864;
  const char* osrc = (a.ws + WS_OF) + (size_t)(wm * 4 + wave) * 36864;
  const unsigned xo0 = lane * 16;
  const unsigned wv1k = wave * 1024, wv4k = wave * 4096;
  const unsigned xrd = H_OFF + th * 2048 + xo0;
  const unsigned trow = (unsigned)((m * 4 + wave) * 9 + (lane < 8 ? lane : 8));
  #define TOUCH(voff, sbase) asm volatile("s_mov_b32 m0, %2\n\ts_nop 3\n\tglobal_load_lds_dword %0, %1" :: "v"(voff), "s"(sbase), "s"(LDS_BYTES - 1024 + wave * 256) : "memory")
  const int q4 = t16 >> 2, p4 = lane & 3;
  const unsigned lbA = (unsigned)((8 * h + q4) * 128) + (((unsigned)(64 * cg)) ^ ((unsigned)(q4 >> 1) << 6)) + 32 * (g & 1) + 8 * p4;
  s16x4 afl[4], afh[4];
  const f32x4 zero4 = {0,0,0,0};
  const f32x16 zero16 = {0,0,0,0,0,0,0,0,0,0,0,0,0,0,0,0};
  f32x16 X[2], Y[2];
  const unsigned qvo = (unsigned)(h * 1024 + tok * 8);
  const char* qdst = (a.ws + WS_QKV) + ((size_t)(m * 432 + cg * 8) * 128 + th * 64) * 8;
  const float C0 = -1.5957691216057308f * 1.4426950408889634f;
  const float C1 = C0 * 0.044715f;
  float T[32];
  float gt0 = 0.f, gt1 = 0.f, gt2 = 0.f, hy0 = 0.f, hy1 = 0.f, pa = 0.f, pb = 0.f;
  u32x4 hh; u32x2 h2;
  unsigned pw0 = 0, pw1 = 0;
  const unsigned hw_ad = H_OFF + (cg * 8 + th * 4 + (tok >> 4)) * 1024 + (h * 16 + t16) * 16;
  const char* hxb = (a.ws + WS_HX) + (size_t)(m * 8 + j) * 147456;
  const unsigned hxvo = (unsigned)((tok >> 4) * 1024 + (h * 16 + t16) * 16);
  const char* hxw = hxb + (TYPE * 10 + cg) * 8192 + th * 4096;
  const char* hxr4 = hxb + wave * 4096;
  const char* pbase0 = (a.ws + WS_PART) + ((size_t)(((m * 8 + th * 4) * 18 + cg) * NSL + j)) * 1024;
  const char* sbase0 = (a.ws + WS_PART) + ((size_t)(((wm * 8 + wave * 2) * 18) * NSL + 8)) * 1024;
  const unsigned zvo = 0;
  unsigned* const fQ = ((unsigned*)(a.ws + WS_FLAGS)) + FLAG_Q(m);
  unsigned* const fQp = ((unsigned*)(a.ws + WS_FLAGS)) + FLAG_Q(mp);
  unsigned* const fO = ((unsigned*)(a.ws + WS_FLAGS)) + FLAG_O(m);
  unsigned* const fOw = ((unsigned*)(a.ws + WS_FLAGS)) + FLAG_O(wm);
  unsigned* const fP = ((unsigned*)(a.ws + WS_FLAGS)) + FLAG_P(m);
  unsigned* const fPw = ((unsigned*)(a.ws + WS_FLAGS)) + FLAG_P(wm);
  unsigned* const fHme = ((unsigned*)(a.ws + WS_FLAGS)) + FLAG_H(m, j, TYPE);
  unsigned* const fHpa = ((unsigned*)(a.ws + WS_FLAGS)) + FLAG_H(m, j, 1 - TYPE);
  #define SIGNAL_ADD(p, val, cnd) { const unsigned sv_ = (unsigned)(val); \
      asm volatile("s_cmp_eq_u32 %3, 0\n\ts_cselect_b64 exec, 1, 0\n\ts_nop 3\n\tglobal_atomic_add %0, %1, %2\n\ts_mov_b64 exec, -1" :: "v"(zvo), "v"(sv_), "s"(p), "s"(wave) : "memory", "scc"); }
  #define SIGNAL_SET(p, val, cnd) { const unsigned sv_ = (unsigned)(val); \
      asm volatile("s_cmp_eq_u32 %3, 0\n\ts_cselect_b64 exec, 1, 0\n\ts_nop 3\n\tglobal_store_dword %0, %1, %2 sc1\n\ts_mov_b64 exec, -1" :: "v"(zvo), "v"(sv_), "s"(p), "s"(wave) : "memory", "scc"); }
  #define CHECK_GE(w, p, target) { unsigned t1_, t2_; asm volatile( \
      "s_nop 1\n\tv_readfirstlane_b32 %1, %0\n\ts_nop 0\n\ts_cmp_ge_u32 %1, %5\n\ts_cbranch_scc1 .Lck_done%=\n\ts_mov_b32 %2, 0x400000\n" \
      ".Lck_loop%=:\n\ts_sleep 2\n\tglobal_load_dword %0, %3, %4 sc1\n\ts_waitcnt vmcnt(0)\n\tv_readfirstlane_b32 %1, %0\n\ts_nop 0\n\t" \
      "s_cmp_ge_u32 %1, %5\n\ts_cbranch_scc1 .Lck_done%=\n\ts_sub_u32 %2, %2, 1\n\ts_cmp_lg_u32 %2, 0\n\ts_cbranch_scc1 .Lck_loop%=\n.Lck_done%=:" \
      : "+&v"(w), "=&s"(t1_), "=&s"(t2_) : "v"(zvo), "s"(p), "i"(target) : "memory", "scc"); }
  #define CHECK_GE0(w, p, target) { unsigned t1_, t2_; asm volatile( \
      "s_nop 1\n\tv_readfirstlane_b32 %1, %0\n\ts_nop 0\n\ts_cmp_ge_u32 %1, %5\n\ts_cbranch_scc1 .Lcz_done%=\n\ts_mov_b32 %2, 0x400000\n" \
      ".Lcz_loop%=:\n\tglobal_load_dword %0, %3, %4 sc1\n\ts_waitcnt vmcnt(0)\n\tv_readfirstlane_b32 %1, %0\n\ts_nop 0\n\t" \
      "s_cmp_ge_u32 %1, %5\n\ts_cbranch_scc1 .Lcz_done%=\n\ts_sub_u32 %2, %2, 1\n\ts_cmp_lg_u32 %2, 0\n\ts_cbranch_scc1 .Lcz_loop%=\n.Lcz_done%=:" \
      : "+&v"(w), "=&s"(t1_), "=&s"(t2_) : "v"(zvo), "s"(p), "i"(target) : "memory", "scc"); }

const unsigned vA0 = rowA * 3456 + c16P1, vB0 = (rowA + 8) * 3456 + c16P1 - 1024;
const unsigned st1 = nq > 1 ? 3456u : 9216u; const char* const b1b = nq > 1 ? wqb + 128 : wgb;
const unsigned vA1 = rowA * st1 + c16P1, vB1 = (rowA + 8) * st1 + c16P1 - 1024;
asm volatile("s_mov_b32 m0, %0\n\ts_nop 0" :: "s"(wv2k + 0) : "memory"); GLDS(vA0, wqb + 0, 0); GLDS(vB0, wqb + 0, 1024);
asm volatile("s_mov_b32 m0, %0\n\ts_nop 0" :: "s"(wv1k + 73728) : "memory"); GLDS(xo0, xsrc + 0, 0);
asm volatile("s_mov_b32 m0, %0\n\ts_nop 0" :: "s"(wv1k + 77824) : "memory"); GLDS(xo0, xsrc + 1024, 0);
asm volatile("s_mov_b32 m0, %0\n\ts_nop 0" :: "s"(wv1k + 81920) : "memory"); GLDS(xo0, xsrc + 2048, 0);
asm volatile("s_mov_b32 m0, %0\n\ts_nop 0" :: "s"(wv1k + 86016) : "memory"); GLDS(xo0, xsrc + 3072, 0);
asm volatile("s_mov_b32 m0, %0\n\ts_nop 0" :: "s"(wv2k + 8192) : "memory"); GLDS(vA1, b1b + 0 * st1, 0); GLDS(vB1, b1b + 0 * st1, 1024);
asm volatile("s_mov_b32 m0, %0\n\ts_nop 0" :: "s"(wv2k + 16384) : "memory"); GLDS(vA0, wqb + 221184, 0); GLDS(vB0, wqb + 221184, 1024);
asm volatile("s_mov_b32 m0, %0\n\ts_nop 0" :: "s"(wv1k + 90112) : "memory"); GLDS(xo0, xsrc + 4096, 0);
asm volatile("s_mov_b32 m0, %0\n\ts_nop 0" :: "s"(wv1k + 94208) : "memory"); GLDS(xo0, xsrc + 5120, 0);
asm volatile("s_mov_b32 m0, %0\n\ts_nop 0" :: "s"(wv1k + 98304) : "memory"); GLDS(xo0, xsrc + 6144, 0);
asm volatile("s_mov_b32 m0, %0\n\ts_nop 0" :: "s"(wv1k + 102400) : "memory"); GLDS(xo0, xsrc + 7168, 0);
asm volatile("s_mov_b32 m0, %0\n\ts_nop 0" :: "s"(wv2k + 24576) : "memory"); GLDS(vA1, b1b + 64 * st1, 0); GLDS(vB1, b1b + 64 * st1, 1024);
asm volatile("s_mov_b32 m0, %0\n\ts_nop 0" :: "s"(wv2k + 32768) : "memory"); GLDS(vA0, wqb + 442368, 0); GLDS(vB0, wqb + 442368, 1024);
lbase = wqb + 884736; lvoffA = vA0; lvoffB = vB0;
asm volatile("s_waitcnt vmcnt(12)\n\ts_barrier" ::: "memory");
STAMP(1);
afl[0] = TR(lbA, 0); afh[0] = TR(lbA, 512);
afl[1] = TR(lbA, 2048); afh[1] = TR(lbA, 2560);
afl[2] = TR(lbA, 4096); afh[2] = TR(lbA, 4608);
xb[0][0] = *(AS3 u32x4*)(uintptr_t)(xrd + 0);
xb[1][0] = *(AS3 u32x4*)(uintptr_t)(xrd + 1024);
xb[0][1] = *(AS3 u32x4*)(uintptr_t)(xrd + 4096);
xb[1][1] = *(AS3 u32x4*)(uintptr_t)(xrd + 5120);
xb[0][2] = *(AS3 u32x4*)(uintptr_t)(xrd + 8192);
xb[1][2] = *(AS3 u32x4*)(uintptr_t)(xrd + 9216);
xb[0][3] = *(AS3 u32x4*)(uintptr_t)(xrd + 12288);
xb[1][3] = *(AS3 u32x4*)(uintptr_t)(xrd + 13312);
asm volatile("s_mov_b32 m0, %0\n\ts_nop 0" :: "s"(wv1k + 106496) : "memory"); GLDS(xo0, xsrc + 8192, 0);
asm volatile("s_mov_b32 m0, %0\n\ts_nop 0" :: "s"(wv1k + 110592) : "memory"); GLDS(xo0, xsrc + 9216, 0);
asm volatile("s_mov_b32 m0, %0\n\ts_nop 0" :: "s"(wv1k + 114688) : "memory"); GLDS(xo0, xsrc + 10240, 0);
asm volatile("s_mov_b32 m0, %0\n\ts_nop 0" :: "s"(wv1k + 118784) : "memory"); GLDS(xo0, xsrc + 11264, 0);
asm volatile("s_mov_b32 m0, %0\n\ts_nop 0" :: "s"(wv2k + 40960) : "memory"); GLDS(vA1, b1b + 128 * st1, 0); GLDS(vB1, b1b + 128 * st1, 1024);
asm volatile("s_mov_b32 m0, %0\n\ts_nop 0" :: "s"(wv2k + 49152) : "memory"); GLDS(vA0, wqb + 663552, 0); GLDS(vB0, wqb + 663552, 1024);
{
const char* nbase = nq == 3 ? wqb + 256 : (nq == 2 ? wgb : wub); const unsigned nstride = nq == 3 ? 3456u : 9216u;
SB();
asm volatile("s_waitcnt vmcnt(18)\n\ts_barrier" : "+a"(xb[0][0]), "+a"(xb[1][0]), "+a"(xb[0][1]), "+a"(xb[1][1]), "+a"(xb[0][2]), "+a"(xb[1][2]), "+a"(xb[0][3]), "+a"(xb[1][3]) :: "memory");
SB(); M32ZA(Y[0], AF(0), xb[0][0]); SB();
afl[3] = TR(lbA, 6144);
asm volatile("s_mov_b32 m0, %0\n\ts_nop 0" :: "s"(wv1k + 122880) : "memory"); GLDS(xo0, xsrc + 12288, 0);
asm volatile("s_mov_b32 m0, %0\n\ts_nop 0" :: "s"(wv1k + 126976) : "memory"); GLDS(xo0, xsrc + 13312, 0);
SB(); M32ZA(Y[1], AF(0), xb[1][0]); SB();
afh[3] = TR(lbA, 6656);
asm volatile("s_mov_b32 m0, %0\n\ts_nop 0" :: "s"(wv1k + 131072) : "memory"); GLDS(xo0, xsrc + 14336, 0);
asm volatile("s_mov_b32 m0, %0\n\ts_nop 0" :: "s"(wv1k + 135168) : "memory"); GLDS(xo0, xsrc + 15360, 0);
asm volatile("s_mov_b32 m0, %0\n\ts_nop 0" :: "s"(wv2k + 57344) : "memory"); GLDS(vA1, b1b + 192 * st1, 0); GLDS(vB1, b1b + 192 * st1, 1024);
SB(); M32A(Y[0], AF(1), xb[0][1]); SB();
afl[0] = TR(lbA, 8192);
SETM0(wv2k + 65536);
SB(); M32A(Y[1], AF(1), xb[1][1]); SB();
afh[0] = TR(lbA, 8704);
GLDS(lvoffA, lbase, 0);
SB(); M32A(Y[0], AF(2), xb[0][2]); SB();
afl[1] = TR(lbA, 10240);
SB(); M32A(Y[1], AF(2), xb[1][2]); SB();
afh[1] = TR(lbA, 10752);
SB(); M32A(Y[0], AF(3), xb[0][3]); SB();
afl[2] = TR(lbA, 12288);
GLDS(lvoffB, lbase, 1024);
SB(); M32A(Y[1], AF(3), xb[1][3]); SB();
afh[2] = TR(lbA, 12800);
lbase = b1b + 256 * st1; lvoffA = vA1; lvoffB = vB1;
SB();
asm volatile("s_waitcnt vmcnt(20)\n\ts_barrier" ::: "memory");
SB(); M32ZA(X[0], AF(0), xb[0][0]); SB();
afl[3] = TR(lbA, 14336);
asm volatile("s_mov_b32 m0, %0\n\ts_nop 0" :: "s"(wv1k + 139264) : "memory"); GLDS(xo0, xsrc + 16384, 0);
SB(); M32ZA(X[1], AF(0), xb[1][0]); SB();
afh[3] = TR(lbA, 14848);
asm volatile("s_mov_b32 m0, %0\n\ts_nop 0" :: "s"(wv1k + 143360) : "memory"); GLDS(xo0, xsrc + 17408, 0);
xb[0][4] = *(AS3 u32x4*)(uintptr_t)(xrd + 16384);
xb[1][4] = *(AS3 u32x4*)(uintptr_t)(xrd + 17408);
SB(); M32A(X[0], AF(1), xb[0][1]); SB();
afl[0] = TR(lbA, 16384);
SETM0(wv2k + 0);
xb[0][5] = *(AS3 u32x4*)(uintptr_t)(xrd + 20480);
xb[1][5] = *(AS3 u32x4*)(uintptr_t)(xrd + 21504);
SB(); M32A(X[1], AF(1), xb[1][1]); SB();
afh[0] = TR(lbA, 16896);
GLDS(lvoffA, lbase, 0);
xb[0][6] = *(AS3 u32x4*)(uintptr_t)(xrd + 24576);
SB(); M32A(X[0], AF(2), xb[0][2]); SB();
afl[1] = TR(lbA, 18432);
xb[1][6] = *(AS3 u32x4*)(uintptr_t)(xrd + 25600);
xb[0][7] = *(AS3 u32x4*)(uintptr_t)(xrd + 28672);
SB(); M32A(X[1], AF(2), xb[1][2]); SB();
afh[1] = TR(lbA, 18944);
xb[1][7] = *(AS3 u32x4*)(uintptr_t)(xrd + 29696);
SB(); M32A(X[0], AF(3), xb[0][3]); SB();
afl[2] = TR(lbA, 20480);
GLDS(lvoffB, lbase, 1024);
SB(); M32A(X[1], AF(3), xb[1][3]); SB();
afh[2] = TR(lbA, 20992);
lbase = wqb + 1105920; lvoffA = vA0; lvoffB = vB0;
SB();
asm volatile("s_waitcnt vmcnt(22)\n\ts_barrier" : "+a"(xb[0][4]), "+a"(xb[1][4]), "+a"(xb[0][5]), "+a"(xb[1][5]), "+a"(xb[0][6]), "+a"(xb[1][6]), "+a"(xb[0][7]), "+a"(xb[1][7]) :: "memory");
SB(); M32A(Y[0], AF(0), xb[0][4]); SB();
afl[3] = TR(lbA, 22528);
asm volatile("s_mov_b32 m0, %0\n\ts_nop 0" :: "s"(wv1k + 147456) : "memory"); GLDS(xo0, xsrc + 18432, 0);
SB(); M32A(Y[1], AF(0), xb[1][4]); SB();
afh[3] = TR(lbA, 23040);
asm volatile("s_mov_b32 m0, %0\n\ts_nop 0" :: "s"(wv1k + 151552) : "memory"); GLDS(xo0, xsrc + 19456, 0);
SB(); M32A(Y[0], AF(1), xb[0][5]); SB();
afl[0] = TR(lbA, 24576);
SETM0(wv2k + 8192);
SB(); M32A(Y[1], AF(1), xb[1][5]); SB();
afh[0] = TR(lbA, 25088);
GLDS(lvoffA, lbase, 0);
SB(); M32A(Y[0], AF(2), xb[0][6]); SB();
afl[1] = TR(lbA, 26624);
SB(); M32A(Y[1], AF(2), xb[1][6]); SB();
afh[1] = TR(lbA, 27136);
SB(); M32A(Y[0], AF(3), xb[0][7]); SB();
afl[2] = TR(lbA, 28672);
GLDS(lvoffB, lbase, 1024);
SB(); M32A(Y[1], AF(3), xb[1][7]); SB();
afh[2] = TR(lbA, 29184);
lbase = wqb + 1327104; lvoffA = vA0; lvoffB = vB0;
  {
    unsigned off = ((unsigned)((m * 2 + TYPE) * 4 + wave) & 15u) * 8192u + (unsigned)lane * 128u;
    off = off < code_len ? off : 0u;
    asm volatile("s_mov_b32 m0, %2\n\ts_nop 4\n\tglobal_load_lds_dword %0, %1" :: "v"(off), "s"(code_base), "s"(wv1k + H_OFF + 4 * 4096) : "memory");
  }
SB();
asm volatile("s_waitcnt vmcnt(21)\n\ts_barrier" ::: "memory");
SB(); M32A(X[0], AF(0), xb[0][4]); SB();
afl[3] = TR(lbA, 30720);
asm volatile("s_mov_b32 m0, %0\n\ts_nop 0" :: "s"(wv1k + 155648) : "memory"); GLDS(xo0, xsrc + 20480, 0);
SB(); M32A(X[1], AF(0), xb[1][4]); SB();
afh[3] = TR(lbA, 31232);
asm volatile("s_mov_b32 m0, %0\n\ts_nop 0" :: "s"(wv1k + 159744) : "memory"); GLDS(xo0, xsrc + 21504, 0);
xb[0][8] = *(AS3 u32x4*)(uintptr_t)(xrd + 32768);
xb[1][8] = *(AS3 u32x4*)(uintptr_t)(xrd + 33792);
SB(); M32A(X[0], AF(1), xb[0][5]); SB();
afl[0] = TR(lbA, 32768);
SETM0(wv2k + 16384);
xb[0][9] = *(AS3 u32x4*)(uintptr_t)(xrd + 36864);
xb[1][9] = *(AS3 u32x4*)(uintptr_t)(xrd + 37888);
SB(); M32A(X[1], AF(1), xb[1][5]); SB();
afh[0] = TR(lbA, 33280);
GLDS(lvoffA, lbase, 0);
xb[0][10] = *(AS3 u32x4*)(uintptr_t)(xrd + 40960);
SB(); M32A(X[0], AF(2), xb[0][6]); SB();
afl[1] = TR(lbA, 34816);
xb[1][10] = *(AS3 u32x4*)(uintptr_t)(xrd + 41984);
xb[0][11] = *(AS3 u32x4*)(uintptr_t)(xrd + 45056);
SB(); M32A(X[1], AF(2), xb[1][6]); SB();
afh[1] = TR(lbA, 35328);
xb[1][11] = *(AS3 u32x4*)(uintptr_t)(xrd + 46080);
SB(); M32A(X[0], AF(3), xb[0][7]); SB();
afl[2] = TR(lbA, 36864);
GLDS(lvoffB, lbase, 1024);
SB(); M32A(X[1], AF(3), xb[1][7]); SB();
afh[2] = TR(lbA, 37376);
lbase = b1b + 320 * st1; lvoffA = vA1; lvoffB = vB1;
SB();
asm volatile("s_waitcnt vmcnt(23)\n\ts_barrier" : "+a"(xb[0][8]), "+a"(xb[1][8]), "+a"(xb[0][9]), "+a"(xb[1][9]), "+a"(xb[0][10]), "+a"(xb[1][10]), "+a"(xb[0][11]), "+a"(xb[1][11]) :: "memory");
SB(); M32A(Y[0], AF(0), xb[0][8]); SB();
afl[3] = TR(lbA, 38912);
asm volatile("s_mov_b32 m0, %0\n\ts_nop 0" :: "s"(wv1k + 73728) : "memory"); GLDS(xo0, xsrc + 22528, 0);
SB(); M32A(Y[1], AF(0), xb[1][8]); SB();
afh[3] = TR(lbA, 39424);
asm volatile("s_mov_b32 m0, %0\n\ts_nop 0" :: "s"(wv1k + 77824) : "memory"); GLDS(xo0, xsrc + 23552, 0);
SB(); M32A(Y[0], AF(1), xb[0][9]); SB();
afl[0] = TR(lbA, 40960);
SETM0(wv2k + 24576);
SB(); M32A(Y[1], AF(1), xb[1][9]); SB();
afh[0] = TR(lbA, 41472);
GLDS(lvoffA, lbase, 0);
SB(); M32A(Y[0], AF(2), xb[0][10]); SB();
afl[1] = TR(lbA, 43008);
SB(); M32A(Y[1], AF(2), xb[1][10]); SB();
afh[1] = TR(lbA, 43520);
SB(); M32A(Y[0], AF(3), xb[0][11]); SB();
afl[2] = TR(lbA, 45056);
GLDS(lvoffB, lbase, 1024);
SB(); M32A(Y[1], AF(3), xb[1][11]); SB();
afh[2] = TR(lbA, 45568);
lbase = wqb + 1548288; lvoffA = vA0; lvoffB = vB0;
SB();
asm volatile("s_waitcnt vmcnt(21)\n\ts_barrier" ::: "memory");
SB(); M32A(X[0], AF(0), xb[0][8]); SB();
afl[3] = TR(lbA, 47104);
asm volatile("s_mov_b32 m0, %0\n\ts_nop 0" :: "s"(wv1k + 81920) : "memory"); GLDS(xo0, xsrc + 24576, 0);
SB(); M32A(X[1], AF(0), xb[1][8]); SB();
afh[3] = TR(lbA, 47616);
asm volatile("s_mov_b32 m0, %0\n\ts_nop 0" :: "s"(wv1k + 86016) : "memory"); GLDS(xo0, xsrc + 25600, 0);
xb[0][12] = *(AS3 u32x4*)(uintptr_t)(xrd + 49152);
xb[1][12] = *(AS3 u32x4*)(uintptr_t)(xrd + 50176);
SB(); M32A(X[0], AF(1), xb[0][9]); SB();
afl[0] = TR(lbA, 49152);
SETM0(wv2k + 32768);
xb[0][13] = *(AS3 u32x4*)(uintptr_t)(xrd + 53248);
xb[1][13] = *(AS3 u32x4*)(uintptr_t)(xrd + 54272);
SB(); M32A(X[1], AF(1), xb[1][9]); SB();
afh[0] = TR(lbA, 49664);
GLDS(lvoffA, lbase, 0);
xb[0][14] = *(AS3 u32x4*)(uintptr_t)(xrd + 57344);
SB(); M32A(X[0], AF(2), xb[0][10]); SB();
afl[1] = TR(lbA, 51200);
xb[1][14] = *(AS3 u32x4*)(uintptr_t)(xrd + 58368);
xb[0][15] = *(AS3 u32x4*)(uintptr_t)(xrd + 61440);
SB(); M32A(X[1], AF(2), xb[1][10]); SB();
afh[1] = TR(lbA, 51712);
xb[1][15] = *(AS3 u32x4*)(uintptr_t)(xrd + 62464);
SB(); M32A(X[0], AF(3), xb[0][11]); SB();
afl[2] = TR(lbA, 53248);
GLDS(lvoffB, lbase, 1024);
SB(); M32A(X[1], AF(3), xb[1][11]); SB();
afh[2] = TR(lbA, 53760);
lbase = wqb + 1769472; lvoffA = vA0; lvoffB = vB0;
SB();
asm volatile("s_waitcnt vmcnt(23)\n\ts_barrier" : "+a"(xb[0][12]), "+a"(xb[1][12]), "+a"(xb[0][13]), "+a"(xb[1][13]), "+a"(xb[0][14]), "+a"(xb[1][14]), "+a"(xb[0][15]), "+a"(xb[1][15]) :: "memory");
SB(); M32A(Y[0], AF(0), xb[0][12]); SB();
afl[3] = TR(lbA, 55296);
asm volatile("s_mov_b32 m0, %0\n\ts_nop 0" :: "s"(wv1k + 90112) : "memory"); GLDS(xo0, xsrc + 26624, 0);
SB(); M32A(Y[1], AF(0), xb[1][12]); SB();
afh[3] = TR(lbA, 55808);
asm volatile("s_mov_b32 m0, %0\n\ts_nop 0" :: "s"(wv1k + 94208) : "memory"); GLDS(xo0, xsrc + 27648, 0);
SB(); M32A(Y[0], AF(1), xb[0][13]); SB();
afl[0] = TR(lbA, 57344);
SETM0(wv2k + 40960);
SB(); M32A(Y[1], AF(1), xb[1][13]); SB();
afh[0] = TR(lbA, 57856);
GLDS(lvoffA, lbase, 0);
SB(); M32A(Y[0], AF(2), xb[0][14]); SB();
afl[1] = TR(lbA, 59392);
SB(); M32A(Y[1], AF(2), xb[1][14]); SB();
afh[1] = TR(lbA, 59904);
SB(); M32A(Y[0], AF(3), xb[0][15]); SB();
afl[2] = TR(lbA, 61440);
GLDS(lvoffB, lbase, 1024);
SB(); M32A(Y[1], AF(3), xb[1][15]); SB();
afh[2] = TR(lbA, 61952);
lbase = b1b + 384 * st1; lvoffA = vA1; lvoffB = vB1;
SB();
asm volatile("s_waitcnt vmcnt(19)\n\ts_barrier" ::: "memory");
SB(); M32A(X[0], AF(0), xb[0][12]); SB();
afl[3] = TR(lbA, 63488);
asm volatile("s_mov_b32 m0, %0\n\ts_nop 0" :: "s"(wv1k + 98304) : "memory"); GLDS(xo0, xsrc + 28672, 0);
SB(); M32A(X[1], AF(0), xb[1][12]); SB();
afh[3] = TR(lbA, 64000);
asm volatile("s_mov_b32 m0, %0\n\ts_nop 0" :: "s"(wv1k + 102400) : "memory"); GLDS(xo0, xsrc + 29696, 0);
xb[0][16] = *(AS3 u32x4*)(uintptr_t)(xrd + 65536);
xb[1][16] = *(AS3 u32x4*)(uintptr_t)(xrd + 66560);
SB(); M32A(X[0], AF(1), xb[0][13]); SB();
afl[0] = TR(lbA, 65536);
SETM0(wv2k + 49152);
xb[0][17] = *(AS3 u32x4*)(uintptr_t)(xrd + 69632);
xb[1][17] = *(AS3 u32x4*)(uintptr_t)(xrd + 70656);
SB(); M32A(X[1], AF(1), xb[1][13]); SB();
afh[0] = TR(lbA, 66048);
GLDS(lvoffA, lbase, 0);
xb[0][18] = *(AS3 u32x4*)(uintptr_t)(xrd + 73728);
SB(); M32A(X[0], AF(2), xb[0][14]); SB();
afl[1] = TR(lbA, 67584);
xb[1][18] = *(AS3 u32x4*)(uintptr_t)(xrd + 74752);
xb[0][19] = *(AS3 u32x4*)(uintptr_t)(xrd + 77824);
SB(); M32A(X[1], AF(2), xb[1][14]); SB();
afh[1] = TR(lbA, 68096);
xb[1][19] = *(AS3 u32x4*)(uintptr_t)(xrd + 78848);
SB(); M32A(X[0], AF(3), xb[0][15]); SB();
afl[2] = TR(lbA, 69632);
GLDS(lvoffB, lbase, 1024);
SB(); M32A(X[1], AF(3), xb[1][15]); SB();
afh[2] = TR(lbA, 70144);
lbase = b1b + 448 * st1; lvoffA = vA1; lvoffB = vB1;
SB();
asm volatile("s_waitcnt vmcnt(25)\n\ts_barrier" : "+a"(xb[0][16]), "+a"(xb[1][16]), "+a"(xb[0][17]), "+a"(xb[1][17]), "+a"(xb[0][18]), "+a"(xb[1][18]), "+a"(xb[0][19]), "+a"(xb[1][19]) :: "memory");
SB(); M32A(Y[0], AF(0), xb[0][16]); SB();
afl[3] = TR(lbA, 71680);
asm volatile("s_mov_b32 m0, %0\n\ts_nop 0" :: "s"(wv1k + 106496) : "memory"); GLDS(xo0, xsrc + 30720, 0);
SB(); M32A(Y[1], AF(0), xb[1][16]); SB();
afh[3] = TR(lbA, 72192);
asm volatile("s_mov_b32 m0, %0\n\ts_nop 0" :: "s"(wv1k + 110592) : "memory"); GLDS(xo0, xsrc + 31744, 0);
SB(); M32A(Y[0], AF(1), xb[0][17]); SB();
afl[0] = TR(lbA, 0);
SETM0(wv2k + 57344);
SB(); M32A(Y[1], AF(1), xb[1][17]); SB();
afh[0] = TR(lbA, 512);
GLDS(lvoffA, lbase, 0);
SB(); M32A(Y[0], AF(2), xb[0][18]); SB();
afl[1] = TR(lbA, 2048);
SB(); M32A(Y[1], AF(2), xb[1][18]); SB();
afh[1] = TR(lbA, 2560);
SB(); M32A(Y[0], AF(3), xb[0][19]); SB();
afl[2] = TR(lbA, 4096);
GLDS(lvoffB, lbase, 1024);
SB(); M32A(Y[1], AF(3), xb[1][19]); SB();
afh[2] = TR(lbA, 4608);
lbase = b1b + 512 * st1; lvoffA = vA1; lvoffB = vB1;
STAMP(2);
SB();
asm volatile("s_waitcnt vmcnt(18)\n\ts_barrier" ::: "memory");
SB(); M32A(X[0], AF(0), xb[0][16]); SB();
afl[3] = TR(lbA, 6144);
asm volatile("s_mov_b32 m0, %0\n\ts_nop 0" :: "s"(wv1k + 114688) : "memory"); GLDS(xo0, xsrc + 32768, 0);
SB(); M32A(X[1], AF(0), xb[1][16]); SB();
afh[3] = TR(lbA, 6656);
asm volatile("s_mov_b32 m0, %0\n\ts_nop 0" :: "s"(wv1k + 118784) : "memory"); GLDS(xo0, xsrc + 33792, 0);
xb[0][20] = *(AS3 u32x4*)(uintptr_t)(xrd + 81920);
xb[1][20] = *(AS3 u32x4*)(uintptr_t)(xrd + 82944);
SB(); M32A(X[0], AF(1), xb[0][17]); SB();
afl[0] = TR(lbA, 8192);
SETM0(wv2k + 65536);
xb[0][21] = *(AS3 u32x4*)(uintptr_t)(xrd + 86016);
xb[1][21] = *(AS3 u32x4*)(uintptr_t)(xrd + 87040);
SB(); M32A(X[1], AF(1), xb[1][17]); SB();
afh[0] = TR(lbA, 8704);
GLDS(lvoffA, lbase, 0);
xb[0][22] = *(AS3 u32x4*)(uintptr_t)(xrd + 0);
SB(); M32A(X[0], AF(2), xb[0][18]); SB();
afl[1] = TR(lbA, 10240);
xb[1][22] = *(AS3 u32x4*)(uintptr_t)(xrd + 1024);
xb[0][23] = *(AS3 u32x4*)(uintptr_t)(xrd + 4096);
SB(); M32A(X[1], AF(2), xb[1][18]); SB();
afh[1] = TR(lbA, 10752);
xb[1][23] = *(AS3 u32x4*)(uintptr_t)(xrd + 5120);
SB(); M32A(X[0], AF(3), xb[0][19]); SB();
afl[2] = TR(lbA, 12288);
GLDS(lvoffB, lbase, 1024);
SB(); M32A(X[1], AF(3), xb[1][19]); SB();
afh[2] = TR(lbA, 12800);
lbase = nbase; lstep = 64 * nstride; lvoffA = rowA * nstride + c16P1; lvoffB = (rowA + 8) * nstride + c16P1 - 1024;
SB();
asm volatile("s_waitcnt vmcnt(14)\n\ts_barrier" : "+a"(xb[0][20]), "+a"(xb[1][20]), "+a"(xb[0][21]), "+a"(xb[1][21]), "+a"(xb[0][22]), "+a"(xb[1][22]), "+a"(xb[0][23]), "+a"(xb[1][23]) :: "memory");
SB(); M32A(Y[0], AF(0), xb[0][20]); SB();
afl[3] = TR(lbA, 14336);
asm volatile("s_mov_b32 m0, %0\n\ts_nop 0" :: "s"(wv1k + 122880) : "memory"); GLDS(xo0, xsrc + 34816, 0);
SB(); M32A(Y[1], AF(0), xb[1][20]); SB();
afh[3] = TR(lbA, 14848);
asm volatile("s_mov_b32 m0, %0\n\ts_nop 0" :: "s"(wv1k + 126976) : "memory"); GLDS(xo0, xsrc + 35840, 0);
xb[0][24] = *(AS3 u32x4*)(uintptr_t)(xrd + 8192);
xb[1][24] = *(AS3 u32x4*)(uintptr_t)(xrd + 9216);
SB(); M32A(Y[0], AF(1), xb[0][21]); SB();
afl[0] = TR(lbA, 16384);
SETM0(wv2k + 0);
xb[0][25] = *(AS3 u32x4*)(uintptr_t)(xrd + 12288);
xb[1][25] = *(AS3 u32x4*)(uintptr_t)(xrd + 13312);
SB(); M32A(Y[1], AF(1), xb[1][21]); SB();
afh[0] = TR(lbA, 16896);
GLDS(lvoffA, lbase, 0);
xb[0][26] = *(AS3 u32x4*)(uintptr_t)(xrd + 16384);
SB(); M32A(Y[0], AF(2), xb[0][22]); SB();
afl[1] = TR(lbA, 18432);
xb[1][26] = *(AS3 u32x4*)(uintptr_t)(xrd + 17408);
xb[0][27] = *(AS3 u32x4*)(uintptr_t)(xrd + 20480);
SB(); M32A(Y[1], AF(2), xb[1][22]); SB();
afh[1] = TR(lbA, 18944);
xb[1][27] = *(AS3 u32x4*)(uintptr_t)(xrd + 21504);
SB(); M32A(Y[0], AF(3), xb[0][23]); SB();
afl[2] = TR(lbA, 20480);
GLDS(lvoffB, lbase, 1024);
SB(); M32A(Y[1], AF(3), xb[1][23]); SB();
afh[2] = TR(lbA, 20992);
lbase += lstep;
SB();
asm volatile("s_waitcnt vmcnt(24)\n\ts_barrier" : "+a"(xb[0][24]), "+a"(xb[1][24]), "+a"(xb[0][25]), "+a"(xb[1][25]), "+a"(xb[0][26]), "+a"(xb[1][26]), "+a"(xb[0][27]), "+a"(xb[1][27]) :: "memory");
SB(); M32A(Y[0], AF(0), xb[0][24]); SB();
afl[3] = TR(lbA, 22528);
SB(); M32A(Y[1], AF(0), xb[1][24]); SB();
afh[3] = TR(lbA, 23040);
SB(); M32A(Y[0], AF(1), xb[0][25]); SB();
afl[0] = TR(lbA, 24576);
SETM0(wv2k + 8192);
SB(); M32A(Y[1], AF(1), xb[1][25]); SB();
afh[0] = TR(lbA, 25088);
GLDS(lvoffA, lbase, 0);
SB(); M32A(Y[0], AF(2), xb[0][26]); SB();
afl[1] = TR(lbA, 26624);
SB(); M32A(Y[1], AF(2), xb[1][26]); SB();
afh[1] = TR(lbA, 27136);
SB(); M32A(Y[0], AF(3), xb[0][27]); SB();
afl[2] = TR(lbA, 28672);
GLDS(lvoffB, lbase, 1024);
SB(); M32A(Y[1], AF(3), xb[1][27]); SB();
afh[2] = TR(lbA, 29184);
lbase += lstep;
SB();
asm volatile("s_waitcnt vmcnt(12)\n\ts_barrier" ::: "memory");
SB(); M32A(X[0], AF(0), xb[0][20]); SB();
afl[3] = TR(lbA, 30720);
SB(); M32A(X[1], AF(0), xb[1][20]); SB();
afh[3] = TR(lbA, 31232);
xb[0][28] = *(AS3 u32x4*)(uintptr_t)(xrd + 24576);
xb[1][28] = *(AS3 u32x4*)(uintptr_t)(xrd + 25600);
SB(); M32A(X[0], AF(1), xb[0][21]); SB();
afl[0] = TR(lbA, 32768);
SETM0(wv2k + 16384);
xb[0][29] = *(AS3 u32x4*)(uintptr_t)(xrd + 28672);
xb[1][29] = *(AS3 u32x4*)(uintptr_t)(xrd + 29696);
SB(); M32A(X[1], AF(1), xb[1][21]); SB();
afh[0] = TR(lbA, 33280);
GLDS(lvoffA, lbase, 0);
xb[0][30] = *(AS3 u32x4*)(uintptr_t)(xrd + 32768);
SB(); M32A(X[0], AF(2), xb[0][22]); SB();
afl[1] = TR(lbA, 34816);
xb[1][30] = *(AS3 u32x4*)(uintptr_t)(xrd + 33792);
xb[0][31] = *(AS3 u32x4*)(uintptr_t)(xrd + 36864);
SB(); M32A(X[1], AF(2), xb[1][22]); SB();
afh[1] = TR(lbA, 35328);
xb[1][31] = *(AS3 u32x4*)(uintptr_t)(xrd + 37888);
SB(); M32A(X[0], AF(3), xb[0][23]); SB();
afl[2] = TR(lbA, 36864);
GLDS(lvoffB, lbase, 1024);
SB(); M32A(X[1], AF(3), xb[1][23]); SB();
afh[2] = TR(lbA, 37376);
lbase += lstep;
SB();
asm volatile("s_waitcnt vmcnt(6)\n\ts_barrier" : "+a"(xb[0][28]), "+a"(xb[1][28]), "+a"(xb[0][29]), "+a"(xb[1][29]), "+v"(xb[0][30]), "+v"(xb[1][30]), "+v"(xb[0][31]), "+v"(xb[1][31]) :: "memory");
SB(); M32A(Y[0], AF(0), xb[0][28]); SB();
afl[3] = TR(lbA, 38912);
SB(); M32A(Y[1], AF(0), xb[1][28]); SB();
afh[3] = TR(lbA, 39424);
xb[0][32] = *(AS3 u32x4*)(uintptr_t)(xrd + 40960);
xb[1][32] = *(AS3 u32x4*)(uintptr_t)(xrd + 41984);
SB(); M32A(Y[0], AF(1), xb[0][29]); SB();
afl[0] = TR(lbA, 40960);
SETM0(wv2k + 24576);
xb[0][33] = *(AS3 u32x4*)(uintptr_t)(xrd + 45056);
xb[1][33] = *(AS3 u32x4*)(uintptr_t)(xrd + 46080);
SB(); M32A(Y[1], AF(1), xb[1][29]); SB();
afh[0] = TR(lbA, 41472);
GLDS(lvoffA, lbase, 0);
xb[0][34] = *(AS3 u32x4*)(uintptr_t)(xrd + 49152);
SB(); M32V(Y[0], AF(2), xb[0][30]); SB();
afl[1] = TR(lbA, 43008);
xb[1][34] = *(AS3 u32x4*)(uintptr_t)(xrd + 50176);
xb[0][35] = *(AS3 u32x4*)(uintptr_t)(xrd + 53248);
SB(); M32V(Y[1], AF(2), xb[1][30]); SB();
afh[1] = TR(lbA, 43520);
xb[1][35] = *(AS3 u32x4*)(uintptr_t)(xrd + 54272);
SB(); M32V(Y[0], AF(3), xb[0][31]); SB();
afl[2] = TR(lbA, 45056);
GLDS(lvoffB, lbase, 1024);
SB(); M32V(Y[1], AF(3), xb[1][31]); SB();
afh[2] = TR(lbA, 45568);
lbase += lstep;
SB();
asm volatile("s_waitcnt vmcnt(18)\n\ts_barrier" : "+v"(xb[0][32]), "+v"(xb[1][32]), "+v"(xb[0][33]), "+v"(xb[1][33]), "+v"(xb[0][34]), "+v"(xb[1][34]), "+v"(xb[0][35]), "+v"(xb[1][35]) :: "memory");
SB(); M32V(Y[0], AF(0), xb[0][32]); SB();
afl[3] = TR(lbA, 47104);
SB(); M32V(Y[1], AF(0), xb[1][32]); SB();
afh[3] = TR(lbA, 47616);
SB(); M32V(Y[0], AF(1), xb[0][33]); SB();
afl[0] = TR(lbA, 49152);
SETM0(wv2k + 32768);
SB(); M32V(Y[1], AF(1), xb[1][33]); SB();
afh[0] = TR(lbA, 49664);
GLDS(lvoffA, lbase, 0);
SB(); M32V(Y[0], AF(2), xb[0][34]); SB();
afl[1] = TR(lbA, 51200);
SB(); M32V(Y[1], AF(2), xb[1][34]); SB();
afh[1] = TR(lbA, 51712);
SB(); M32V(Y[0], AF(3), xb[0][35]); SB();
afl[2] = TR(lbA, 53248);
GLDS(lvoffB, lbase, 1024);
SB(); M32V(Y[1], AF(3), xb[1][35]); SB();
afh[2] = TR(lbA, 53760);
lbase += lstep;
SB();
asm volatile("s_waitcnt vmcnt(16)\n\ts_barrier" ::: "memory");
SB(); M32A(X[0], AF(0), xb[0][24]); SB();
afl[3] = TR(lbA, 55296);
SB(); M32A(X[1], AF(0), xb[1][24]); SB();
afh[3] = TR(lbA, 55808);
SB(); M32A(X[0], AF(1), xb[0][25]); SB();
afl[0] = TR(lbA, 57344);
SETM0(wv2k + 40960);
SB(); M32A(X[1], AF(1), xb[1][25]); SB();
afh[0] = TR(lbA, 57856);
GLDS(lvoffA, lbase, 0);
SB(); M32A(X[0], AF(2), xb[0][26]); SB();
afl[1] = TR(lbA, 59392);
SB(); M32A(X[1], AF(2), xb[1][26]); SB();
afh[1] = TR(lbA, 59904);
SB(); M32A(X[0], AF(3), xb[0][27]); SB();
afl[2] = TR(lbA, 61440);
GLDS(lvoffB, lbase, 1024);
{ unsigned u = pk_bf16(Y[0][0], Y[0][1]); PIN(u); h2[0] = u; }
{ unsigned u = pk_bf16(Y[0][2], Y[0][3]); PIN(u); h2[1] = u; }
SB(); M32A(X[1], AF(3), xb[1][27]); SB();
afh[2] = TR(lbA, 61952);
lbase += lstep;
SB();
asm volatile("s_waitcnt vmcnt(14)\n\ts_barrier" ::: "memory");
SB(); M32A(X[0], AF(0), xb[0][28]); SB();
afl[3] = TR(lbA, 63488);
ST21(qvo, h2, qdst + (qp0) * 16384 + 0);
{ unsigned u = pk_bf16(Y[0][4], Y[0][5]); PIN(u); h2[0] = u; }
SB(); M32A(X[1], AF(0), xb[1][28]); SB();
afh[3] = TR(lbA, 64000);
{ unsigned u = pk_bf16(Y[0][6], Y[0][7]); PIN(u); h2[1] = u; }
ST21(qvo, h2, qdst + (qp0) * 16384 + 2048);
SB(); M32A(X[0], AF(1), xb[0][29]); SB();
afl[0] = TR(lbA, 65536);
SETM0(wv2k + 49152);
{ unsigned u = pk_bf16(Y[0][8], Y[0][9]); PIN(u); h2[0] = u; }
{ unsigned u = pk_bf16(Y[0][10], Y[0][11]); PIN(u); h2[1] = u; }
SB(); M32A(X[1], AF(1), xb[1][29]); SB();
afh[0] = TR(lbA, 66048);
GLDS(lvoffA, lbase, 0);
SB(); M32V(X[0], AF(2), xb[0][30]); SB();
afl[1] = TR(lbA, 67584);
ST21(qvo, h2, qdst + (qp0) * 16384 + 4096);
{ unsigned u = pk_bf16(Y[0][12], Y[0][13]); PIN(u); h2[0] = u; }
SB(); M32V(X[1], AF(2), xb[1][30]); SB();
afh[1] = TR(lbA, 68096);
{ unsigned u = pk_bf16(Y[0][14], Y[0][15]); PIN(u); h2[1] = u; }
ST21(qvo, h2, qdst + (qp0) * 16384 + 6144);
SB(); M32V(X[0], AF(3), xb[0][31]); SB();
afl[2] = TR(lbA, 69632);
GLDS(lvoffB, lbase, 1024);
{ unsigned u = pk_bf16(Y[1][0], Y[1][1]); PIN(u); h2[0] = u; }
{ unsigned u = pk_bf16(Y[1][2], Y[1][3]); PIN(u); h2[1] = u; }
SB(); M32V(X[1], AF(3), xb[1][31]); SB();
afh[2] = TR(lbA, 70144);
lbase += lstep;
SB();
asm volatile("s_waitcnt vmcnt(16)\n\ts_barrier" ::: "memory");
SB(); M32V(X[0], AF(0), xb[0][32]); SB();
afl[3] = TR(lbA, 71680);
ST21(qvo, h2, qdst + (qp0) * 16384 + 256);
{ unsigned u = pk_bf16(Y[1][4], Y[1][5]); PIN(u); h2[0] = u; }
SB(); M32V(X[1], AF(0), xb[1][32]); SB();
afh[3] = TR(lbA, 72192);
{ unsigned u = pk_bf16(Y[1][6], Y[1][7]); PIN(u); h2[1] = u; }
ST21(qvo, h2, qdst + (qp0) * 16384 + 2304);
SB(); M32V(X[0], AF(1), xb[0][33]); SB();
afl[0] = TR(lbA, 0);
SETM0(wv2k + 57344);
{ unsigned u = pk_bf16(Y[1][8], Y[1][9]); PIN(u); h2[0] = u; }
{ unsigned u = pk_bf16(Y[1][10], Y[1][11]); PIN(u); h2[1] = u; }
SB(); M32V(X[1], AF(1), xb[1][33]); SB();
afh[0] = TR(lbA, 512);
GLDS(lvoffA, lbase, 0);
SB(); M32V(X[0], AF(2), xb[0][34]); SB();
afl[1] = TR(lbA, 2048);
ST21(qvo, h2, qdst + (qp0) * 16384 + 4352);
{ unsigned u = pk_bf16(Y[1][12], Y[1][13]); PIN(u); h2[0] = u; }
SB(); M32V(X[1], AF(2), xb[1][34]); SB();
afh[1] = TR(lbA, 2560);
{ unsigned u = pk_bf16(Y[1][14], Y[1][15]); PIN(u); h2[1] = u; }
ST21(qvo, h2, qdst + (qp0) * 16384 + 6400);
SB(); M32V(X[0], AF(3), xb[0][35]); SB();
afl[2] = TR(lbA, 4096);
GLDS(lvoffB, lbase, 1024);
SB(); M32V(X[1], AF(3), xb[1][35]); SB();
afh[2] = TR(lbA, 4608);
lbase += lstep;
}
MNOPX(X);
if (nq > 1) {
if (nq == 3) {
{
const char* nbase = wgb; const unsigned nstride = 9216u;
SB();
asm volatile("s_waitcnt vmcnt(12)\n\ts_barrier" ::: "memory");
SB(); M32ZA(Y[0], AF(0), xb[0][0]); SB();
afl[3] = TR(lbA, 6144);
SB(); M32ZA(Y[1], AF(0), xb[1][0]); SB();
afh[3] = TR(lbA, 6656);
SB(); M32A(Y[0], AF(1), xb[0][1]); SB();
afl[0] = TR(lbA, 8192);
SETM0(wv2k + 65536);
SB(); M32A(Y[1], AF(1), xb[1][1]); SB();
afh[0] = TR(lbA, 8704);
GLDS(lvoffA, lbase, 0);
SB(); M32A(Y[0], AF(2), xb[0][2]); SB();
afl[1] = TR(lbA, 10240);
SB(); M32A(Y[1], AF(2), xb[1][2]); SB();
afh[1] = TR(lbA, 10752);
SB(); M32A(Y[0], AF(3), xb[0][3]); SB();
afl[2] = TR(lbA, 12288);
GLDS(lvoffB, lbase, 1024);
{ unsigned u = pk_bf16(X[0][0], X[0][1]); PIN(u); h2[0] = u; }
{ unsigned u = pk_bf16(X[0][2], X[0][3]); PIN(u); h2[1] = u; }
SB(); M32A(Y[1], AF(3), xb[1][3]); SB();
afh[2] = TR(lbA, 12800);
lbase = nbase; lstep = 64 * nstride; lvoffA = rowA * nstride + c16P1; lvoffB = (rowA + 8) * nstride + c16P1 - 1024;
SB();
asm volatile("s_waitcnt vmcnt(12)\n\ts_barrier" ::: "memory");
SB(); M32A(Y[0], AF(0), xb[0][4]); SB();
afl[3] = TR(lbA, 14336);
ST21(qvo, h2, qdst + (qp0 + 1) * 16384 + 0);
SB(); M32A(Y[1], AF(0), xb[1][4]); SB();
afh[3] = TR(lbA, 14848);
{ unsigned u = pk_bf16(X[0][4], X[0][5]); PIN(u); h2[0] = u; }
{ unsigned u = pk_bf16(X[0][6], X[0][7]); PIN(u); h2[1] = u; }
SB(); M32A(Y[0], AF(1), xb[0][5]); SB();
afl[0] = TR(lbA, 16384);
SETM0(wv2k + 0);
ST21(qvo, h2, qdst + (qp0 + 1) * 16384 + 2048);
SB(); M32A(Y[1], AF(1), xb[1][5]); SB();
afh[0] = TR(lbA, 16896);
GLDS(lvoffA, lbase, 0);
{ unsigned u = pk_bf16(X[0][8], X[0][9]); PIN(u); h2[0] = u; }
{ unsigned u = pk_bf16(X[0][10], X[0][11]); PIN(u); h2[1] = u; }
SB(); M32A(Y[0], AF(2), xb[0][6]); SB();
afl[1] = TR(lbA, 18432);
ST21(qvo, h2, qdst + (qp0 + 1) * 16384 + 4096);
SB(); M32A(Y[1], AF(2), xb[1][6]); SB();
afh[1] = TR(lbA, 18944);
{ unsigned u = pk_bf16(X[0][12], X[0][13]); PIN(u); h2[0] = u; }
{ unsigned u = pk_bf16(X[0][14], X[0][15]); PIN(u); h2[1] = u; }
SB(); M32A(Y[0], AF(3), xb[0][7]); SB();
afl[2] = TR(lbA, 20480);
GLDS(lvoffB, lbase, 1024);
ST21(qvo, h2, qdst + (qp0 + 1) * 16384 + 6144);
SB(); M32A(Y[1], AF(3), xb[1][7]); SB();
afh[2] = TR(lbA, 20992);
lbase += lstep;
SB();
asm volatile("s_waitcnt vmcnt(16)\n\ts_barrier" ::: "memory");
SB(); M32A(Y[0], AF(0), xb[0][8]); SB();
afl[3] = TR(lbA, 22528);
{ unsigned u = pk_bf16(X[1][0], X[1][1]); PIN(u); h2[0] = u; }
{ unsigned u = pk_bf16(X[1][2], X[1][3]); PIN(u); h2[1] = u; }
SB(); M32A(Y[1], AF(0), xb[1][8]); SB();
afh[3] = TR(lbA, 23040);
ST21(qvo, h2, qdst + (qp0 + 1) * 16384 + 256);
SB(); M32A(Y[0], AF(1), xb[0][9]); SB();
afl[0] = TR(lbA, 24576);
SETM0(wv2k + 8192);
{ unsigned u = pk_bf16(X[1][4], X[1][5]); PIN(u); h2[0] = u; }
{ unsigned u = pk_bf16(X[1][6], X[1][7]); PIN(u); h2[1] = u; }
SB(); M32A(Y[1], AF(1), xb[1][9]); SB();
afh[0] = TR(lbA, 25088);
GLDS(lvoffA, lbase, 0);
SB(); M32A(Y[0], AF(2), xb[0][10]); SB();
afl[1] = TR(lbA, 26624);
ST21(qvo, h2, qdst + (qp0 + 1) * 16384 + 2304);
SB(); M32A(Y[1], AF(2), xb[1][10]); SB();
afh[1] = TR(lbA, 27136);
{ unsigned u = pk_bf16(X[1][8], X[1][9]); PIN(u); h2[0] = u; }
{ unsigned u = pk_bf16(X[1][10], X[1][11]); PIN(u); h2[1] = u; }
SB(); M32A(Y[0], AF(3), xb[0][11]); SB();
afl[2] = TR(lbA, 28672);
GLDS(lvoffB, lbase, 1024);
ST21(qvo, h2, qdst + (qp0 + 1) * 16384 + 4352);
SB(); M32A(Y[1], AF(3), xb[1][11]); SB();
afh[2] = TR(lbA, 29184);
lbase += lstep;
SB();
asm volatile("s_waitcnt vmcnt(19)\n\ts_barrier" ::: "memory");
SB(); M32A(Y[0], AF(0), xb[0][12]); SB();
afl[3] = TR(lbA, 30720);
{ unsigned u = pk_bf16(X[1][12], X[1][13]); PIN(u); h2[0] = u; }
{ unsigned u = pk_bf16(X[1][14], X[1][15]); PIN(u); h2[1] = u; }
SB(); M32A(Y[1], AF(0), xb[1][12]); SB();
afh[3] = TR(lbA, 31232);
ST21(qvo, h2, qdst + (qp0 + 1) * 16384 + 6400);
SB(); M32A(Y[0], AF(1), xb[0][13]); SB();
afl[0] = TR(lbA, 32768);
SETM0(wv2k + 16384);
SB(); M32A(Y[1], AF(1), xb[1][13]); SB();
afh[0] = TR(lbA, 33280);
GLDS(lvoffA, lbase, 0);
SB(); M32A(Y[0], AF(2), xb[0][14]); SB();
afl[1] = TR(lbA, 34816);
SB(); M32A(Y[1], AF(2), xb[1][14]); SB();
afh[1] = TR(lbA, 35328);
SB(); M32A(Y[0], AF(3), xb[0][15]); SB();
afl[2] = TR(lbA, 36864);
GLDS(lvoffB, lbase, 1024);
SB(); M32A(Y[1], AF(3), xb[1][15]); SB();
afh[2] = TR(lbA, 37376);
lbase += lstep;
SB();
asm volatile("s_waitcnt vmcnt(20)\n\ts_barrier" ::: "memory");
SB(); M32A(Y[0], AF(0), xb[0][16]); SB();
afl[3] = TR(lbA, 38912);
SB(); M32A(Y[1], AF(0), xb[1][16]); SB();
afh[3] = TR(lbA, 39424);
SB(); M32A(Y[0], AF(1), xb[0][17]); SB();
afl[0] = TR(lbA, 40960);
SETM0(wv2k + 24576);
SB(); M32A(Y[1], AF(1), xb[1][17]); SB();
afh[0] = TR(lbA, 41472);
GLDS(lvoffA, lbase, 0);
SB(); M32A(Y[0], AF(2), xb[0][18]); SB();
afl[1] = TR(lbA, 43008);
SB(); M32A(Y[1], AF(2), xb[1][18]); SB();
afh[1] = TR(lbA, 43520);
SB(); M32A(Y[0], AF(3), xb[0][19]); SB();
afl[2] = TR(lbA, 45056);
GLDS(lvoffB, lbase, 1024);
SB(); M32A(Y[1], AF(3), xb[1][19]); SB();
afh[2] = TR(lbA, 45568);
lbase += lstep;
SB();
asm volatile("s_waitcnt vmcnt(20)\n\ts_barrier" ::: "memory");
SB(); M32A(Y[0], AF(0), xb[0][20]); SB();
afl[3] = TR(lbA, 47104);
SB(); M32A(Y[1], AF(0), xb[1][20]); SB();
afh[3] = TR(lbA, 47616);
SB(); M32A(Y[0], AF(1), xb[0][21]); SB();
afl[0] = TR(lbA, 49152);
SETM0(wv2k + 32768);
SB(); M32A(Y[1], AF(1), xb[1][21]); SB();
afh[0] = TR(lbA, 49664);
GLDS(lvoffA, lbase, 0);
SB(); M32A(Y[0], AF(2), xb[0][22]); SB();
afl[1] = TR(lbA, 51200);
SB(); M32A(Y[1], AF(2), xb[1][22]); SB();
afh[1] = TR(lbA, 51712);
SB(); M32A(Y[0], AF(3), xb[0][23]); SB();
afl[2] = TR(lbA, 53248);
GLDS(lvoffB, lbase, 1024);
SB(); M32A(Y[1], AF(3), xb[1][23]); SB();
afh[2] = TR(lbA, 53760);
lbase += lstep;
SB();
asm volatile("s_waitcnt vmcnt(20)\n\ts_barrier" ::: "memory");
SB(); M32A(Y[0], AF(0), xb[0][24]); SB();
afl[3] = TR(lbA, 55296);
SB(); M32A(Y[1], AF(0), xb[1][24]); SB();
afh[3] = TR(lbA, 55808);
SB(); M32A(Y[0], AF(1), xb[0][25]); SB();
afl[0] = TR(lbA, 57344);
SETM0(wv2k + 40960);
SB(); M32A(Y[1], AF(1), xb[1][25]); SB();
afh[0] = TR(lbA, 57856);
GLDS(lvoffA, lbase, 0);
SB(); M32A(Y[0], AF(2), xb[0][26]); SB();
afl[1] = TR(lbA, 59392);
SB(); M32A(Y[1], AF(2), xb[1][26]); SB();
afh[1] = TR(lbA, 59904);
SB(); M32A(Y[0], AF(3), xb[0][27]); SB();
afl[2] = TR(lbA, 61440);
GLDS(lvoffB, lbase, 1024);
SB(); M32A(Y[1], AF(3), xb[1][27]); SB();
afh[2] = TR(lbA, 61952);
lbase += lstep;
SB();
asm volatile("s_waitcnt vmcnt(20)\n\ts_barrier" ::: "memory");
SB(); M32A(Y[0], AF(0), xb[0][28]); SB();
afl[3] = TR(lbA, 63488);
SB(); M32A(Y[1], AF(0), xb[1][28]); SB();
afh[3] = TR(lbA, 64000);
SB(); M32A(Y[0], AF(1), xb[0][29]); SB();
afl[0] = TR(lbA, 65536);
SETM0(wv2k + 49152);
SB(); M32A(Y[1], AF(1), xb[1][29]); SB();
afh[0] = TR(lbA, 66048);
GLDS(lvoffA, lbase, 0);
SB(); M32V(Y[0], AF(2), xb[0][30]); SB();
afl[1] = TR(lbA, 67584);
SB(); M32V(Y[1], AF(2), xb[1][30]); SB();
afh[1] = TR(lbA, 68096);
SB(); M32V(Y[0], AF(3), xb[0][31]); SB();
afl[2] = TR(lbA, 69632);
GLDS(lvoffB, lbase, 1024);
SB(); M32V(Y[1], AF(3), xb[1][31]); SB();
afh[2] = TR(lbA, 70144);
lbase += lstep;
SB();
asm volatile("s_waitcnt vmcnt(17)\n\ts_barrier" ::: "memory");
SB(); M32V(Y[0], AF(0), xb[0][32]); SB();
afl[3] = TR(lbA, 71680);
SB(); M32V(Y[1], AF(0), xb[1][32]); SB();
afh[3] = TR(lbA, 72192);
SB(); M32V(Y[0], AF(1), xb[0][33]); SB();
afl[0] = TR(lbA, 0);
SETM0(wv2k + 57344);
SB(); M32V(Y[1], AF(1), xb[1][33]); SB();
afh[0] = TR(lbA, 512);
GLDS(lvoffA, lbase, 0);
SB(); M32V(Y[0], AF(2), xb[0][34]); SB();
afl[1] = TR(lbA, 2048);
SB(); M32V(Y[1], AF(2), xb[1][34]); SB();
afh[1] = TR(lbA, 2560);
SB(); M32V(Y[0], AF(3), xb[0][35]); SB();
afl[2] = TR(lbA, 4096);
GLDS(lvoffB, lbase, 1024);
SB(); M32V(Y[1], AF(3), xb[1][35]); SB();
afh[2] = TR(lbA, 4608);
lbase += lstep;
}
MNOPX(Y);
} else { Y[0] = X[0]; Y[1] = X[1]; }
{
const char* nbase = wub; const unsigned nstride = 9216u;
SB();
asm volatile("s_waitcnt vmcnt(12)\n\ts_barrier" ::: "memory");
SB(); M32ZA(X[0], AF(0), xb[0][0]); SB();
afl[3] = TR(lbA, 6144);
SB(); M32ZA(X[1], AF(0), xb[1][0]); SB();
afh[3] = TR(lbA, 6656);
SB(); M32A(X[0], AF(1), xb[0][1]); SB();
afl[0] = TR(lbA, 8192);
SETM0(wv2k + 65536);
SB(); M32A(X[1], AF(1), xb[1][1]); SB();
afh[0] = TR(lbA, 8704);
GLDS(lvoffA, lbase, 0);
SB(); M32A(X[0], AF(2), xb[0][2]); SB();
afl[1] = TR(lbA, 10240);
SB(); M32A(X[1], AF(2), xb[1][2]); SB();
afh[1] = TR(lbA, 10752);
SB(); M32A(X[0], AF(3), xb[0][3]); SB();
afl[2] = TR(lbA, 12288);
GLDS(lvoffB, lbase, 1024);
{ unsigned u = pk_bf16(Y[0][0], Y[0][1]); PIN(u); h2[0] = u; }
{ unsigned u = pk_bf16(Y[0][2], Y[0][3]); PIN(u); h2[1] = u; }
SB(); M32A(X[1], AF(3), xb[1][3]); SB();
afh[2] = TR(lbA, 12800);
lbase = nbase; lstep = 64 * nstride; lvoffA = rowA * nstride + c16P1; lvoffB = (rowA + 8) * nstride + c16P1 - 1024;
SB();
asm volatile("s_waitcnt vmcnt(12)\n\ts_barrier" ::: "memory");
SB(); M32A(X[0], AF(0), xb[0][4]); SB();
afl[3] = TR(lbA, 14336);
ST21(qvo, h2, qdst + (qp0 + nq - 1) * 16384 + 0);
SB(); M32A(X[1], AF(0), xb[1][4]); SB();
afh[3] = TR(lbA, 14848);
{ unsigned u = pk_bf16(Y[0][4], Y[0][5]); PIN(u); h2[0] = u; }
{ unsigned u = pk_bf16(Y[0][6], Y[0][7]); PIN(u); h2[1] = u; }
SB(); M32A(X[0], AF(1), xb[0][5]); SB();
afl[0] = TR(lbA, 16384);
SETM0(wv2k + 0);
ST21(qvo, h2, qdst + (qp0 + nq - 1) * 16384 + 2048);
SB(); M32A(X[1], AF(1), xb[1][5]); SB();
afh[0] = TR(lbA, 16896);
GLDS(lvoffA, lbase, 0);
{ unsigned u = pk_bf16(Y[0][8], Y[0][9]); PIN(u); h2[0] = u; }
{ unsigned u = pk_bf16(Y[0][10], Y[0][11]); PIN(u); h2[1] = u; }
SB(); M32A(X[0], AF(2), xb[0][6]); SB();
afl[1] = TR(lbA, 18432);
ST21(qvo, h2, qdst + (qp0 + nq - 1) * 16384 + 4096);
SB(); M32A(X[1], AF(2), xb[1][6]); SB();
afh[1] = TR(lbA, 18944);
{ unsigned u = pk_bf16(Y[0][12], Y[0][13]); PIN(u); h2[0] = u; }
{ unsigned u = pk_bf16(Y[0][14], Y[0][15]); PIN(u); h2[1] = u; }
SB(); M32A(X[0], AF(3), xb[0][7]); SB();
afl[2] = TR(lbA, 20480);
GLDS(lvoffB, lbase, 1024);
ST21(qvo, h2, qdst + (qp0 + nq - 1) * 16384 + 6144);
SB(); M32A(X[1], AF(3), xb[1][7]); SB();
afh[2] = TR(lbA, 20992);
lbase += lstep;
SB();
asm volatile("s_waitcnt vmcnt(16)\n\ts_barrier" ::: "memory");
SB(); M32A(X[0], AF(0), xb[0][8]); SB();
afl[3] = TR(lbA, 22528);
{ unsigned u = pk_bf16(Y[1][0], Y[1][1]); PIN(u); h2[0] = u; }
{ unsigned u = pk_bf16(Y[1][2], Y[1][3]); PIN(u); h2[1] = u; }
SB(); M32A(X[1], AF(0), xb[1][8]); SB();
afh[3] = TR(lbA, 23040);
ST21(qvo, h2, qdst + (qp0 + nq - 1) * 16384 + 256);
SB(); M32A(X[0], AF(1), xb[0][9]); SB();
afl[0] = TR(lbA, 24576);
SETM0(wv2k + 8192);
{ unsigned u = pk_bf16(Y[1][4], Y[1][5]); PIN(u); h2[0] = u; }
{ unsigned u = pk_bf16(Y[1][6], Y[1][7]); PIN(u); h2[1] = u; }
SB(); M32A(X[1], AF(1), xb[1][9]); SB();
afh[0] = TR(lbA, 25088);
GLDS(lvoffA, lbase, 0);
SB(); M32A(X[0], AF(2), xb[0][10]); SB();
afl[1] = TR(lbA, 26624);
ST21(qvo, h2, qdst + (qp0 + nq - 1) * 16384 + 2304);
SB(); M32A(X[1], AF(2), xb[1][10]); SB();
afh[1] = TR(lbA, 27136);
{ unsigned u = pk_bf16(Y[1][8], Y[1][9]); PIN(u); h2[0] = u; }
{ unsigned u = pk_bf16(Y[1][10], Y[1][11]); PIN(u); h2[1] = u; }
SB(); M32A(X[0], AF(3), xb[0][11]); SB();
afl[2] = TR(lbA, 28672);
GLDS(lvoffB, lbase, 1024);
ST21(qvo, h2, qdst + (qp0 + nq - 1) * 16384 + 4352);
SB(); M32A(X[1], AF(3), xb[1][11]); SB();
afh[2] = TR(lbA, 29184);
lbase += lstep;
SB();
asm volatile("s_waitcnt vmcnt(19)\n\ts_barrier" ::: "memory");
SB(); M32A(X[0], AF(0), xb[0][12]); SB();
afl[3] = TR(lbA, 30720);
{ unsigned u = pk_bf16(Y[1][12], Y[1][13]); PIN(u); h2[0] = u; }
{ unsigned u = pk_bf16(Y[1][14], Y[1][15]); PIN(u); h2[1] = u; }
SB(); M32A(X[1], AF(0), xb[1][12]); SB();
afh[3] = TR(lbA, 31232);
ST21(qvo, h2, qdst + (qp0 + nq - 1) * 16384 + 6400);
SB(); M32A(X[0], AF(1), xb[0][13]); SB();
afl[0] = TR(lbA, 32768);
SETM0(wv2k + 16384);
SB(); M32A(X[1], AF(1), xb[1][13]); SB();
afh[0] = TR(lbA, 33280);
GLDS(lvoffA, lbase, 0);
SB(); M32A(X[0], AF(2), xb[0][14]); SB();
afl[1] = TR(lbA, 34816);
SB(); M32A(X[1], AF(2), xb[1][14]); SB();
afh[1] = TR(lbA, 35328);
SB(); M32A(X[0], AF(3), xb[0][15]); SB();
afl[2] = TR(lbA, 36864);
GLDS(lvoffB, lbase, 1024);
SB(); M32A(X[1], AF(3), xb[1][15]); SB();
afh[2] = TR(lbA, 37376);
lbase += lstep;
SB();
asm volatile("s_waitcnt vmcnt(20)\n\ts_barrier" ::: "memory");
SB(); M32A(X[0], AF(0), xb[0][16]); SB();
afl[3] = TR(lbA, 38912);
SB(); M32A(X[1], AF(0), xb[1][16]); SB();
afh[3] = TR(lbA, 39424);
SB(); M32A(X[0], AF(1), xb[0][17]); SB();
afl[0] = TR(lbA, 40960);
SETM0(wv2k + 24576);
SB(); M32A(X[1], AF(1), xb[1][17]); SB();
afh[0] = TR(lbA, 41472);
GLDS(lvoffA, lbase, 0);
SB(); M32A(X[0], AF(2), xb[0][18]); SB();
afl[1] = TR(lbA, 43008);
SB(); M32A(X[1], AF(2), xb[1][18]); SB();
afh[1] = TR(lbA, 43520);
SB(); M32A(X[0], AF(3), xb[0][19]); SB();
afl[2] = TR(lbA, 45056);
GLDS(lvoffB, lbase, 1024);
SB(); M32A(X[1], AF(3), xb[1][19]); SB();
afh[2] = TR(lbA, 45568);
lbase += lstep;
SB();
asm volatile("s_waitcnt vmcnt(20)\n\ts_barrier" ::: "memory");
SB(); M32A(X[0], AF(0), xb[0][20]); SB();
afl[3] = TR(lbA, 47104);
SB(); M32A(X[1], AF(0), xb[1][20]); SB();
afh[3] = TR(lbA, 47616);
SB(); M32A(X[0], AF(1), xb[0][21]); SB();
afl[0] = TR(lbA, 49152);
SETM0(wv2k + 32768);
SB(); M32A(X[1], AF(1), xb[1][21]); SB();
afh[0] = TR(lbA, 49664);
GLDS(lvoffA, lbase, 0);
SB(); M32A(X[0], AF(2), xb[0][22]); SB();
afl[1] = TR(lbA, 51200);
SB(); M32A(X[1], AF(2), xb[1][22]); SB();
afh[1] = TR(lbA, 51712);
SB(); M32A(X[0], AF(3), xb[0][23]); SB();
afl[2] = TR(lbA, 53248);
GLDS(lvoffB, lbase, 1024);
SB(); M32A(X[1], AF(3), xb[1][23]); SB();
afh[2] = TR(lbA, 53760);
lbase += lstep;
SB();
asm volatile("s_waitcnt vmcnt(20)\n\ts_barrier" ::: "memory");
SB(); M32A(X[0], AF(0), xb[0][24]); SB();
afl[3] = TR(lbA, 55296);
SB(); M32A(X[1], AF(0), xb[1][24]); SB();
afh[3] = TR(lbA, 55808);
SB(); M32A(X[0], AF(1), xb[0][25]); SB();
afl[0] = TR(lbA, 57344);
SETM0(wv2k + 40960);
SB(); M32A(X[1], AF(1), xb[1][25]); SB();
afh[0] = TR(lbA, 57856);
GLDS(lvoffA, lbase, 0);
SB(); M32A(X[0], AF(2), xb[0][26]); SB();
afl[1] = TR(lbA, 59392);
SB(); M32A(X[1], AF(2), xb[1][26]); SB();
afh[1] = TR(lbA, 59904);
SB(); M32A(X[0], AF(3), xb[0][27]); SB();
afl[2] = TR(lbA, 61440);
GLDS(lvoffB, lbase, 1024);
SB(); M32A(X[1], AF(3), xb[1][27]); SB();
afh[2] = TR(lbA, 61952);
lbase += lstep;
SB();
asm volatile("s_waitcnt vmcnt(20)\n\ts_barrier" ::: "memory");
SB(); M32A(X[0], AF(0), xb[0][28]); SB();
afl[3] = TR(lbA, 63488);
SB(); M32A(X[1], AF(0), xb[1][28]); SB();
afh[3] = TR(lbA, 64000);
SB(); M32A(X[0], AF(1), xb[0][29]); SB();
afl[0] = TR(lbA, 65536);
SETM0(wv2k + 49152);
SB(); M32A(X[1], AF(1), xb[1][29]); SB();
afh[0] = TR(lbA, 66048);
GLDS(lvoffA, lbase, 0);
SB(); M32V(X[0], AF(2), xb[0][30]); SB();
afl[1] = TR(lbA, 67584);
SB(); M32V(X[1], AF(2), xb[1][30]); SB();
afh[1] = TR(lbA, 68096);
SB(); M32V(X[0], AF(3), xb[0][31]); SB();
afl[2] = TR(lbA, 69632);
GLDS(lvoffB, lbase, 1024);
SB(); M32V(X[1], AF(3), xb[1][31]); SB();
afh[2] = TR(lbA, 70144);
lbase += lstep;
SB();
asm volatile("s_waitcnt vmcnt(17)\n\ts_barrier" ::: "memory");
SB(); M32V(X[0], AF(0), xb[0][32]); SB();
afl[3] = TR(lbA, 71680);
SB(); M32V(X[1], AF(0), xb[1][32]); SB();
afh[3] = TR(lbA, 72192);
SB(); M32V(X[0], AF(1), xb[0][33]); SB();
afl[0] = TR(lbA, 0);
SETM0(wv2k + 57344);
SB(); M32V(X[1], AF(1), xb[1][33]); SB();
afh[0] = TR(lbA, 512);
GLDS(lvoffA, lbase, 0);
SB(); M32V(X[0], AF(2), xb[0][34]); SB();
afl[1] = TR(lbA, 2048);
SB(); M32V(X[1], AF(2), xb[1][34]); SB();
afh[1] = TR(lbA, 2560);
SB(); M32V(X[0], AF(3), xb[0][35]); SB();
afl[2] = TR(lbA, 4096);
GLDS(lvoffB, lbase, 1024);
SB(); M32V(X[1], AF(3), xb[1][35]); SB();
afh[2] = TR(lbA, 4608);
lbase += lstep;
}
MNOPX(X);
}
STAMP(3); JIT(3);
{
const char* nbase = wgb + 128; const unsigned nstride = 9216u;
SB();
asm volatile("s_waitcnt vmcnt(12)\n\ts_barrier" ::: "memory");
SB(); M32ZA(Y[0], AF(0), xb[0][0]); SB();
afl[3] = TR(lbA, 6144);
T[0] = X[0][0];
T[1] = X[0][1];
T[2] = X[0][2];
T[3] = X[0][3];
T[4] = X[0][8];
T[5] = X[0][9];
T[6] = X[0][10];
T[7] = X[0][11];
T[8] = X[0][4];
T[9] = X[0][5];
T[10] = X[0][6];
T[11] = X[0][7];
T[12] = X[0][12];
T[13] = X[0][13];
T[14] = X[0][14];
T[15] = X[0][15];
T[16] = X[1][0];
T[17] = X[1][1];
T[18] = X[1][2];
T[19] = X[1][3];
T[20] = X[1][8];
T[21] = X[1][9];
T[22] = X[1][10];
T[23] = X[1][11];
T[24] = X[1][4];
T[25] = X[1][5];
T[26] = X[1][6];
T[27] = X[1][7];
T[28] = X[1][12];
T[29] = X[1][13];
T[30] = X[1][14];
T[31] = X[1][15];
SB(); M32ZA(Y[1], AF(0), xb[1][0]); SB();
afh[3] = TR(lbA, 6656);
SB(); M32A(Y[0], AF(1), xb[0][1]); SB();
afl[0] = TR(lbA, 8192);
SETM0(wv2k + 65536);
SB(); M32A(Y[1], AF(1), xb[1][1]); SB();
afh[0] = TR(lbA, 8704);
GLDS(lvoffA, lbase, 0);
SB(); M32A(Y[0], AF(2), xb[0][2]); SB();
afl[1] = TR(lbA, 10240);
SB(); M32A(Y[1], AF(2), xb[1][2]); SB();
afh[1] = TR(lbA, 10752);
SB(); M32A(Y[0], AF(3), xb[0][3]); SB();
afl[2] = TR(lbA, 12288);
GLDS(lvoffB, lbase, 1024);
gt0 = T[0] * T[0]; PIN(gt0);
SB(); M32A(Y[1], AF(3), xb[1][3]); SB();
afh[2] = TR(lbA, 12800);
lbase = nbase; lstep = 64 * nstride; lvoffA = rowA * nstride + c16P1; lvoffB = (rowA + 8) * nstride + c16P1 - 1024;
SB();
asm volatile("s_waitcnt vmcnt(12)\n\ts_barrier" ::: "memory");
SB(); M32A(Y[0], AF(0), xb[0][4]); SB();
afl[3] = TR(lbA, 14336);
gt0 = __builtin_fmaf(gt0, C1, C0); PIN(gt0);
SB(); M32A(Y[1], AF(0), xb[1][4]); SB();
afh[3] = TR(lbA, 14848);
gt0 = T[0] * gt0; PIN(gt0);
gt1 = T[1] * T[1]; PIN(gt1);
SB(); M32A(Y[0], AF(1), xb[0][5]); SB();
afl[0] = TR(lbA, 16384);
SETM0(wv2k + 0);
gt0 = __builtin_amdgcn_exp2f(gt0); PIN(gt0);
gt1 = __builtin_fmaf(gt1, C1, C0); PIN(gt1);
SB(); M32A(Y[1], AF(1), xb[1][5]); SB();
afh[0] = TR(lbA, 16896);
GLDS(lvoffA, lbase, 0);
gt0 = gt0 + 1.0f; PIN(gt0);
gt1 = T[1] * gt1; PIN(gt1);
SB(); M32A(Y[0], AF(2), xb[0][6]); SB();
afl[1] = TR(lbA, 18432);
gt0 = __builtin_amdgcn_rcpf(gt0); PIN(gt0);
gt1 = __builtin_amdgcn_exp2f(gt1); PIN(gt1);
SB(); M32A(Y[1], AF(2), xb[1][6]); SB();
afh[1] = TR(lbA, 18944);
{ float v = T[0] * gt0; PIN(v); T[0] = v; }
gt1 = gt1 + 1.0f; PIN(gt1);
gt2 = T[2] * T[2]; PIN(gt2);
SB(); M32A(Y[0], AF(3), xb[0][7]); SB();
afl[2] = TR(lbA, 20480);
GLDS(lvoffB, lbase, 1024);
gt1 = __builtin_amdgcn_rcpf(gt1); PIN(gt1);
gt2 = __builtin_fmaf(gt2, C1, C0); PIN(gt2);
SB(); M32A(Y[1], AF(3), xb[1][7]); SB();
afh[2] = TR(lbA, 20992);
lbase += lstep;
SB();
asm volatile("s_waitcnt vmcnt(12)\n\ts_barrier" ::: "memory");
SB(); M32A(Y[0], AF(0), xb[0][8]); SB();
afl[3] = TR(lbA, 22528);
{ float v = T[1] * gt1; PIN(v); T[1] = v; }
gt2 = T[2] * gt2; PIN(gt2);
gt0 = T[3] * T[3]; PIN(gt0);
SB(); M32A(Y[1], AF(0), xb[1][8]); SB();
afh[3] = TR(lbA, 23040);
TOUCH(trow * 9216u, wub + 128);
gt2 = __builtin_amdgcn_exp2f(gt2); PIN(gt2);
gt0 = __builtin_fmaf(gt0, C1, C0); PIN(gt0);
SB(); M32A(Y[0], AF(1), xb[0][9]); SB();
afl[0] = TR(lbA, 24576);
SETM0(wv2k + 8192);
gt2 = gt2 + 1.0f; PIN(gt2);
gt0 = T[3] * gt0; PIN(gt0);
gt1 = T[4] * T[4]; PIN(gt1);
SB(); M32A(Y[1], AF(1), xb[1][9]); SB();
afh[0] = TR(lbA, 25088);
GLDS(lvoffA, lbase, 0);
gt2 = __builtin_amdgcn_rcpf(gt2); PIN(gt2);
SB(); M32A(Y[0], AF(2), xb[0][10]); SB();
afl[1] = TR(lbA, 26624);
{ float v = T[2] * gt2; PIN(v); T[2] = v; }
gt0 = __builtin_amdgcn_exp2f(gt0); PIN(gt0);
gt1 = __builtin_fmaf(gt1, C1, C0); PIN(gt1);
SB(); M32A(Y[1], AF(2), xb[1][10]); SB();
afh[1] = TR(lbA, 27136);
gt0 = gt0 + 1.0f; PIN(gt0);
gt1 = T[4] * gt1; PIN(gt1);
gt2 = T[5] * T[5]; PIN(gt2);
SB(); M32A(Y[0], AF(3), xb[0][11]); SB();
afl[2] = TR(lbA, 28672);
GLDS(lvoffB, lbase, 1024);
gt0 = __builtin_amdgcn_rcpf(gt0); PIN(gt0);
gt2 = __builtin_fmaf(gt2, C1, C0); PIN(gt2);
SB(); M32A(Y[1], AF(3), xb[1][11]); SB();
afh[2] = TR(lbA, 29184);
lbase += lstep;
SB();
asm volatile("s_waitcnt vmcnt(13)\n\ts_barrier" ::: "memory");
SB(); M32A(Y[0], AF(0), xb[0][12]); SB();
afl[3] = TR(lbA, 30720);
{ float v = T[3] * gt0; PIN(v); T[3] = v; }
gt1 = __builtin_amdgcn_exp2f(gt1); PIN(gt1);
gt2 = T[5] * gt2; PIN(gt2);
SB(); M32A(Y[1], AF(0), xb[1][12]); SB();
afh[3] = TR(lbA, 31232);
gt1 = gt1 + 1.0f; PIN(gt1);
gt2 = __builtin_amdgcn_exp2f(gt2); PIN(gt2);
SB(); M32A(Y[0], AF(1), xb[0][13]); SB();
afl[0] = TR(lbA, 32768);
SETM0(wv2k + 16384);
gt1 = __builtin_amdgcn_rcpf(gt1); PIN(gt1);
gt2 = gt2 + 1.0f; PIN(gt2);
SB(); M32A(Y[1], AF(1), xb[1][13]); SB();
afh[0] = TR(lbA, 33280);
GLDS(lvoffA, lbase, 0);
{ float v = T[4] * gt1; PIN(v); T[4] = v; }
gt0 = T[6] * T[6]; PIN(gt0);
SB(); M32A(Y[0], AF(2), xb[0][14]); SB();
afl[1] = TR(lbA, 34816);
gt2 = __builtin_amdgcn_rcpf(gt2); PIN(gt2);
gt0 = __builtin_fmaf(gt0, C1, C0); PIN(gt0);
gt1 = T[7] * T[7]; PIN(gt1);
SB(); M32A(Y[1], AF(2), xb[1][14]); SB();
afh[1] = TR(lbA, 35328);
{ float v = T[5] * gt2; PIN(v); T[5] = v; }
gt0 = T[6] * gt0; PIN(gt0);
gt1 = __builtin_fmaf(gt1, C1, C0); PIN(gt1);
SB(); M32A(Y[0], AF(3), xb[0][15]); SB();
afl[2] = TR(lbA, 36864);
GLDS(lvoffB, lbase, 1024);
gt0 = __builtin_amdgcn_exp2f(gt0); PIN(gt0);
gt1 = T[7] * gt1; PIN(gt1);
SB(); M32A(Y[1], AF(3), xb[1][15]); SB();
afh[2] = TR(lbA, 37376);
lbase += lstep;
SB();
asm volatile("s_waitcnt vmcnt(13)\n\ts_barrier" ::: "memory");
SB(); M32A(Y[0], AF(0), xb[0][16]); SB();
afl[3] = TR(lbA, 38912);
gt0 = gt0 + 1.0f; PIN(gt0);
gt1 = __builtin_amdgcn_exp2f(gt1); PIN(gt1);
gt2 = T[8] * T[8]; PIN(gt2);
SB(); M32A(Y[1], AF(0), xb[1][16]); SB();
afh[3] = TR(lbA, 39424);
TOUCH(trow * 9216u, wgb + 256);
gt0 = __builtin_amdgcn_rcpf(gt0); PIN(gt0);
gt1 = gt1 + 1.0f; PIN(gt1);
SB(); M32A(Y[0], AF(1), xb[0][17]); SB();
afl[0] = TR(lbA, 40960);
SETM0(wv2k + 24576);
{ float v = T[6] * gt0; PIN(v); T[6] = v; }
gt1 = __builtin_amdgcn_rcpf(gt1); PIN(gt1);
SB(); M32A(Y[1], AF(1), xb[1][17]); SB();
afh[0] = TR(lbA, 41472);
GLDS(lvoffA, lbase, 0);
{ float v = T[7] * gt1; PIN(v); T[7] = v; }
gt2 = __builtin_fmaf(gt2, C1, C0); PIN(gt2);
SB(); M32A(Y[0], AF(2), xb[0][18]); SB();
afl[1] = TR(lbA, 43008);
gt2 = T[8] * gt2; PIN(gt2);
gt0 = T[9] * T[9]; PIN(gt0);
gt1 = T[10] * T[10]; PIN(gt1);
SB(); M32A(Y[1], AF(2), xb[1][18]); SB();
afh[1] = TR(lbA, 43520);
gt2 = __builtin_amdgcn_exp2f(gt2); PIN(gt2);
gt0 = __builtin_fmaf(gt0, C1, C0); PIN(gt0);
SB(); M32A(Y[0], AF(3), xb[0][19]); SB();
afl[2] = TR(lbA, 45056);
GLDS(lvoffB, lbase, 1024);
gt2 = gt2 + 1.0f; PIN(gt2);
gt0 = T[9] * gt0; PIN(gt0);
gt1 = __builtin_fmaf(gt1, C1, C0); PIN(gt1);
SB(); M32A(Y[1], AF(3), xb[1][19]); SB();
afh[2] = TR(lbA, 45568);
lbase += lstep;
SB();
asm volatile("s_waitcnt vmcnt(14)\n\ts_barrier" ::: "memory");
SB(); M32A(Y[0], AF(0), xb[0][20]); SB();
afl[3] = TR(lbA, 47104);
gt2 = __builtin_amdgcn_rcpf(gt2); PIN(gt2);
gt0 = __builtin_amdgcn_exp2f(gt0); PIN(gt0);
SB(); M32A(Y[1], AF(0), xb[1][20]); SB();
afh[3] = TR(lbA, 47616);
{ float v = T[8] * gt2; PIN(v); T[8] = v; }
gt0 = gt0 + 1.0f; PIN(gt0);
gt1 = T[10] * gt1; PIN(gt1);
SB(); M32A(Y[0], AF(1), xb[0][21]); SB();
afl[0] = TR(lbA, 49152);
SETM0(wv2k + 32768);
gt0 = __builtin_amdgcn_rcpf(gt0); PIN(gt0);
gt2 = T[11] * T[11]; PIN(gt2);
SB(); M32A(Y[1], AF(1), xb[1][21]); SB();
afh[0] = TR(lbA, 49664);
GLDS(lvoffA, lbase, 0);
{ float v = T[9] * gt0; PIN(v); T[9] = v; }
gt2 = __builtin_fmaf(gt2, C1, C0); PIN(gt2);
SB(); M32A(Y[0], AF(2), xb[0][22]); SB();
afl[1] = TR(lbA, 51200);
gt1 = __builtin_amdgcn_exp2f(gt1); PIN(gt1);
gt2 = T[11] * gt2; PIN(gt2);
gt0 = T[12] * T[12]; PIN(gt0);
SB(); M32A(Y[1], AF(2), xb[1][22]); SB();
afh[1] = TR(lbA, 51712);
gt1 = gt1 + 1.0f; PIN(gt1);
gt2 = __builtin_amdgcn_exp2f(gt2); PIN(gt2);
SB(); M32A(Y[0], AF(3), xb[0][23]); SB();
afl[2] = TR(lbA, 53248);
GLDS(lvoffB, lbase, 1024);
gt1 = __builtin_amdgcn_rcpf(gt1); PIN(gt1);
gt2 = gt2 + 1.0f; PIN(gt2);
SB(); M32A(Y[1], AF(3), xb[1][23]); SB();
afh[2] = TR(lbA, 53760);
lbase += lstep;
SB();
asm volatile("s_waitcnt vmcnt(14)\n\ts_barrier" ::: "memory");
SB(); M32A(Y[0], AF(0), xb[0][24]); SB();
afl[3] = TR(lbA, 55296);
{ float v = T[10] * gt1; PIN(v); T[10] = v; }
gt2 = __builtin_amdgcn_rcpf(gt2); PIN(gt2);
gt0 = __builtin_fmaf(gt0, C1, C0); PIN(gt0);
SB(); M32A(Y[1], AF(0), xb[1][24]); SB();
afh[3] = TR(lbA, 55808);
{ float v = T[11] * gt2; PIN(v); T[11] = v; }
gt0 = T[12] * gt0; PIN(gt0);
gt1 = T[13] * T[13]; PIN(gt1);
SB(); M32A(Y[0], AF(1), xb[0][25]); SB();
afl[0] = TR(lbA, 57344);
SETM0(wv2k + 40960);
gt0 = __builtin_amdgcn_exp2f(gt0); PIN(gt0);
gt1 = __builtin_fmaf(gt1, C1, C0); PIN(gt1);
SB(); M32A(Y[1], AF(1), xb[1][25]); SB();
afh[0] = TR(lbA, 57856);
GLDS(lvoffA, lbase, 0);
gt0 = gt0 + 1.0f; PIN(gt0);
gt1 = T[13] * gt1; PIN(gt1);
SB(); M32A(Y[0], AF(2), xb[0][26]); SB();
afl[1] = TR(lbA, 59392);
gt0 = __builtin_amdgcn_rcpf(gt0); PIN(gt0);
gt1 = __builtin_amdgcn_exp2f(gt1); PIN(gt1);
SB(); M32A(Y[1], AF(2), xb[1][26]); SB();
afh[1] = TR(lbA, 59904);
{ float v = T[12] * gt0; PIN(v); T[12] = v; }
gt1 = gt1 + 1.0f; PIN(gt1);
gt2 = T[14] * T[14]; PIN(gt2);
SB(); M32A(Y[0], AF(3), xb[0][27]); SB();
afl[2] = TR(lbA, 61440);
GLDS(lvoffB, lbase, 1024);
gt1 = __builtin_amdgcn_rcpf(gt1); PIN(gt1);
gt2 = __builtin_fmaf(gt2, C1, C0); PIN(gt2);
SB(); M32A(Y[1], AF(3), xb[1][27]); SB();
afh[2] = TR(lbA, 61952);
lbase += lstep;
SB();
asm volatile("s_waitcnt vmcnt(14)\n\ts_barrier" ::: "memory");
SIGNAL_ADD(fQ, nq, nq > 0);
SB(); M32A(Y[0], AF(0), xb[0][28]); SB();
afl[3] = TR(lbA, 63488);
{ float v = T[13] * gt1; PIN(v); T[13] = v; }
gt2 = T[14] * gt2; PIN(gt2);
gt0 = T[15] * T[15]; PIN(gt0);
SB(); M32A(Y[1], AF(0), xb[1][28]); SB();
afh[3] = TR(lbA, 64000);
gt2 = __builtin_amdgcn_exp2f(gt2); PIN(gt2);
gt0 = __builtin_fmaf(gt0, C1, C0); PIN(gt0);
SB(); M32A(Y[0], AF(1), xb[0][29]); SB();
afl[0] = TR(lbA, 65536);
SETM0(wv2k + 49152);
gt2 = gt2 + 1.0f; PIN(gt2);
gt0 = T[15] * gt0; PIN(gt0);
gt1 = T[16] * T[16]; PIN(gt1);
SB(); M32A(Y[1], AF(1), xb[1][29]); SB();
afh[0] = TR(lbA, 66048);
GLDS(lvoffA, lbase, 0);
gt2 = __builtin_amdgcn_rcpf(gt2); PIN(gt2);
SB(); M32V(Y[0], AF(2), xb[0][30]); SB();
afl[1] = TR(lbA, 67584);
{ float v = T[14] * gt2; PIN(v); T[14] = v; }
gt0 = __builtin_amdgcn_exp2f(gt0); PIN(gt0);
gt1 = __builtin_fmaf(gt1, C1, C0); PIN(gt1);
SB(); M32V(Y[1], AF(2), xb[1][30]); SB();
afh[1] = TR(lbA, 68096);
gt0 = gt0 + 1.0f; PIN(gt0);
gt1 = T[16] * gt1; PIN(gt1);
gt2 = T[17] * T[17]; PIN(gt2);
SB(); M32V(Y[0], AF(3), xb[0][31]); SB();
afl[2] = TR(lbA, 69632);
GLDS(lvoffB, lbase, 1024);
gt0 = __builtin_amdgcn_rcpf(gt0); PIN(gt0);
gt2 = __builtin_fmaf(gt2, C1, C0); PIN(gt2);
SB(); M32V(Y[1], AF(3), xb[1][31]); SB();
afh[2] = TR(lbA, 70144);
lbase += lstep;
SB();
asm volatile("s_waitcnt vmcnt(14)\n\ts_barrier" ::: "memory");
SB(); M32V(Y[0], AF(0), xb[0][32]); SB();
afl[3] = TR(lbA, 71680);
{ float v = T[15] * gt0; PIN(v); T[15] = v; }
gt1 = __builtin_amdgcn_exp2f(gt1); PIN(gt1);
gt2 = T[17] * gt2; PIN(gt2);
SB(); M32V(Y[1], AF(0), xb[1][32]); SB();
afh[3] = TR(lbA, 72192);
gt1 = gt1 + 1.0f; PIN(gt1);
gt2 = __builtin_amdgcn_exp2f(gt2); PIN(gt2);
SB(); M32V(Y[0], AF(1), xb[0][33]); SB();
afl[0] = TR(lbA, 0);
SETM0(wv2k + 57344);
gt1 = __builtin_amdgcn_rcpf(gt1); PIN(gt1);
gt2 = gt2 + 1.0f; PIN(gt2);
SB(); M32V(Y[1], AF(1), xb[1][33]); SB();
afh[0] = TR(lbA, 512);
GLDS(lvoffA, lbase, 0);
{ float v = T[16] * gt1; PIN(v); T[16] = v; }
gt0 = T[18] * T[18]; PIN(gt0);
SB(); M32V(Y[0], AF(2), xb[0][34]); SB();
afl[1] = TR(lbA, 2048);
gt2 = __builtin_amdgcn_rcpf(gt2); PIN(gt2);
gt0 = __builtin_fmaf(gt0, C1, C0); PIN(gt0);
gt1 = T[19] * T[19]; PIN(gt1);
SB(); M32V(Y[1], AF(2), xb[1][34]); SB();
afh[1] = TR(lbA, 2560);
{ float v = T[17] * gt2; PIN(v); T[17] = v; }
gt0 = T[18] * gt0; PIN(gt0);
gt1 = __builtin_fmaf(gt1, C1, C0); PIN(gt1);
SB(); M32V(Y[0], AF(3), xb[0][35]); SB();
afl[2] = TR(lbA, 4096);
GLDS(lvoffB, lbase, 1024);
gt0 = __builtin_amdgcn_exp2f(gt0); PIN(gt0);
gt1 = T[19] * gt1; PIN(gt1);
SB(); M32V(Y[1], AF(3), xb[1][35]); SB();
afh[2] = TR(lbA, 4608);
lbase += lstep;
}
{
const char* nbase = wub + 128; const unsigned nstride = 9216u;
SB();
asm volatile("s_waitcnt vmcnt(13)\n\ts_barrier" ::: "memory");
SB(); M32ZA(X[0], AF(0), xb[0][0]); SB();
afl[3] = TR(lbA, 6144);
gt0 = gt0 + 1.0f; PIN(gt0);
gt1 = __builtin_amdgcn_exp2f(gt1); PIN(gt1);
gt2 = T[20] * T[20]; PIN(gt2);
SB(); M32ZA(X[1], AF(0), xb[1][0]); SB();
afh[3] = TR(lbA, 6656);
gt0 = __builtin_amdgcn_rcpf(gt0); PIN(gt0);
gt1 = gt1 + 1.0f; PIN(gt1);
SB(); M32A(X[0], AF(1), xb[0][1]); SB();
afl[0] = TR(lbA, 8192);
SETM0(wv2k + 65536);
{ float v = T[18] * gt0; PIN(v); T[18] = v; }
gt1 = __builtin_amdgcn_rcpf(gt1); PIN(gt1);
SB(); M32A(X[1], AF(1), xb[1][1]); SB();
afh[0] = TR(lbA, 8704);
GLDS(lvoffA, lbase, 0);
{ float v = T[19] * gt1; PIN(v); T[19] = v; }
gt2 = __builtin_fmaf(gt2, C1, C0); PIN(gt2);
SB(); M32A(X[0], AF(2), xb[0][2]); SB();
afl[1] = TR(lbA, 10240);
gt2 = T[20] * gt2; PIN(gt2);
gt0 = T[21] * T[21]; PIN(gt0);
gt1 = T[22] * T[22]; PIN(gt1);
SB(); M32A(X[1], AF(2), xb[1][2]); SB();
afh[1] = TR(lbA, 10752);
gt2 = __builtin_amdgcn_exp2f(gt2); PIN(gt2);
gt0 = __builtin_fmaf(gt0, C1, C0); PIN(gt0);
SB(); M32A(X[0], AF(3), xb[0][3]); SB();
afl[2] = TR(lbA, 12288);
GLDS(lvoffB, lbase, 1024);
gt2 = gt2 + 1.0f; PIN(gt2);
gt0 = T[21] * gt0; PIN(gt0);
gt1 = __builtin_fmaf(gt1, C1, C0); PIN(gt1);
SB(); M32A(X[1], AF(3), xb[1][3]); SB();
afh[2] = TR(lbA, 12800);
lbase = nbase; lstep = 64 * nstride; lvoffA = rowA * nstride + c16P1; lvoffB = (rowA + 8) * nstride + c16P1 - 1024;
SB();
asm volatile("s_waitcnt vmcnt(13)\n\ts_barrier" ::: "memory");
SB(); M32A(X[0], AF(0), xb[0][4]); SB();
afl[3] = TR(lbA, 14336);
gt2 = __builtin_amdgcn_rcpf(gt2); PIN(gt2);
gt0 = __builtin_amdgcn_exp2f(gt0); PIN(gt0);
SB(); M32A(X[1], AF(0), xb[1][4]); SB();
afh[3] = TR(lbA, 14848);
{ float v = T[20] * gt2; PIN(v); T[20] = v; }
gt0 = gt0 + 1.0f; PIN(gt0);
gt1 = T[22] * gt1; PIN(gt1);
SB(); M32A(X[0], AF(1), xb[0][5]); SB();
afl[0] = TR(lbA, 16384);
SETM0(wv2k + 0);
gt0 = __builtin_amdgcn_rcpf(gt0); PIN(gt0);
gt2 = T[23] * T[23]; PIN(gt2);
SB(); M32A(X[1], AF(1), xb[1][5]); SB();
afh[0] = TR(lbA, 16896);
GLDS(lvoffA, lbase, 0);
{ float v = T[21] * gt0; PIN(v); T[21] = v; }
gt2 = __builtin_fmaf(gt2, C1, C0); PIN(gt2);
SB(); M32A(X[0], AF(2), xb[0][6]); SB();
afl[1] = TR(lbA, 18432);
gt1 = __builtin_amdgcn_exp2f(gt1); PIN(gt1);
gt2 = T[23] * gt2; PIN(gt2);
gt0 = T[24] * T[24]; PIN(gt0);
SB(); M32A(X[1], AF(2), xb[1][6]); SB();
afh[1] = TR(lbA, 18944);
gt1 = gt1 + 1.0f; PIN(gt1);
gt2 = __builtin_amdgcn_exp2f(gt2); PIN(gt2);
SB(); M32A(X[0], AF(3), xb[0][7]); SB();
afl[2] = TR(lbA, 20480);
GLDS(lvoffB, lbase, 1024);
gt1 = __builtin_amdgcn_rcpf(gt1); PIN(gt1);
gt2 = gt2 + 1.0f; PIN(gt2);
SB(); M32A(X[1], AF(3), xb[1][7]); SB();
afh[2] = TR(lbA, 20992);
lbase += lstep;
SB();
asm volatile("s_waitcnt vmcnt(12)\n\ts_barrier" ::: "memory");
SB(); M32A(X[0], AF(0), xb[0][8]); SB();
afl[3] = TR(lbA, 22528);
{ float v = T[22] * gt1; PIN(v); T[22] = v; }
gt2 = __builtin_amdgcn_rcpf(gt2); PIN(gt2);
gt0 = __builtin_fmaf(gt0, C1, C0); PIN(gt0);
SB(); M32A(X[1], AF(0), xb[1][8]); SB();
afh[3] = TR(lbA, 23040);
TOUCH(trow * 9216u, wub + 256);
{ float v = T[23] * gt2; PIN(v); T[23] = v; }
gt0 = T[24] * gt0; PIN(gt0);
gt1 = T[25] * T[25]; PIN(gt1);
SB(); M32A(X[0], AF(1), xb[0][9]); SB();
afl[0] = TR(lbA, 24576);
SETM0(wv2k + 8192);
gt0 = __builtin_amdgcn_exp2f(gt0); PIN(gt0);
gt1 = __builtin_fmaf(gt1, C1, C0); PIN(gt1);
SB(); M32A(X[1], AF(1), xb[1][9]); SB();
afh[0] = TR(lbA, 25088);
GLDS(lvoffA, lbase, 0);
gt0 = gt0 + 1.0f; PIN(gt0);
gt1 = T[25] * gt1; PIN(gt1);
SB(); M32A(X[0], AF(2), xb[0][10]); SB();
afl[1] = TR(lbA, 26624);
gt0 = __builtin_amdgcn_rcpf(gt0); PIN(gt0);
gt1 = __builtin_amdgcn_exp2f(gt1); PIN(gt1);
SB(); M32A(X[1], AF(2), xb[1][10]); SB();
afh[1] = TR(lbA, 27136);
{ float v = T[24] * gt0; PIN(v); T[24] = v; }
gt1 = gt1 + 1.0f; PIN(gt1);
gt2 = T[26] * T[26]; PIN(gt2);
SB(); M32A(X[0], AF(3), xb[0][11]); SB();
afl[2] = TR(lbA, 28672);
GLDS(lvoffB, lbase, 1024);
gt1 = __builtin_amdgcn_rcpf(gt1); PIN(gt1);
gt2 = __builtin_fmaf(gt2, C1, C0); PIN(gt2);
SB(); M32A(X[1], AF(3), xb[1][11]); SB();
afh[2] = TR(lbA, 29184);
lbase += lstep;
SB();
asm volatile("s_waitcnt vmcnt(13)\n\ts_barrier" ::: "memory");
SB(); M32A(X[0], AF(0), xb[0][12]); SB();
afl[3] = TR(lbA, 30720);
{ float v = T[25] * gt1; PIN(v); T[25] = v; }
gt2 = T[26] * gt2; PIN(gt2);
gt0 = T[27] * T[27]; PIN(gt0);
{ float v = T[0] * Y[0][0]; PIN(v); T[0] = v; }
SB(); M32A(X[1], AF(0), xb[1][12]); SB();
afh[3] = TR(lbA, 31232);
gt2 = __builtin_amdgcn_exp2f(gt2); PIN(gt2);
gt0 = __builtin_fmaf(gt0, C1, C0); PIN(gt0);
SB(); M32A(X[0], AF(1), xb[0][13]); SB();
afl[0] = TR(lbA, 32768);
SETM0(wv2k + 16384);
gt2 = gt2 + 1.0f; PIN(gt2);
gt0 = T[27] * gt0; PIN(gt0);
gt1 = T[28] * T[28]; PIN(gt1);
SB(); M32A(X[1], AF(1), xb[1][13]); SB();
afh[0] = TR(lbA, 33280);
GLDS(lvoffA, lbase, 0);
gt2 = __builtin_amdgcn_rcpf(gt2); PIN(gt2);
SB(); M32A(X[0], AF(2), xb[0][14]); SB();
afl[1] = TR(lbA, 34816);
{ float v = T[26] * gt2; PIN(v); T[26] = v; }
gt0 = __builtin_amdgcn_exp2f(gt0); PIN(gt0);
gt1 = __builtin_fmaf(gt1, C1, C0); PIN(gt1);
SB(); M32A(X[1], AF(2), xb[1][14]); SB();
afh[1] = TR(lbA, 35328);
gt0 = gt0 + 1.0f; PIN(gt0);
gt1 = T[28] * gt1; PIN(gt1);
gt2 = T[29] * T[29]; PIN(gt2);
SB(); M32A(X[0], AF(3), xb[0][15]); SB();
afl[2] = TR(lbA, 36864);
GLDS(lvoffB, lbase, 1024);
gt0 = __builtin_amdgcn_rcpf(gt0); PIN(gt0);
gt2 = __builtin_fmaf(gt2, C1, C0); PIN(gt2);
SB(); M32A(X[1], AF(3), xb[1][15]); SB();
afh[2] = TR(lbA, 37376);
lbase += lstep;
SB();
asm volatile("s_waitcnt vmcnt(13)\n\ts_barrier" ::: "memory");
SB(); M32A(X[0], AF(0), xb[0][16]); SB();
afl[3] = TR(lbA, 38912);
{ float v = T[27] * gt0; PIN(v); T[27] = v; }
gt1 = __builtin_amdgcn_exp2f(gt1); PIN(gt1);
gt2 = T[29] * gt2; PIN(gt2);
SB(); M32A(X[1], AF(0), xb[1][16]); SB();
afh[3] = TR(lbA, 39424);
gt1 = gt1 + 1.0f; PIN(gt1);
gt2 = __builtin_amdgcn_exp2f(gt2); PIN(gt2);
SB(); M32A(X[0], AF(1), xb[0][17]); SB();
afl[0] = TR(lbA, 40960);
SETM0(wv2k + 24576);
gt1 = __builtin_amdgcn_rcpf(gt1); PIN(gt1);
gt2 = gt2 + 1.0f; PIN(gt2);
SB(); M32A(X[1], AF(1), xb[1][17]); SB();
afh[0] = TR(lbA, 41472);
GLDS(lvoffA, lbase, 0);
{ float v = T[28] * gt1; PIN(v); T[28] = v; }
gt0 = T[30] * T[30]; PIN(gt0);
SB(); M32A(X[0], AF(2), xb[0][18]); SB();
afl[1] = TR(lbA, 43008);
gt2 = __builtin_amdgcn_rcpf(gt2); PIN(gt2);
gt0 = __builtin_fmaf(gt0, C1, C0); PIN(gt0);
gt1 = T[31] * T[31]; PIN(gt1);
SB(); M32A(X[1], AF(2), xb[1][18]); SB();
afh[1] = TR(lbA, 43520);
{ float v = T[29] * gt2; PIN(v); T[29] = v; }
gt0 = T[30] * gt0; PIN(gt0);
gt1 = __builtin_fmaf(gt1, C1, C0); PIN(gt1);
SB(); M32A(X[0], AF(3), xb[0][19]); SB();
afl[2] = TR(lbA, 45056);
GLDS(lvoffB, lbase, 1024);
gt0 = __builtin_amdgcn_exp2f(gt0); PIN(gt0);
gt1 = T[31] * gt1; PIN(gt1);
SB(); M32A(X[1], AF(3), xb[1][19]); SB();
afh[2] = TR(lbA, 45568);
lbase += lstep;
SB();
asm volatile("s_waitcnt vmcnt(13)\n\ts_barrier" ::: "memory");
SB(); M32A(X[0], AF(0), xb[0][20]); SB();
afl[3] = TR(lbA, 47104);
gt0 = gt0 + 1.0f; PIN(gt0);
gt1 = __builtin_amdgcn_exp2f(gt1); PIN(gt1);
{ float v = T[1] * Y[0][1]; PIN(v); T[1] = v; }
SB(); M32A(X[1], AF(0), xb[1][20]); SB();
afh[3] = TR(lbA, 47616);
gt0 = __builtin_amdgcn_rcpf(gt0); PIN(gt0);
gt1 = gt1 + 1.0f; PIN(gt1);
SB(); M32A(X[0], AF(1), xb[0][21]); SB();
afl[0] = TR(lbA, 49152);
SETM0(wv2k + 32768);
{ float v = T[30] * gt0; PIN(v); T[30] = v; }
gt1 = __builtin_amdgcn_rcpf(gt1); PIN(gt1);
SB(); M32A(X[1], AF(1), xb[1][21]); SB();
afh[0] = TR(lbA, 49664);
GLDS(lvoffA, lbase, 0);
{ float v = T[31] * gt1; PIN(v); T[31] = v; }
{ unsigned u = pk_bf16(T[0], T[1]); PIN(u); hh[0] = u; }
SB(); M32A(X[0], AF(2), xb[0][22]); SB();
afl[1] = TR(lbA, 51200);
{ float v = T[2] * Y[0][2]; PIN(v); T[2] = v; }
{ float v = T[3] * Y[0][3]; PIN(v); T[3] = v; }
{ float v = T[4] * Y[0][8]; PIN(v); T[4] = v; }
{ float v = T[5] * Y[0][9]; PIN(v); T[5] = v; }
SB(); M32A(X[1], AF(2), xb[1][22]); SB();
afh[1] = TR(lbA, 51712);
{ unsigned u = pk_bf16(T[2], T[3]); PIN(u); hh[1] = u; }
{ unsigned u = pk_bf16(T[4], T[5]); PIN(u); hh[2] = u; }
{ float v = T[6] * Y[0][10]; PIN(v); T[6] = v; }
SB(); M32A(X[0], AF(3), xb[0][23]); SB();
afl[2] = TR(lbA, 53248);
GLDS(lvoffB, lbase, 1024);
{ float v = T[7] * Y[0][11]; PIN(v); T[7] = v; }
{ float v = T[8] * Y[0][4]; PIN(v); T[8] = v; }
{ float v = T[9] * Y[0][5]; PIN(v); T[9] = v; }
SB(); M32A(X[1], AF(3), xb[1][23]); SB();
afh[2] = TR(lbA, 53760);
lbase += lstep;
SB();
asm volatile("s_waitcnt vmcnt(13)\n\ts_barrier" ::: "memory");
SB(); M32A(X[0], AF(0), xb[0][24]); SB();
afl[3] = TR(lbA, 55296);
{ unsigned u = pk_bf16(T[6], T[7]); PIN(u); hh[3] = u; }
{ float v = T[10] * Y[0][6]; PIN(v); T[10] = v; }
{ float v = T[11] * Y[0][7]; PIN(v); T[11] = v; }
{ float v = T[12] * Y[0][12]; PIN(v); T[12] = v; }
SB(); M32A(X[1], AF(0), xb[1][24]); SB();
afh[3] = TR(lbA, 55808);
*(AS3 u32x4*)(uintptr_t)(hw_ad + 0) = hh;
{ float v = T[13] * Y[0][13]; PIN(v); T[13] = v; }
SB(); M32A(X[0], AF(1), xb[0][25]); SB();
afl[0] = TR(lbA, 57344);
SETM0(wv2k + 40960);
ST41(hxvo, hh, hxw + 0);
SB(); M32A(X[1], AF(1), xb[1][25]); SB();
afh[0] = TR(lbA, 57856);
GLDS(lvoffA, lbase, 0);
{ unsigned u = pk_bf16(T[8], T[9]); PIN(u); hh[0] = u; }
{ unsigned u = pk_bf16(T[10], T[11]); PIN(u); hh[1] = u; }
SB(); M32A(X[0], AF(2), xb[0][26]); SB();
afl[1] = TR(lbA, 59392);
{ unsigned u = pk_bf16(T[12], T[13]); PIN(u); hh[2] = u; }
{ float v = T[14] * Y[0][14]; PIN(v); T[14] = v; }
{ float v = T[15] * Y[0][15]; PIN(v); T[15] = v; }
{ float v = T[16] * Y[1][0]; PIN(v); T[16] = v; }
SB(); M32A(X[1], AF(2), xb[1][26]); SB();
afh[1] = TR(lbA, 59904);
{ unsigned u = pk_bf16(T[14], T[15]); PIN(u); hh[3] = u; }
{ float v = T[17] * Y[1][1]; PIN(v); T[17] = v; }
{ float v = T[18] * Y[1][2]; PIN(v); T[18] = v; }
SB(); M32A(X[0], AF(3), xb[0][27]); SB();
afl[2] = TR(lbA, 61440);
GLDS(lvoffB, lbase, 1024);
*(AS3 u32x4*)(uintptr_t)(hw_ad + 512) = hh;
{ float v = T[19] * Y[1][3]; PIN(v); T[19] = v; }
SB(); M32A(X[1], AF(3), xb[1][27]); SB();
afh[2] = TR(lbA, 61952);
lbase += lstep;
SB();
asm volatile("s_waitcnt vmcnt(14)\n\ts_barrier" ::: "memory");
SB(); M32A(X[0], AF(0), xb[0][28]); SB();
afl[3] = TR(lbA, 63488);
ST41(hxvo, hh, hxw + 512);
{ float v = T[20] * Y[1][8]; PIN(v); T[20] = v; }
SB(); M32A(X[1], AF(0), xb[1][28]); SB();
afh[3] = TR(lbA, 64000);
{ unsigned u = pk_bf16(T[16], T[17]); PIN(u); hh[0] = u; }
{ unsigned u = pk_bf16(T[18], T[19]); PIN(u); hh[1] = u; }
{ float v = T[21] * Y[1][9]; PIN(v); T[21] = v; }
SB(); M32A(X[0], AF(1), xb[0][29]); SB();
afl[0] = TR(lbA, 65536);
SETM0(wv2k + 49152);
{ unsigned u = pk_bf16(T[20], T[21]); PIN(u); hh[2] = u; }
{ float v = T[22] * Y[1][10]; PIN(v); T[22] = v; }
{ float v = T[23] * Y[1][11]; PIN(v); T[23] = v; }
SB(); M32A(X[1], AF(1), xb[1][29]); SB();
afh[0] = TR(lbA, 66048);
GLDS(lvoffA, lbase, 0);
{ unsigned u = pk_bf16(T[22], T[23]); PIN(u); hh[3] = u; }
{ float v = T[24] * Y[1][4]; PIN(v); T[24] = v; }
SB(); M32V(X[0], AF(2), xb[0][30]); SB();
afl[1] = TR(lbA, 67584);
*(AS3 u32x4*)(uintptr_t)(hw_ad + 2048) = hh;
{ float v = T[25] * Y[1][5]; PIN(v); T[25] = v; }
{ float v = T[26] * Y[1][6]; PIN(v); T[26] = v; }
SB(); M32V(X[1], AF(2), xb[1][30]); SB();
afh[1] = TR(lbA, 68096);
ST41(hxvo, hh, hxw + 2048);
SB(); M32V(X[0], AF(3), xb[0][31]); SB();
afl[2] = TR(lbA, 69632);
GLDS(lvoffB, lbase, 1024);
{ unsigned u = pk_bf16(T[24], T[25]); PIN(u); hh[0] = u; }
{ float v = T[27] * Y[1][7]; PIN(v); T[27] = v; }
{ float v = T[28] * Y[1][12]; PIN(v); T[28] = v; }
SB(); M32V(X[1], AF(3), xb[1][31]); SB();
afh[2] = TR(lbA, 70144);
lbase += lstep;
SB();
asm volatile("s_waitcnt vmcnt(16)\n\ts_barrier" ::: "memory");
SB(); M32V(X[0], AF(0), xb[0][32]); SB();
afl[3] = TR(lbA, 71680);
{ unsigned u = pk_bf16(T[26], T[27]); PIN(u); hh[1] = u; }
{ float v = T[29] * Y[1][13]; PIN(v); T[29] = v; }
{ float v = T[30] * Y[1][14]; PIN(v); T[30] = v; }
{ float v = T[31] * Y[1][15]; PIN(v); T[31] = v; }
SB(); M32V(X[1], AF(0), xb[1][32]); SB();
afh[3] = TR(lbA, 72192);
{ unsigned u = pk_bf16(T[28], T[29]); PIN(u); hh[2] = u; }
{ unsigned u = pk_bf16(T[30], T[31]); PIN(u); hh[3] = u; }
SB(); M32V(X[0], AF(1), xb[0][33]); SB();
afl[0] = TR(lbA, 0);
SETM0(wv2k + 57344);
*(AS3 u32x4*)(uintptr_t)(hw_ad + 2560) = hh;
SB(); M32V(X[1], AF(1), xb[1][33]); SB();
afh[0] = TR(lbA, 512);
GLDS(lvoffA, lbase, 0);
SB(); M32V(X[0], AF(2), xb[0][34]); SB();
afl[1] = TR(lbA, 2048);
ST41(hxvo, hh, hxw + 2560);
SB(); M32V(X[1], AF(2), xb[1][34]); SB();
afh[1] = TR(lbA, 2560);
SB(); M32V(X[0], AF(3), xb[0][35]); SB();
afl[2] = TR(lbA, 4096);
GLDS(lvoffB, lbase, 1024);
SB(); M32V(X[1], AF(3), xb[1][35]); SB();
afh[2] = TR(lbA, 4608);
lbase += lstep;
}
  const int ln1 = (int)lane_id();
  const int atok = cg * 64 + ln1;
  const int atg = m * 128 + atok;
  const float ASC = 0.23570226039551584f;
  const char* asrc = (a.ws + WS_QKV) + (size_t)(m * 432 + 9 * s) * 1024;
  const char* hsrc = (a.ws + WS_QKV) + (size_t)(mp * 432 + 144 + 9 * s) * 1024 + 896;
  #define ADMA(i) { const int pc_ = (wave + 4 * (i)) < 26 ? (wave + 4 * (i)) : 26; const int ar_ = (pc_ * 57) >> 9; \
      asm volatile("s_mov_b32 m0, %2\n\ts_nop 0\n\tglobal_load_lds_dwordx4 %0, %1 sc1" :: "v"(xo0), "s"(asrc + pc_ * 1024 + ar_ * 138240), "s"(ATT_OFF + 128 + pc_ * 1152) : "memory"); }
  #define HDMA(i) { const int hp_ = (wave + 4 * (i)) < 17 ? (wave + 4 * (i)) : 17; const int ar_ = (hp_ * 57) >> 9; \
      asm volatile("s_mov_b32 m0, %2\n\ts_mov_b64 exec, 0xff\n\tglobal_load_lds_dwordx4 %0, %1 sc1\n\ts_mov_b64 exec, -1" :: "v"(xo0), "s"(hsrc + hp_ * 1024 + ar_ * 138240), "s"(ATT_OFF + 10368 + hp_ * 1152) : "memory"); }

{
const char* nbase = wgb + 256; const unsigned nstride = 9216u;
SB();
asm volatile("s_waitcnt vmcnt(16)\n\ts_barrier" ::: "memory");
SB(); M32ZA(Y[0], AF(0), xb[0][0]); SB();
afl[3] = TR(lbA, 6144);
T[0] = X[0][0];
T[1] = X[0][1];
T[2] = X[0][2];
T[3] = X[0][3];
T[4] = X[0][8];
T[5] = X[0][9];
T[6] = X[0][10];
T[7] = X[0][11];
T[8] = X[0][4];
T[9] = X[0][5];
T[10] = X[0][6];
T[11] = X[0][7];
T[12] = X[0][12];
T[13] = X[0][13];
T[14] = X[0][14];
T[15] = X[0][15];
T[16] = X[1][0];
T[17] = X[1][1];
T[18] = X[1][2];
T[19] = X[1][3];
T[20] = X[1][8];
T[21] = X[1][9];
T[22] = X[1][10];
T[23] = X[1][11];
T[24] = X[1][4];
T[25] = X[1][5];
T[26] = X[1][6];
T[27] = X[1][7];
T[28] = X[1][12];
T[29] = X[1][13];
T[30] = X[1][14];
T[31] = X[1][15];
SB(); M32ZA(Y[1], AF(0), xb[1][0]); SB();
afh[3] = TR(lbA, 6656);
SB(); M32A(Y[0], AF(1), xb[0][1]); SB();
afl[0] = TR(lbA, 8192);
SETM0(wv2k + 65536);
SB(); M32A(Y[1], AF(1), xb[1][1]); SB();
afh[0] = TR(lbA, 8704);
GLDS(lvoffA, lbase, 0);
SB(); M32A(Y[0], AF(2), xb[0][2]); SB();
afl[1] = TR(lbA, 10240);
SB(); M32A(Y[1], AF(2), xb[1][2]); SB();
afh[1] = TR(lbA, 10752);
SB(); M32A(Y[0], AF(3), xb[0][3]); SB();
afl[2] = TR(lbA, 12288);
GLDS(lvoffB, lbase, 1024);
gt0 = T[0] * T[0]; PIN(gt0);
SB(); M32A(Y[1], AF(3), xb[1][3]); SB();
afh[2] = TR(lbA, 12800);
lbase = nbase; lstep = 64 * nstride; lvoffA = rowA * nstride + c16P1; lvoffB = (rowA + 8) * nstride + c16P1 - 1024;
gt0 = __builtin_fmaf(gt0, C1, C0); PIN(gt0);
SB();
asm volatile("s_waitcnt vmcnt(16)\n\ts_barrier" ::: "memory");
SB(); M32A(Y[0], AF(0), xb[0][4]); SB();
afl[3] = TR(lbA, 14336);
gt0 = T[0] * gt0; PIN(gt0);
SB(); M32A(Y[1], AF(0), xb[1][4]); SB();
afh[3] = TR(lbA, 14848);
gt0 = __builtin_amdgcn_exp2f(gt0); PIN(gt0);
gt1 = T[1] * T[1]; PIN(gt1);
SB(); M32A(Y[0], AF(1), xb[0][5]); SB();
afl[0] = TR(lbA, 16384);
SETM0(wv2k + 0);
gt0 = gt0 + 1.0f; PIN(gt0);
gt1 = __builtin_fmaf(gt1, C1, C0); PIN(gt1);
SB(); M32A(Y[1], AF(1), xb[1][5]); SB();
afh[0] = TR(lbA, 16896);
GLDS(lvoffA, lbase, 0);
gt0 = __builtin_amdgcn_rcpf(gt0); PIN(gt0);
SB(); M32A(Y[0], AF(2), xb[0][6]); SB();
afl[1] = TR(lbA, 18432);
{ float v = T[0] * gt0; PIN(v); T[0] = v; }
gt1 = T[1] * gt1; PIN(gt1);
gt2 = T[2] * T[2]; PIN(gt2);
SB(); M32A(Y[1], AF(2), xb[1][6]); SB();
afh[1] = TR(lbA, 18944);
gt1 = __builtin_amdgcn_exp2f(gt1); PIN(gt1);
gt2 = __builtin_fmaf(gt2, C1, C0); PIN(gt2);
SB(); M32A(Y[0], AF(3), xb[0][7]); SB();
afl[2] = TR(lbA, 20480);
GLDS(lvoffB, lbase, 1024);
gt1 = gt1 + 1.0f; PIN(gt1);
gt2 = T[2] * gt2; PIN(gt2);
SB(); M32A(Y[1], AF(3), xb[1][7]); SB();
afh[2] = TR(lbA, 20992);
lbase += lstep;
gt0 = T[3] * T[3]; PIN(gt0);
SB();
asm volatile("s_waitcnt vmcnt(16)\n\ts_barrier" ::: "memory");
SB(); M32A(Y[0], AF(0), xb[0][8]); SB();
afl[3] = TR(lbA, 22528);
gt1 = __builtin_amdgcn_rcpf(gt1); PIN(gt1);
gt2 = __builtin_amdgcn_exp2f(gt2); PIN(gt2);
SB(); M32A(Y[1], AF(0), xb[1][8]); SB();
afh[3] = TR(lbA, 23040);
TOUCH(trow * 9216u, wgb + 384);
{ float v = T[1] * gt1; PIN(v); T[1] = v; }
gt2 = gt2 + 1.0f; PIN(gt2);
gt0 = __builtin_fmaf(gt0, C1, C0); PIN(gt0);
SB(); M32A(Y[0], AF(1), xb[0][9]); SB();
afl[0] = TR(lbA, 24576);
SETM0(wv2k + 8192);
gt2 = __builtin_amdgcn_rcpf(gt2); PIN(gt2);
gt0 = T[3] * gt0; PIN(gt0);
SB(); M32A(Y[1], AF(1), xb[1][9]); SB();
afh[0] = TR(lbA, 25088);
GLDS(lvoffA, lbase, 0);
{ float v = T[2] * gt2; PIN(v); T[2] = v; }
gt1 = T[4] * T[4]; PIN(gt1);
SB(); M32A(Y[0], AF(2), xb[0][10]); SB();
afl[1] = TR(lbA, 26624);
gt0 = __builtin_amdgcn_exp2f(gt0); PIN(gt0);
gt1 = __builtin_fmaf(gt1, C1, C0); PIN(gt1);
SB(); M32A(Y[1], AF(2), xb[1][10]); SB();
afh[1] = TR(lbA, 27136);
gt0 = gt0 + 1.0f; PIN(gt0);
gt1 = T[4] * gt1; PIN(gt1);
gt2 = T[5] * T[5]; PIN(gt2);
SB(); M32A(Y[0], AF(3), xb[0][11]); SB();
afl[2] = TR(lbA, 28672);
GLDS(lvoffB, lbase, 1024);
gt0 = __builtin_amdgcn_rcpf(gt0); PIN(gt0);
gt2 = __builtin_fmaf(gt2, C1, C0); PIN(gt2);
SB(); M32A(Y[1], AF(3), xb[1][11]); SB();
afh[2] = TR(lbA, 29184);
lbase += lstep;
{ float v = T[3] * gt0; PIN(v); T[3] = v; }
SB();
asm volatile("s_waitcnt vmcnt(8)\n\ts_barrier" ::: "memory");
SIGNAL_SET(fHme, 1, true);
SB(); M32A(Y[0], AF(0), xb[0][12]); SB();
afl[3] = TR(lbA, 30720);
gt1 = __builtin_amdgcn_exp2f(gt1); PIN(gt1);
gt2 = T[5] * gt2; PIN(gt2);
gt0 = T[6] * T[6]; PIN(gt0);
SB(); M32A(Y[1], AF(0), xb[1][12]); SB();
afh[3] = TR(lbA, 31232);
gt1 = gt1 + 1.0f; PIN(gt1);
gt2 = __builtin_amdgcn_exp2f(gt2); PIN(gt2);
gt0 = __builtin_fmaf(gt0, C1, C0); PIN(gt0);
SB(); M32A(Y[0], AF(1), xb[0][13]); SB();
afl[0] = TR(lbA, 32768);
SETM0(wv2k + 16384);
gt1 = __builtin_amdgcn_rcpf(gt1); PIN(gt1);
gt2 = gt2 + 1.0f; PIN(gt2);
SB(); M32A(Y[1], AF(1), xb[1][13]); SB();
afh[0] = TR(lbA, 33280);
GLDS(lvoffA, lbase, 0);
{ float v = T[4] * gt1; PIN(v); T[4] = v; }
gt0 = T[6] * gt0; PIN(gt0);
SB(); M32A(Y[0], AF(2), xb[0][14]); SB();
afl[1] = TR(lbA, 34816);
gt2 = __builtin_amdgcn_rcpf(gt2); PIN(gt2);
gt0 = __builtin_amdgcn_exp2f(gt0); PIN(gt0);
SB(); M32A(Y[1], AF(2), xb[1][14]); SB();
afh[1] = TR(lbA, 35328);
{ float v = T[5] * gt2; PIN(v); T[5] = v; }
gt0 = gt0 + 1.0f; PIN(gt0);
gt1 = T[7] * T[7]; PIN(gt1);
SB(); M32A(Y[0], AF(3), xb[0][15]); SB();
afl[2] = TR(lbA, 36864);
GLDS(lvoffB, lbase, 1024);
gt0 = __builtin_amdgcn_rcpf(gt0); PIN(gt0);
gt1 = __builtin_fmaf(gt1, C1, C0); PIN(gt1);
SB(); M32A(Y[1], AF(3), xb[1][15]); SB();
afh[2] = TR(lbA, 37376);
lbase += lstep;
{ float v = T[6] * gt0; PIN(v); T[6] = v; }
SB();
asm volatile("s_waitcnt vmcnt(16)\n\ts_barrier" ::: "memory");
SB(); M32A(Y[0], AF(0), xb[0][16]); SB();
afl[3] = TR(lbA, 38912);
gt1 = T[7] * gt1; PIN(gt1);
gt2 = T[8] * T[8]; PIN(gt2);
SB(); M32A(Y[1], AF(0), xb[1][16]); SB();
afh[3] = TR(lbA, 39424);
gt1 = __builtin_amdgcn_exp2f(gt1); PIN(gt1);
gt2 = __builtin_fmaf(gt2, C1, C0); PIN(gt2);
gt0 = T[9] * T[9]; PIN(gt0);
SB(); M32A(Y[0], AF(1), xb[0][17]); SB();
afl[0] = TR(lbA, 40960);
SETM0(wv2k + 24576);
gt1 = gt1 + 1.0f; PIN(gt1);
gt2 = T[8] * gt2; PIN(gt2);
gt0 = __builtin_fmaf(gt0, C1, C0); PIN(gt0);
SB(); M32A(Y[1], AF(1), xb[1][17]); SB();
afh[0] = TR(lbA, 41472);
GLDS(lvoffA, lbase, 0);
gt1 = __builtin_amdgcn_rcpf(gt1); PIN(gt1);
SB(); M32A(Y[0], AF(2), xb[0][18]); SB();
afl[1] = TR(lbA, 43008);
{ float v = T[7] * gt1; PIN(v); T[7] = v; }
gt2 = __builtin_amdgcn_exp2f(gt2); PIN(gt2);
gt0 = T[9] * gt0; PIN(gt0);
SB(); M32A(Y[1], AF(2), xb[1][18]); SB();
afh[1] = TR(lbA, 43520);
gt2 = gt2 + 1.0f; PIN(gt2);
gt0 = __builtin_amdgcn_exp2f(gt0); PIN(gt0);
gt1 = T[10] * T[10]; PIN(gt1);
SB(); M32A(Y[0], AF(3), xb[0][19]); SB();
afl[2] = TR(lbA, 45056);
GLDS(lvoffB, lbase, 1024);
gt2 = __builtin_amdgcn_rcpf(gt2); PIN(gt2);
gt0 = gt0 + 1.0f; PIN(gt0);
SB(); M32A(Y[1], AF(3), xb[1][19]); SB();
afh[2] = TR(lbA, 45568);
lbase += lstep;
{ float v = T[8] * gt2; PIN(v); T[8] = v; }
SB();
asm volatile("s_waitcnt vmcnt(14)\n\ts_barrier" ::: "memory");
SB(); M32A(Y[0], AF(0), xb[0][20]); SB();
afl[3] = TR(lbA, 47104);
gt0 = __builtin_amdgcn_rcpf(gt0); PIN(gt0);
gt1 = __builtin_fmaf(gt1, C1, C0); PIN(gt1);
gt2 = T[11] * T[11]; PIN(gt2);
SB(); M32A(Y[1], AF(0), xb[1][20]); SB();
afh[3] = TR(lbA, 47616);
{ float v = T[9] * gt0; PIN(v); T[9] = v; }
gt1 = T[10] * gt1; PIN(gt1);
gt2 = __builtin_fmaf(gt2, C1, C0); PIN(gt2);
SB(); M32A(Y[0], AF(1), xb[0][21]); SB();
afl[0] = TR(lbA, 49152);
SETM0(wv2k + 32768);
gt1 = __builtin_amdgcn_exp2f(gt1); PIN(gt1);
gt2 = T[11] * gt2; PIN(gt2);
SB(); M32A(Y[1], AF(1), xb[1][21]); SB();
afh[0] = TR(lbA, 49664);
GLDS(lvoffA, lbase, 0);
gt1 = gt1 + 1.0f; PIN(gt1);
gt0 = T[12] * T[12]; PIN(gt0);
SB(); M32A(Y[0], AF(2), xb[0][22]); SB();
afl[1] = TR(lbA, 51200);
gt1 = __builtin_amdgcn_rcpf(gt1); PIN(gt1);
gt2 = __builtin_amdgcn_exp2f(gt2); PIN(gt2);
SB(); M32A(Y[1], AF(2), xb[1][22]); SB();
afh[1] = TR(lbA, 51712);
{ float v = T[10] * gt1; PIN(v); T[10] = v; }
gt2 = gt2 + 1.0f; PIN(gt2);
gt0 = __builtin_fmaf(gt0, C1, C0); PIN(gt0);
SB(); M32A(Y[0], AF(3), xb[0][23]); SB();
afl[2] = TR(lbA, 53248);
GLDS(lvoffB, lbase, 1024);
gt2 = __builtin_amdgcn_rcpf(gt2); PIN(gt2);
gt0 = T[12] * gt0; PIN(gt0);
SB(); M32A(Y[1], AF(3), xb[1][23]); SB();
afh[2] = TR(lbA, 53760);
lbase += lstep;
{ float v = T[11] * gt2; PIN(v); T[11] = v; }
SB();
asm volatile("s_waitcnt vmcnt(13)\n\ts_barrier" ::: "memory");
SB(); M32A(Y[0], AF(0), xb[0][24]); SB();
afl[3] = TR(lbA, 55296);
gt0 = __builtin_amdgcn_exp2f(gt0); PIN(gt0);
gt1 = T[13] * T[13]; PIN(gt1);
gt2 = T[14] * T[14]; PIN(gt2);
SB(); M32A(Y[1], AF(0), xb[1][24]); SB();
afh[3] = TR(lbA, 55808);
gt0 = gt0 + 1.0f; PIN(gt0);
gt1 = __builtin_fmaf(gt1, C1, C0); PIN(gt1);
gt2 = __builtin_fmaf(gt2, C1, C0); PIN(gt2);
SB(); M32A(Y[0], AF(1), xb[0][25]); SB();
afl[0] = TR(lbA, 57344);
SETM0(wv2k + 40960);
LDW1(pw0, zvo, fQ);
gt1 = T[13] * gt1; PIN(gt1);
SB(); M32A(Y[1], AF(1), xb[1][25]); SB();
afh[0] = TR(lbA, 57856);
GLDS(lvoffA, lbase, 0);
LDW1(pw1, zvo, fQp);
SB(); M32A(Y[0], AF(2), xb[0][26]); SB();
afl[1] = TR(lbA, 59392);
gt0 = __builtin_amdgcn_rcpf(gt0); PIN(gt0);
gt1 = __builtin_amdgcn_exp2f(gt1); PIN(gt1);
SB(); M32A(Y[1], AF(2), xb[1][26]); SB();
afh[1] = TR(lbA, 59904);
{ float v = T[12] * gt0; PIN(v); T[12] = v; }
gt1 = gt1 + 1.0f; PIN(gt1);
gt2 = T[14] * gt2; PIN(gt2);
SB(); M32A(Y[0], AF(3), xb[0][27]); SB();
afl[2] = TR(lbA, 61440);
GLDS(lvoffB, lbase, 1024);
gt1 = __builtin_amdgcn_rcpf(gt1); PIN(gt1);
gt0 = T[15] * T[15]; PIN(gt0);
SB(); M32A(Y[1], AF(3), xb[1][27]); SB();
afh[2] = TR(lbA, 61952);
lbase += lstep;
{ float v = T[13] * gt1; PIN(v); T[13] = v; }
SB();
asm volatile("s_waitcnt vmcnt(15)\n\ts_barrier" ::: "memory");
SB(); M32A(Y[0], AF(0), xb[0][28]); SB();
afl[3] = TR(lbA, 63488);
gt2 = __builtin_amdgcn_exp2f(gt2); PIN(gt2);
gt0 = __builtin_fmaf(gt0, C1, C0); PIN(gt0);
gt1 = T[16] * T[16]; PIN(gt1);
SB(); M32A(Y[1], AF(0), xb[1][28]); SB();
afh[3] = TR(lbA, 64000);
gt2 = gt2 + 1.0f; PIN(gt2);
gt0 = T[15] * gt0; PIN(gt0);
gt1 = __builtin_fmaf(gt1, C1, C0); PIN(gt1);
SB(); M32A(Y[0], AF(1), xb[0][29]); SB();
afl[0] = TR(lbA, 65536);
SETM0(wv2k + 49152);
gt2 = __builtin_amdgcn_rcpf(gt2); PIN(gt2);
gt1 = T[16] * gt1; PIN(gt1);
SB(); M32A(Y[1], AF(1), xb[1][29]); SB();
afh[0] = TR(lbA, 66048);
GLDS(lvoffA, lbase, 0);
{ float v = T[14] * gt2; PIN(v); T[14] = v; }
SB(); M32V(Y[0], AF(2), xb[0][30]); SB();
afl[1] = TR(lbA, 67584);
gt0 = __builtin_amdgcn_exp2f(gt0); PIN(gt0);
gt1 = __builtin_amdgcn_exp2f(gt1); PIN(gt1);
SB(); M32V(Y[1], AF(2), xb[1][30]); SB();
afh[1] = TR(lbA, 68096);
gt0 = gt0 + 1.0f; PIN(gt0);
gt1 = gt1 + 1.0f; PIN(gt1);
gt2 = T[17] * T[17]; PIN(gt2);
SB(); M32V(Y[0], AF(3), xb[0][31]); SB();
afl[2] = TR(lbA, 69632);
GLDS(lvoffB, lbase, 1024);
gt0 = __builtin_amdgcn_rcpf(gt0); PIN(gt0);
gt2 = __builtin_fmaf(gt2, C1, C0); PIN(gt2);
SB(); M32V(Y[1], AF(3), xb[1][31]); SB();
afh[2] = TR(lbA, 70144);
lbase += lstep;
{ float v = T[15] * gt0; PIN(v); T[15] = v; }
SB();
asm volatile("s_waitcnt vmcnt(15)\n\ts_barrier" ::: "memory");
SB(); M32V(Y[0], AF(0), xb[0][32]); SB();
afl[3] = TR(lbA, 71680);
gt1 = __builtin_amdgcn_rcpf(gt1); PIN(gt1);
gt2 = T[17] * gt2; PIN(gt2);
gt0 = T[18] * T[18]; PIN(gt0);
SB(); M32V(Y[1], AF(0), xb[1][32]); SB();
afh[3] = TR(lbA, 72192);
{ float v = T[16] * gt1; PIN(v); T[16] = v; }
gt2 = __builtin_amdgcn_exp2f(gt2); PIN(gt2);
gt0 = __builtin_fmaf(gt0, C1, C0); PIN(gt0);
SB(); M32V(Y[0], AF(1), xb[0][33]); SB();
afl[0] = TR(lbA, 0);
SETM0(wv2k + 57344);
gt2 = gt2 + 1.0f; PIN(gt2);
gt0 = T[18] * gt0; PIN(gt0);
gt1 = T[19] * T[19]; PIN(gt1);
SB(); M32V(Y[1], AF(1), xb[1][33]); SB();
afh[0] = TR(lbA, 512);
GLDS(lvoffA, lbase, 0);
gt2 = __builtin_amdgcn_rcpf(gt2); PIN(gt2);
SB(); M32V(Y[0], AF(2), xb[0][34]); SB();
afl[1] = TR(lbA, 2048);
{ float v = T[17] * gt2; PIN(v); T[17] = v; }
gt0 = __builtin_amdgcn_exp2f(gt0); PIN(gt0);
gt1 = __builtin_fmaf(gt1, C1, C0); PIN(gt1);
SB(); M32V(Y[1], AF(2), xb[1][34]); SB();
afh[1] = TR(lbA, 2560);
gt0 = gt0 + 1.0f; PIN(gt0);
gt1 = T[19] * gt1; PIN(gt1);
gt2 = T[20] * T[20]; PIN(gt2);
SB(); M32V(Y[0], AF(3), xb[0][35]); SB();
afl[2] = TR(lbA, 4096);
GLDS(lvoffB, lbase, 1024);
gt0 = __builtin_amdgcn_rcpf(gt0); PIN(gt0);
gt2 = __builtin_fmaf(gt2, C1, C0); PIN(gt2);
SB(); M32V(Y[1], AF(3), xb[1][35]); SB();
afh[2] = TR(lbA, 4608);
lbase += lstep;
{ float v = T[18] * gt0; PIN(v); T[18] = v; }
}
{
const char* nbase = wub + 256; const unsigned nstride = 9216u;
SB();
asm volatile("s_waitcnt vmcnt(5)\n\ts_barrier" : "+v"(pw0), "+v"(pw1) :: "memory");
CHECK_GE(pw0, fQ, 27);
CHECK_GE(pw1, fQp, 27);
SB(); M32ZA(X[0], AF(0), xb[0][0]); SB();
afl[3] = TR(lbA, 6144);
gt1 = __builtin_amdgcn_exp2f(gt1); PIN(gt1);
gt2 = T[20] * gt2; PIN(gt2);
gt0 = T[21] * T[21]; PIN(gt0);
SB(); M32ZA(X[1], AF(0), xb[1][0]); SB();
afh[3] = TR(lbA, 6656);
ADMA(0);
gt1 = gt1 + 1.0f; PIN(gt1);
SB(); M32A(X[0], AF(1), xb[0][1]); SB();
afl[0] = TR(lbA, 8192);
SETM0(wv2k + 65536);
gt1 = __builtin_amdgcn_rcpf(gt1); PIN(gt1);
gt0 = __builtin_fmaf(gt0, C1, C0); PIN(gt0);
SB(); M32A(X[1], AF(1), xb[1][1]); SB();
afh[0] = TR(lbA, 8704);
GLDS(lvoffA, lbase, 0);
{ float v = T[19] * gt1; PIN(v); T[19] = v; }
gt0 = T[21] * gt0; PIN(gt0);
SB(); M32A(X[0], AF(2), xb[0][2]); SB();
afl[1] = TR(lbA, 10240);
gt2 = __builtin_amdgcn_exp2f(gt2); PIN(gt2);
gt0 = __builtin_amdgcn_exp2f(gt0); PIN(gt0);
SB(); M32A(X[1], AF(2), xb[1][2]); SB();
afh[1] = TR(lbA, 10752);
gt2 = gt2 + 1.0f; PIN(gt2);
gt0 = gt0 + 1.0f; PIN(gt0);
gt1 = T[22] * T[22]; PIN(gt1);
SB(); M32A(X[0], AF(3), xb[0][3]); SB();
afl[2] = TR(lbA, 12288);
GLDS(lvoffB, lbase, 1024);
gt2 = __builtin_amdgcn_rcpf(gt2); PIN(gt2);
gt1 = __builtin_fmaf(gt1, C1, C0); PIN(gt1);
SB(); M32A(X[1], AF(3), xb[1][3]); SB();
afh[2] = TR(lbA, 12800);
lbase = nbase; lstep = 64 * nstride; lvoffA = rowA * nstride + c16P1; lvoffB = (rowA + 8) * nstride + c16P1 - 1024;
ADMA(1);
ADMA(2);
ADMA(3);
SB();
asm volatile("s_waitcnt vmcnt(18)\n\ts_barrier" ::: "memory");
SB(); M32A(X[0], AF(0), xb[0][4]); SB();
afl[3] = TR(lbA, 14336);
ADMA(4);
{ float v = T[20] * gt2; PIN(v); T[20] = v; }
gt1 = T[22] * gt1; PIN(gt1);
SB(); M32A(X[1], AF(0), xb[1][4]); SB();
afh[3] = TR(lbA, 14848);
ADMA(5);
gt2 = T[23] * T[23]; PIN(gt2);
SB(); M32A(X[0], AF(1), xb[0][5]); SB();
afl[0] = TR(lbA, 16384);
SETM0(wv2k + 0);
gt0 = __builtin_amdgcn_rcpf(gt0); PIN(gt0);
gt2 = __builtin_fmaf(gt2, C1, C0); PIN(gt2);
SB(); M32A(X[1], AF(1), xb[1][5]); SB();
afh[0] = TR(lbA, 16896);
GLDS(lvoffA, lbase, 0);
{ float v = T[21] * gt0; PIN(v); T[21] = v; }
gt2 = T[23] * gt2; PIN(gt2);
SB(); M32A(X[0], AF(2), xb[0][6]); SB();
afl[1] = TR(lbA, 18432);
gt1 = __builtin_amdgcn_exp2f(gt1); PIN(gt1);
gt2 = __builtin_amdgcn_exp2f(gt2); PIN(gt2);
SB(); M32A(X[1], AF(2), xb[1][6]); SB();
afh[1] = TR(lbA, 18944);
gt1 = gt1 + 1.0f; PIN(gt1);
gt2 = gt2 + 1.0f; PIN(gt2);
gt0 = T[24] * T[24]; PIN(gt0);
{ float v = T[0] * Y[0][0]; PIN(v); T[0] = v; }
SB(); M32A(X[0], AF(3), xb[0][7]); SB();
afl[2] = TR(lbA, 20480);
GLDS(lvoffB, lbase, 1024);
gt1 = __builtin_amdgcn_rcpf(gt1); PIN(gt1);
gt0 = __builtin_fmaf(gt0, C1, C0); PIN(gt0);
SB(); M32A(X[1], AF(3), xb[1][7]); SB();
afh[2] = TR(lbA, 20992);
lbase += lstep;
ADMA(6);
HDMA(0);
SB();
asm volatile("s_waitcnt vmcnt(22)\n\ts_barrier" ::: "memory");
SB(); M32A(X[0], AF(0), xb[0][8]); SB();
afl[3] = TR(lbA, 22528);
HDMA(1);
{ float v = T[22] * gt1; PIN(v); T[22] = v; }
SB(); M32A(X[1], AF(0), xb[1][8]); SB();
afh[3] = TR(lbA, 23040);
TOUCH(trow * 9216u, wub + 384);
HDMA(2);
SB(); M32A(X[0], AF(1), xb[0][9]); SB();
afl[0] = TR(lbA, 24576);
SETM0(wv2k + 8192);
gt2 = __builtin_amdgcn_rcpf(gt2); PIN(gt2);
gt0 = T[24] * gt0; PIN(gt0);
SB(); M32A(X[1], AF(1), xb[1][9]); SB();
afh[0] = TR(lbA, 25088);
GLDS(lvoffA, lbase, 0);
{ float v = T[23] * gt2; PIN(v); T[23] = v; }
gt1 = T[25] * T[25]; PIN(gt1);
SB(); M32A(X[0], AF(2), xb[0][10]); SB();
afl[1] = TR(lbA, 26624);
gt0 = __builtin_amdgcn_exp2f(gt0); PIN(gt0);
gt1 = __builtin_fmaf(gt1, C1, C0); PIN(gt1);
gt2 = T[26] * T[26]; PIN(gt2);
SB(); M32A(X[1], AF(2), xb[1][10]); SB();
afh[1] = TR(lbA, 27136);
gt0 = gt0 + 1.0f; PIN(gt0);
gt1 = T[25] * gt1; PIN(gt1);
gt2 = __builtin_fmaf(gt2, C1, C0); PIN(gt2);
{ float v = T[1] * Y[0][1]; PIN(v); T[1] = v; }
SB(); M32A(X[0], AF(3), xb[0][11]); SB();
afl[2] = TR(lbA, 28672);
GLDS(lvoffB, lbase, 1024);
gt0 = __builtin_amdgcn_rcpf(gt0); PIN(gt0);
gt2 = T[26] * gt2; PIN(gt2);
SB(); M32A(X[1], AF(3), xb[1][11]); SB();
afh[2] = TR(lbA, 29184);
lbase += lstep;
HDMA(3);
HDMA(4);
SB();
asm volatile("s_waitcnt vmcnt(27)\n\ts_barrier" ::: "memory");
SB(); M32A(X[0], AF(0), xb[0][12]); SB();
afl[3] = TR(lbA, 30720);
{ float v = T[24] * gt0; PIN(v); T[24] = v; }
gt1 = __builtin_amdgcn_exp2f(gt1); PIN(gt1);
{ unsigned u = pk_bf16(T[0], T[1]); PIN(u); hh[0] = u; }
SB(); M32A(X[1], AF(0), xb[1][12]); SB();
afh[3] = TR(lbA, 31232);
gt1 = gt1 + 1.0f; PIN(gt1);
gt2 = __builtin_amdgcn_exp2f(gt2); PIN(gt2);
gt0 = T[27] * T[27]; PIN(gt0);
SB(); M32A(X[0], AF(1), xb[0][13]); SB();
afl[0] = TR(lbA, 32768);
SETM0(wv2k + 16384);
gt1 = __builtin_amdgcn_rcpf(gt1); PIN(gt1);
gt2 = gt2 + 1.0f; PIN(gt2);
SB(); M32A(X[1], AF(1), xb[1][13]); SB();
afh[0] = TR(lbA, 33280);
GLDS(lvoffA, lbase, 0);
{ float v = T[25] * gt1; PIN(v); T[25] = v; }
gt0 = __builtin_fmaf(gt0, C1, C0); PIN(gt0);
SB(); M32A(X[0], AF(2), xb[0][14]); SB();
afl[1] = TR(lbA, 34816);
gt2 = __builtin_amdgcn_rcpf(gt2); PIN(gt2);
gt0 = T[27] * gt0; PIN(gt0);
gt1 = T[28] * T[28]; PIN(gt1);
SB(); M32A(X[1], AF(2), xb[1][14]); SB();
afh[1] = TR(lbA, 35328);
{ float v = T[26] * gt2; PIN(v); T[26] = v; }
gt0 = __builtin_amdgcn_exp2f(gt0); PIN(gt0);
gt1 = __builtin_fmaf(gt1, C1, C0); PIN(gt1);
SB(); M32A(X[0], AF(3), xb[0][15]); SB();
afl[2] = TR(lbA, 36864);
GLDS(lvoffB, lbase, 1024);
gt0 = gt0 + 1.0f; PIN(gt0);
gt1 = T[28] * gt1; PIN(gt1);
gt2 = T[29] * T[29]; PIN(gt2);
SB(); M32A(X[1], AF(3), xb[1][15]); SB();
afh[2] = TR(lbA, 37376);
lbase += lstep;
gt2 = __builtin_fmaf(gt2, C1, C0); PIN(gt2);
SB();
asm volatile("s_waitcnt vmcnt(25)\n\ts_barrier" ::: "memory");
SB(); M32A(X[0], AF(0), xb[0][16]); SB();
afl[3] = TR(lbA, 38912);
gt0 = __builtin_amdgcn_rcpf(gt0); PIN(gt0);
gt1 = __builtin_amdgcn_exp2f(gt1); PIN(gt1);
SB(); M32A(X[1], AF(0), xb[1][16]); SB();
afh[3] = TR(lbA, 39424);
{ float v = T[27] * gt0; PIN(v); T[27] = v; }
gt1 = gt1 + 1.0f; PIN(gt1);
gt2 = T[29] * gt2; PIN(gt2);
{ float v = T[2] * Y[0][2]; PIN(v); T[2] = v; }
SB(); M32A(X[0], AF(1), xb[0][17]); SB();
afl[0] = TR(lbA, 40960);
SETM0(wv2k + 24576);
gt1 = __builtin_amdgcn_rcpf(gt1); PIN(gt1);
gt0 = T[30] * T[30]; PIN(gt0);
SB(); M32A(X[1], AF(1), xb[1][17]); SB();
afh[0] = TR(lbA, 41472);
GLDS(lvoffA, lbase, 0);
{ float v = T[28] * gt1; PIN(v); T[28] = v; }
gt0 = __builtin_fmaf(gt0, C1, C0); PIN(gt0);
SB(); M32A(X[0], AF(2), xb[0][18]); SB();
afl[1] = TR(lbA, 43008);
gt2 = __builtin_amdgcn_exp2f(gt2); PIN(gt2);
gt0 = T[30] * gt0; PIN(gt0);
gt1 = T[31] * T[31]; PIN(gt1);
SB(); M32A(X[1], AF(2), xb[1][18]); SB();
afh[1] = TR(lbA, 43520);
gt2 = gt2 + 1.0f; PIN(gt2);
gt0 = __builtin_amdgcn_exp2f(gt0); PIN(gt0);
gt1 = __builtin_fmaf(gt1, C1, C0); PIN(gt1);
SB(); M32A(X[0], AF(3), xb[0][19]); SB();
afl[2] = TR(lbA, 45056);
GLDS(lvoffB, lbase, 1024);
gt2 = __builtin_amdgcn_rcpf(gt2); PIN(gt2);
gt0 = gt0 + 1.0f; PIN(gt0);
SB(); M32A(X[1], AF(3), xb[1][19]); SB();
afh[2] = TR(lbA, 45568);
lbase += lstep;
{ float v = T[29] * gt2; PIN(v); T[29] = v; }
SB();
asm volatile("s_waitcnt vmcnt(25)\n\ts_barrier" ::: "memory");
SB(); M32A(X[0], AF(0), xb[0][20]); SB();
afl[3] = TR(lbA, 47104);
gt0 = __builtin_amdgcn_rcpf(gt0); PIN(gt0);
gt1 = T[31] * gt1; PIN(gt1);
{ float v = T[3] * Y[0][3]; PIN(v); T[3] = v; }
SB(); M32A(X[1], AF(0), xb[1][20]); SB();
afh[3] = TR(lbA, 47616);
{ float v = T[30] * gt0; PIN(v); T[30] = v; }
gt1 = __builtin_amdgcn_exp2f(gt1); PIN(gt1);
{ unsigned u = pk_bf16(T[2], T[3]); PIN(u); hh[1] = u; }
SB(); M32A(X[0], AF(1), xb[0][21]); SB();
afl[0] = TR(lbA, 49152);
SETM0(wv2k + 32768);
gt1 = gt1 + 1.0f; PIN(gt1);
{ float v = T[4] * Y[0][8]; PIN(v); T[4] = v; }
{ float v = T[5] * Y[0][9]; PIN(v); T[5] = v; }
SB(); M32A(X[1], AF(1), xb[1][21]); SB();
afh[0] = TR(lbA, 49664);
GLDS(lvoffA, lbase, 0);
gt1 = __builtin_amdgcn_rcpf(gt1); PIN(gt1);
SB(); M32A(X[0], AF(2), xb[0][22]); SB();
afl[1] = TR(lbA, 51200);
{ float v = T[31] * gt1; PIN(v); T[31] = v; }
{ unsigned u = pk_bf16(T[4], T[5]); PIN(u); hh[2] = u; }
{ float v = T[6] * Y[0][10]; PIN(v); T[6] = v; }
{ float v = T[7] * Y[0][11]; PIN(v); T[7] = v; }
SB(); M32A(X[1], AF(2), xb[1][22]); SB();
afh[1] = TR(lbA, 51712);
{ unsigned u = pk_bf16(T[6], T[7]); PIN(u); hh[3] = u; }
{ float v = T[8] * Y[0][4]; PIN(v); T[8] = v; }
{ float v = T[9] * Y[0][5]; PIN(v); T[9] = v; }
{ float v = T[10] * Y[0][6]; PIN(v); T[10] = v; }
SB(); M32A(X[0], AF(3), xb[0][23]); SB();
afl[2] = TR(lbA, 53248);
GLDS(lvoffB, lbase, 1024);
*(AS3 u32x4*)(uintptr_t)(hw_ad + 16384) = hh;
{ float v = T[11] * Y[0][7]; PIN(v); T[11] = v; }
SB(); M32A(X[1], AF(3), xb[1][23]); SB();
afh[2] = TR(lbA, 53760);
lbase += lstep;
{ float v = T[12] * Y[0][12]; PIN(v); T[12] = v; }
SB();
asm volatile("s_waitcnt vmcnt(25)\n\ts_barrier" ::: "memory");
SB(); M32A(X[0], AF(0), xb[0][24]); SB();
afl[3] = TR(lbA, 55296);
ST41(hxvo, hh, hxw + 16384);
{ float v = T[13] * Y[0][13]; PIN(v); T[13] = v; }
SB(); M32A(X[1], AF(0), xb[1][24]); SB();
afh[3] = TR(lbA, 55808);
{ unsigned u = pk_bf16(T[8], T[9]); PIN(u); hh[0] = u; }
{ unsigned u = pk_bf16(T[10], T[11]); PIN(u); hh[1] = u; }
{ unsigned u = pk_bf16(T[12], T[13]); PIN(u); hh[2] = u; }
{ float v = T[14] * Y[0][14]; PIN(v); T[14] = v; }
SB(); M32A(X[0], AF(1), xb[0][25]); SB();
afl[0] = TR(lbA, 57344);
SETM0(wv2k + 40960);
{ float v = T[15] * Y[0][15]; PIN(v); T[15] = v; }
{ float v = T[16] * Y[1][0]; PIN(v); T[16] = v; }
{ float v = T[17] * Y[1][1]; PIN(v); T[17] = v; }
SB(); M32A(X[1], AF(1), xb[1][25]); SB();
afh[0] = TR(lbA, 57856);
GLDS(lvoffA, lbase, 0);
{ unsigned u = pk_bf16(T[14], T[15]); PIN(u); hh[3] = u; }
{ float v = T[18] * Y[1][2]; PIN(v); T[18] = v; }
SB(); M32A(X[0], AF(2), xb[0][26]); SB();
afl[1] = TR(lbA, 59392);
*(AS3 u32x4*)(uintptr_t)(hw_ad + 16896) = hh;
{ float v = T[19] * Y[1][3]; PIN(v); T[19] = v; }
{ float v = T[20] * Y[1][8]; PIN(v); T[20] = v; }
SB(); M32A(X[1], AF(2), xb[1][26]); SB();
afh[1] = TR(lbA, 59904);
ST41(hxvo, hh, hxw + 16896);
{ float v = T[21] * Y[1][9]; PIN(v); T[21] = v; }
SB(); M32A(X[0], AF(3), xb[0][27]); SB();
afl[2] = TR(lbA, 61440);
GLDS(lvoffB, lbase, 1024);
{ unsigned u = pk_bf16(T[16], T[17]); PIN(u); hh[0] = u; }
{ unsigned u = pk_bf16(T[18], T[19]); PIN(u); hh[1] = u; }
{ unsigned u = pk_bf16(T[20], T[21]); PIN(u); hh[2] = u; }
SB(); M32A(X[1], AF(3), xb[1][27]); SB();
afh[2] = TR(lbA, 61952);
lbase += lstep;
{ float v = T[22] * Y[1][10]; PIN(v); T[22] = v; }
SB();
asm volatile("s_waitcnt vmcnt(26)\n\ts_barrier" ::: "memory");
SB(); M32A(X[0], AF(0), xb[0][28]); SB();
afl[3] = TR(lbA, 63488);
{ float v = T[23] * Y[1][11]; PIN(v); T[23] = v; }
{ float v = T[24] * Y[1][4]; PIN(v); T[24] = v; }
{ float v = T[25] * Y[1][5]; PIN(v); T[25] = v; }
{ float v = T[26] * Y[1][6]; PIN(v); T[26] = v; }
SB(); M32A(X[1], AF(0), xb[1][28]); SB();
afh[3] = TR(lbA, 64000);
{ unsigned u = pk_bf16(T[22], T[23]); PIN(u); hh[3] = u; }
{ float v = T[27] * Y[1][7]; PIN(v); T[27] = v; }
{ float v = T[28] * Y[1][12]; PIN(v); T[28] = v; }
{ float v = T[29] * Y[1][13]; PIN(v); T[29] = v; }
SB(); M32A(X[0], AF(1), xb[0][29]); SB();
afl[0] = TR(lbA, 65536);
SETM0(wv2k + 49152);
*(AS3 u32x4*)(uintptr_t)(hw_ad + 18432) = hh;
{ float v = T[30] * Y[1][14]; PIN(v); T[30] = v; }
SB(); M32A(X[1], AF(1), xb[1][29]); SB();
afh[0] = TR(lbA, 66048);
GLDS(lvoffA, lbase, 0);
{ float v = T[31] * Y[1][15]; PIN(v); T[31] = v; }
SB(); M32V(X[0], AF(2), xb[0][30]); SB();
afl[1] = TR(lbA, 67584);
ST41(hxvo, hh, hxw + 18432);
SB(); M32V(X[1], AF(2), xb[1][30]); SB();
afh[1] = TR(lbA, 68096);
{ unsigned u = pk_bf16(T[24], T[25]); PIN(u); hh[0] = u; }
{ unsigned u = pk_bf16(T[26], T[27]); PIN(u); hh[1] = u; }
{ unsigned u = pk_bf16(T[28], T[29]); PIN(u); hh[2] = u; }
{ unsigned u = pk_bf16(T[30], T[31]); PIN(u); hh[3] = u; }
SB(); M32V(X[0], AF(3), xb[0][31]); SB();
afl[2] = TR(lbA, 69632);
GLDS(lvoffB, lbase, 1024);
*(AS3 u32x4*)(uintptr_t)(hw_ad + 18944) = hh;
SB(); M32V(X[1], AF(3), xb[1][31]); SB();
afh[2] = TR(lbA, 70144);
lbase += lstep;
SB();
asm volatile("s_waitcnt vmcnt(22)\n\ts_barrier" ::: "memory");
SB(); M32V(X[0], AF(0), xb[0][32]); SB();
afl[3] = TR(lbA, 71680);
ST41(hxvo, hh, hxw + 18944);
SB(); M32V(X[1], AF(0), xb[1][32]); SB();
afh[3] = TR(lbA, 72192);
SB(); M32V(X[0], AF(1), xb[0][33]); SB();
afl[0] = TR(lbA, 0);
SETM0(wv2k + 57344);
SB(); M32V(X[1], AF(1), xb[1][33]); SB();
afh[0] = TR(lbA, 512);
GLDS(lvoffA, lbase, 0);
SB(); M32V(X[0], AF(2), xb[0][34]); SB();
afl[1] = TR(lbA, 2048);
SB(); M32V(X[1], AF(2), xb[1][34]); SB();
afh[1] = TR(lbA, 2560);
SB(); M32V(X[0], AF(3), xb[0][35]); SB();
afl[2] = TR(lbA, 4096);
GLDS(lvoffB, lbase, 1024);
SB(); M32V(X[1], AF(3), xb[1][35]); SB();
afh[2] = TR(lbA, 4608);
lbase += lstep;
}
STAMP(4); JIT(4);
TOUCH(trow * 1152u, wdb + 512);
TOUCH(trow * 1152u, wdb + 640);
TOUCH(trow * 1152u, wdb + 768);
TOUCH(trow * 1152u, wdb + 896);
TOUCH(trow * 1152u, wdb + 1024);
{ const unsigned wl_ = (unsigned)(m * 4 + wave) * 10368u + (unsigned)ln1 * 128u; TOUCH(wl_, a.wo); const unsigned w2_ = (unsigned)(m * 4 + wave) * 10368u + 8192u + (unsigned)(ln1 < 16 ? ln1 : 16) * 128u; TOUCH(w2_, a.wo); }
SB(); asm volatile("s_waitcnt vmcnt(23)\n\ts_barrier" ::: "memory");
  {
    const unsigned ab4 = ATT_OFF + th * 5 * 1152 + (2 + atok) * 8;
    const unsigned ab1 = ATT_OFF + 4 * 1152 + (2 + atok) * 8 + th * 4;
    unsigned aq[9], ak0[9], ak1[9], ap[4], au;
    float lg[8], ao[18], am, as_;
    const unsigned ovo = (unsigned)((atok >> 5) * 36864 + (atok & 31) * 16);
    const char* odst = (a.ws + WS_OF) + (size_t)m * 4 * 36864;
    auto ochunk = [&](int gc) { return (gc >> 2) * 1024 + ((gc >> 1) & 1) * 512 + (gc & 1) * 8; };
    #define osoff(c) ochunk(9 * s + th * 5 + (c))
    const int osoff1 = ochunk(9 * s + 4) + th * 4;
    #define AROW(dst, arr, w) { _Pragma("unroll") for (int cc = 0; cc < 4; ++cc) { u32x2 t_ = *(AS3 u32x2*)(uintptr_t)(ab4 + (arr) * 10368 + (14 - 2 * (w)) * 8 + cc * 1152); dst[2 * cc] = t_[0]; dst[2 * cc + 1] = t_[1]; } \
                                dst[8] = *(AS3 unsigned*)(uintptr_t)(ab1 + (arr) * 10368 + (14 - 2 * (w)) * 8); }
    unsigned bk0[9], bk1[9];
    #define QK(dst, w) { float acc_ = 0.f; _Pragma("unroll") for (int d = 0; d < 9; ++d) acc_ = dot2bf(aq[d], dst[d], acc_); lg[w] = acc_; }
    #define PV(r0, r1, p) { _Pragma("unroll") for (int d = 0; d < 9; ++d) { \
        ao[2 * d] = dot2bf(__builtin_amdgcn_perm(r1[d], r0[d], 0x05040100u), ap[p], (p) == 0 ? 0.f : ao[2 * d]); \
        ao[2 * d + 1] = dot2bf(__builtin_amdgcn_perm(r1[d], r0[d], 0x07060302u), ap[p], (p) == 0 ? 0.f : ao[2 * d + 1]); } }
    AROW(aq, 0, 0); AROW(ak0, 1, 0); AROW(ak1, 1, 1); AROW(bk0, 1, 2); AROW(bk1, 1, 3); SB();
    QK(ak0, 0); QK(ak1, 1); SB();
    AROW(ak0, 1, 4); AROW(ak1, 1, 5); SB();
    QK(bk0, 2); QK(bk1, 3); SB();
    AROW(bk0, 1, 6); AROW(bk1, 1, 7); SB();
    QK(ak0, 4); QK(ak1, 5); SB();
    AROW(ak0, 2, 0); AROW(ak1, 2, 1); SB();
    QK(bk0, 6); QK(bk1, 7); SB();
    AROW(bk0, 2, 2); AROW(bk1, 2, 3); SB();
    #pragma unroll
    for (int p = 0; p < 4; ++p) {
      unsigned u = pk_bf16(lg[2 * p], lg[2 * p + 1]);
      u = pk_bf16(bflo(u) * ASC, bfhi(u) * ASC);
      lg[2 * p] = (atg >= 4 * p) ? bflo(u) : -1e9f;
      lg[2 * p + 1] = (atg >= 4 * p + 2) ? bfhi(u) : -1e9f;
    }
    am = fmaxf(fmaxf(fmaxf(lg[0], lg[1]), fmaxf(lg[2], lg[3])), fmaxf(fmaxf(lg[4], lg[5]), fmaxf(lg[6], lg[7]))) * -1.4426950408889634f;
    as_ = 0.f;
    #pragma unroll
    for (int w = 0; w < 8; ++w) { lg[w] = __builtin_amdgcn_exp2f(__builtin_fmaf(lg[w], 1.4426950408889634f, am)); as_ += lg[w]; }
    as_ = __builtin_amdgcn_rcpf(as_);
    #pragma unroll
    for (int p = 0; p < 4; ++p) ap[p] = pk_bf16(lg[2 * p] * as_, lg[2 * p + 1] * as_);
    SB();
    PV(ak0, ak1, 0); SB();
    AROW(ak0, 2, 4); AROW(ak1, 2, 5); SB();
    PV(bk0, bk1, 1); SB();
    AROW(bk0, 2, 6); AROW(bk1, 2, 7); SB();
    PV(ak0, ak1, 2); SB();
    PV(bk0, bk1, 3); SB();
    #pragma unroll
    for (int cc = 0; cc < 4; ++cc) {
      u32x2 o2; o2[0] = pk_bf16(ao[4 * cc], ao[4 * cc + 1]); o2[1] = pk_bf16(ao[4 * cc + 2], ao[4 * cc + 3]);
      ST21(ovo, o2, odst + osoff(cc));
    }
    au = pk_bf16(ao[16], ao[17]);
    ST11(ovo, au, odst + osoff1);
  }

SB();
STAMP(5); JIT(5);
{
const char* nbase = wgb + 384; const unsigned nstride = 9216u;
SB();
asm volatile("s_waitcnt vmcnt(30)\n\ts_barrier" ::: "memory");
SB(); M32ZA(Y[0], AF(0), xb[0][0]); SB();
afl[3] = TR(lbA, 6144);
T[0] = X[0][0];
T[1] = X[0][1];
T[2] = X[0][2];
T[3] = X[0][3];
T[4] = X[0][8];
T[5] = X[0][9];
T[6] = X[0][10];
T[7] = X[0][11];
T[8] = X[0][4];
T[9] = X[0][5];
T[10] = X[0][6];
T[11] = X[0][7];
T[12] = X[0][12];
T[13] = X[0][13];
T[14] = X[0][14];
T[15] = X[0][15];
T[16] = X[1][0];
T[17] = X[1][1];
T[18] = X[1][2];
T[19] = X[1][3];
T[20] = X[1][8];
T[21] = X[1][9];
T[22] = X[1][10];
T[23] = X[1][11];
T[24] = X[1][4];
T[25] = X[1][5];
T[26] = X[1][6];
T[27] = X[1][7];
T[28] = X[1][12];
T[29] = X[1][13];
T[30] = X[1][14];
T[31] = X[1][15];
SB(); M32ZA(Y[1], AF(0), xb[1][0]); SB();
afh[3] = TR(lbA, 6656);
SB(); M32A(Y[0], AF(1), xb[0][1]); SB();
afl[0] = TR(lbA, 8192);
SETM0(wv2k + 65536);
SB(); M32A(Y[1], AF(1), xb[1][1]); SB();
afh[0] = TR(lbA, 8704);
GLDS(lvoffA, lbase, 0);
SB(); M32A(Y[0], AF(2), xb[0][2]); SB();
afl[1] = TR(lbA, 10240);
SB(); M32A(Y[1], AF(2), xb[1][2]); SB();
afh[1] = TR(lbA, 10752);
SB(); M32A(Y[0], AF(3), xb[0][3]); SB();
afl[2] = TR(lbA, 12288);
GLDS(lvoffB, lbase, 1024);
gt0 = T[0] * T[0]; PIN(gt0);
SB(); M32A(Y[1], AF(3), xb[1][3]); SB();
afh[2] = TR(lbA, 12800);
lbase = nbase; lstep = 64 * nstride; lvoffA = rowA * nstride + c16P1; lvoffB = (rowA + 8) * nstride + c16P1 - 1024;
SB();
asm volatile("s_waitcnt vmcnt(28)\n\ts_barrier" ::: "memory");
SB(); M32A(Y[0], AF(0), xb[0][4]); SB();
afl[3] = TR(lbA, 14336);
gt0 = __builtin_fmaf(gt0, C1, C0); PIN(gt0);
SB(); M32A(Y[1], AF(0), xb[1][4]); SB();
afh[3] = TR(lbA, 14848);
gt0 = T[0] * gt0; PIN(gt0);
gt1 = T[1] * T[1]; PIN(gt1);
SB(); M32A(Y[0], AF(1), xb[0][5]); SB();
afl[0] = TR(lbA, 16384);
SETM0(wv2k + 0);
gt0 = __builtin_amdgcn_exp2f(gt0); PIN(gt0);
gt1 = __builtin_fmaf(gt1, C1, C0); PIN(gt1);
SB(); M32A(Y[1], AF(1), xb[1][5]); SB();
afh[0] = TR(lbA, 16896);
GLDS(lvoffA, lbase, 0);
gt0 = gt0 + 1.0f; PIN(gt0);
gt1 = T[1] * gt1; PIN(gt1);
SB(); M32A(Y[0], AF(2), xb[0][6]); SB();
afl[1] = TR(lbA, 18432);
gt0 = __builtin_amdgcn_rcpf(gt0); PIN(gt0);
gt1 = __builtin_amdgcn_exp2f(gt1); PIN(gt1);
SB(); M32A(Y[1], AF(2), xb[1][6]); SB();
afh[1] = TR(lbA, 18944);
{ float v = T[0] * gt0; PIN(v); T[0] = v; }
gt1 = gt1 + 1.0f; PIN(gt1);
gt2 = T[2] * T[2]; PIN(gt2);
SB(); M32A(Y[0], AF(3), xb[0][7]); SB();
afl[2] = TR(lbA, 20480);
GLDS(lvoffB, lbase, 1024);
gt1 = __builtin_amdgcn_rcpf(gt1); PIN(gt1);
gt2 = __builtin_fmaf(gt2, C1, C0); PIN(gt2);
SB(); M32A(Y[1], AF(3), xb[1][7]); SB();
afh[2] = TR(lbA, 20992);
lbase += lstep;
SB();
asm volatile("s_waitcnt vmcnt(28)\n\ts_barrier" ::: "memory");
SB(); M32A(Y[0], AF(0), xb[0][8]); SB();
afl[3] = TR(lbA, 22528);
{ float v = T[1] * gt1; PIN(v); T[1] = v; }
gt2 = T[2] * gt2; PIN(gt2);
gt0 = T[3] * T[3]; PIN(gt0);
SB(); M32A(Y[1], AF(0), xb[1][8]); SB();
afh[3] = TR(lbA, 23040);
gt2 = __builtin_amdgcn_exp2f(gt2); PIN(gt2);
gt0 = __builtin_fmaf(gt0, C1, C0); PIN(gt0);
SB(); M32A(Y[0], AF(1), xb[0][9]); SB();
afl[0] = TR(lbA, 24576);
SETM0(wv2k + 8192);
gt2 = gt2 + 1.0f; PIN(gt2);
gt0 = T[3] * gt0; PIN(gt0);
gt1 = T[4] * T[4]; PIN(gt1);
SB(); M32A(Y[1], AF(1), xb[1][9]); SB();
afh[0] = TR(lbA, 25088);
GLDS(lvoffA, lbase, 0);
gt2 = __builtin_amdgcn_rcpf(gt2); PIN(gt2);
SB(); M32A(Y[0], AF(2), xb[0][10]); SB();
afl[1] = TR(lbA, 26624);
{ float v = T[2] * gt2; PIN(v); T[2] = v; }
gt0 = __builtin_amdgcn_exp2f(gt0); PIN(gt0);
gt1 = __builtin_fmaf(gt1, C1, C0); PIN(gt1);
SB(); M32A(Y[1], AF(2), xb[1][10]); SB();
afh[1] = TR(lbA, 27136);
gt0 = gt0 + 1.0f; PIN(gt0);
gt1 = T[4] * gt1; PIN(gt1);
gt2 = T[5] * T[5]; PIN(gt2);
SB(); M32A(Y[0], AF(3), xb[0][11]); SB();
afl[2] = TR(lbA, 28672);
GLDS(lvoffB, lbase, 1024);
gt0 = __builtin_amdgcn_rcpf(gt0); PIN(gt0);
gt2 = __builtin_fmaf(gt2, C1, C0); PIN(gt2);
SB(); M32A(Y[1], AF(3), xb[1][11]); SB();
afh[2] = TR(lbA, 29184);
lbase += lstep;
SB();
asm volatile("s_waitcnt vmcnt(6)\n\ts_barrier" ::: "memory");
SIGNAL_SET(fHme, 2, true);
SIGNAL_ADD(fO, 1, true);
SB(); M32A(Y[0], AF(0), xb[0][12]); SB();
afl[3] = TR(lbA, 30720);
{ float v = T[3] * gt0; PIN(v); T[3] = v; }
gt1 = __builtin_amdgcn_exp2f(gt1); PIN(gt1);
gt2 = T[5] * gt2; PIN(gt2);
SB(); M32A(Y[1], AF(0), xb[1][12]); SB();
afh[3] = TR(lbA, 31232);
gt1 = gt1 + 1.0f; PIN(gt1);
gt2 = __builtin_amdgcn_exp2f(gt2); PIN(gt2);
SB(); M32A(Y[0], AF(1), xb[0][13]); SB();
afl[0] = TR(lbA, 32768);
SETM0(wv2k + 16384);
gt1 = __builtin_amdgcn_rcpf(gt1); PIN(gt1);
gt2 = gt2 + 1.0f; PIN(gt2);
SB(); M32A(Y[1], AF(1), xb[1][13]); SB();
afh[0] = TR(lbA, 33280);
GLDS(lvoffA, lbase, 0);
{ float v = T[4] * gt1; PIN(v); T[4] = v; }
gt0 = T[6] * T[6]; PIN(gt0);
SB(); M32A(Y[0], AF(2), xb[0][14]); SB();
afl[1] = TR(lbA, 34816);
gt2 = __builtin_amdgcn_rcpf(gt2); PIN(gt2);
gt0 = __builtin_fmaf(gt0, C1, C0); PIN(gt0);
gt1 = T[7] * T[7]; PIN(gt1);
SB(); M32A(Y[1], AF(2), xb[1][14]); SB();
afh[1] = TR(lbA, 35328);
{ float v = T[5] * gt2; PIN(v); T[5] = v; }
gt0 = T[6] * gt0; PIN(gt0);
gt1 = __builtin_fmaf(gt1, C1, C0); PIN(gt1);
SB(); M32A(Y[0], AF(3), xb[0][15]); SB();
afl[2] = TR(lbA, 36864);
GLDS(lvoffB, lbase, 1024);
gt0 = __builtin_amdgcn_exp2f(gt0); PIN(gt0);
gt1 = T[7] * gt1; PIN(gt1);
SB(); M32A(Y[1], AF(3), xb[1][15]); SB();
afh[2] = TR(lbA, 37376);
lbase += lstep;
SB();
asm volatile("s_waitcnt vmcnt(26)\n\ts_barrier" ::: "memory");
SB(); M32A(Y[0], AF(0), xb[0][16]); SB();
afl[3] = TR(lbA, 38912);
gt0 = gt0 + 1.0f; PIN(gt0);
gt1 = __builtin_amdgcn_exp2f(gt1); PIN(gt1);
gt2 = T[8] * T[8]; PIN(gt2);
SB(); M32A(Y[1], AF(0), xb[1][16]); SB();
afh[3] = TR(lbA, 39424);
gt0 = __builtin_amdgcn_rcpf(gt0); PIN(gt0);
gt1 = gt1 + 1.0f; PIN(gt1);
SB(); M32A(Y[0], AF(1), xb[0][17]); SB();
afl[0] = TR(lbA, 40960);
SETM0(wv2k + 24576);
{ float v = T[6] * gt0; PIN(v); T[6] = v; }
gt1 = __builtin_amdgcn_rcpf(gt1); PIN(gt1);
SB(); M32A(Y[1], AF(1), xb[1][17]); SB();
afh[0] = TR(lbA, 41472);
GLDS(lvoffA, lbase, 0);
{ float v = T[7] * gt1; PIN(v); T[7] = v; }
gt2 = __builtin_fmaf(gt2, C1, C0); PIN(gt2);
SB(); M32A(Y[0], AF(2), xb[0][18]); SB();
afl[1] = TR(lbA, 43008);
gt2 = T[8] * gt2; PIN(gt2);
gt0 = T[9] * T[9]; PIN(gt0);
gt1 = T[10] * T[10]; PIN(gt1);
SB(); M32A(Y[1], AF(2), xb[1][18]); SB();
afh[1] = TR(lbA, 43520);
gt2 = __builtin_amdgcn_exp2f(gt2); PIN(gt2);
gt0 = __builtin_fmaf(gt0, C1, C0); PIN(gt0);
SB(); M32A(Y[0], AF(3), xb[0][19]); SB();
afl[2] = TR(lbA, 45056);
GLDS(lvoffB, lbase, 1024);
gt2 = gt2 + 1.0f; PIN(gt2);
gt0 = T[9] * gt0; PIN(gt0);
gt1 = __builtin_fmaf(gt1, C1, C0); PIN(gt1);
SB(); M32A(Y[1], AF(3), xb[1][19]); SB();
afh[2] = TR(lbA, 45568);
lbase += lstep;
SB();
asm volatile("s_waitcnt vmcnt(25)\n\ts_barrier" ::: "memory");
SB(); M32A(Y[0], AF(0), xb[0][20]); SB();
afl[3] = TR(lbA, 47104);
gt2 = __builtin_amdgcn_rcpf(gt2); PIN(gt2);
gt0 = __builtin_amdgcn_exp2f(gt0); PIN(gt0);
SB(); M32A(Y[1], AF(0), xb[1][20]); SB();
afh[3] = TR(lbA, 47616);
{ float v = T[8] * gt2; PIN(v); T[8] = v; }
gt0 = gt0 + 1.0f; PIN(gt0);
gt1 = T[10] * gt1; PIN(gt1);
SB(); M32A(Y[0], AF(1), xb[0][21]); SB();
afl[0] = TR(lbA, 49152);
SETM0(wv2k + 32768);
gt0 = __builtin_amdgcn_rcpf(gt0); PIN(gt0);
gt2 = T[11] * T[11]; PIN(gt2);
SB(); M32A(Y[1], AF(1), xb[1][21]); SB();
afh[0] = TR(lbA, 49664);
GLDS(lvoffA, lbase, 0);
{ float v = T[9] * gt0; PIN(v); T[9] = v; }
gt2 = __builtin_fmaf(gt2, C1, C0); PIN(gt2);
SB(); M32A(Y[0], AF(2), xb[0][22]); SB();
afl[1] = TR(lbA, 51200);
gt1 = __builtin_amdgcn_exp2f(gt1); PIN(gt1);
gt2 = T[11] * gt2; PIN(gt2);
gt0 = T[12] * T[12]; PIN(gt0);
SB(); M32A(Y[1], AF(2), xb[1][22]); SB();
afh[1] = TR(lbA, 51712);
gt1 = gt1 + 1.0f; PIN(gt1);
gt2 = __builtin_amdgcn_exp2f(gt2); PIN(gt2);
SB(); M32A(Y[0], AF(3), xb[0][23]); SB();
afl[2] = TR(lbA, 53248);
GLDS(lvoffB, lbase, 1024);
gt1 = __builtin_amdgcn_rcpf(gt1); PIN(gt1);
gt2 = gt2 + 1.0f; PIN(gt2);
SB(); M32A(Y[1], AF(3), xb[1][23]); SB();
afh[2] = TR(lbA, 53760);
lbase += lstep;
SB();
asm volatile("s_waitcnt vmcnt(24)\n\ts_barrier" ::: "memory");
SB(); M32A(Y[0], AF(0), xb[0][24]); SB();
afl[3] = TR(lbA, 55296);
{ float v = T[10] * gt1; PIN(v); T[10] = v; }
gt2 = __builtin_amdgcn_rcpf(gt2); PIN(gt2);
gt0 = __builtin_fmaf(gt0, C1, C0); PIN(gt0);
SB(); M32A(Y[1], AF(0), xb[1][24]); SB();
afh[3] = TR(lbA, 55808);
{ float v = T[11] * gt2; PIN(v); T[11] = v; }
gt0 = T[12] * gt0; PIN(gt0);
gt1 = T[13] * T[13]; PIN(gt1);
SB(); M32A(Y[0], AF(1), xb[0][25]); SB();
afl[0] = TR(lbA, 57344);
SETM0(wv2k + 40960);
gt0 = __builtin_amdgcn_exp2f(gt0); PIN(gt0);
gt1 = __builtin_fmaf(gt1, C1, C0); PIN(gt1);
SB(); M32A(Y[1], AF(1), xb[1][25]); SB();
afh[0] = TR(lbA, 57856);
GLDS(lvoffA, lbase, 0);
gt0 = gt0 + 1.0f; PIN(gt0);
gt1 = T[13] * gt1; PIN(gt1);
SB(); M32A(Y[0], AF(2), xb[0][26]); SB();
afl[1] = TR(lbA, 59392);
gt0 = __builtin_amdgcn_rcpf(gt0); PIN(gt0);
gt1 = __builtin_amdgcn_exp2f(gt1); PIN(gt1);
SB(); M32A(Y[1], AF(2), xb[1][26]); SB();
afh[1] = TR(lbA, 59904);
{ float v = T[12] * gt0; PIN(v); T[12] = v; }
gt1 = gt1 + 1.0f; PIN(gt1);
gt2 = T[14] * T[14]; PIN(gt2);
SB(); M32A(Y[0], AF(3), xb[0][27]); SB();
afl[2] = TR(lbA, 61440);
GLDS(lvoffB, lbase, 1024);
gt1 = __builtin_amdgcn_rcpf(gt1); PIN(gt1);
gt2 = __builtin_fmaf(gt2, C1, C0); PIN(gt2);
SB(); M32A(Y[1], AF(3), xb[1][27]); SB();
afh[2] = TR(lbA, 61952);
lbase += lstep;
SB();
asm volatile("s_waitcnt vmcnt(12)\n\ts_barrier" ::: "memory");
SB(); M32A(Y[0], AF(0), xb[0][28]); SB();
afl[3] = TR(lbA, 63488);
{ float v = T[13] * gt1; PIN(v); T[13] = v; }
gt2 = T[14] * gt2; PIN(gt2);
gt0 = T[15] * T[15]; PIN(gt0);
SB(); M32A(Y[1], AF(0), xb[1][28]); SB();
afh[3] = TR(lbA, 64000);
gt2 = __builtin_amdgcn_exp2f(gt2); PIN(gt2);
gt0 = __builtin_fmaf(gt0, C1, C0); PIN(gt0);
SB(); M32A(Y[0], AF(1), xb[0][29]); SB();
afl[0] = TR(lbA, 65536);
SETM0(wv2k + 49152);
gt2 = gt2 + 1.0f; PIN(gt2);
gt0 = T[15] * gt0; PIN(gt0);
gt1 = T[16] * T[16]; PIN(gt1);
SB(); M32A(Y[1], AF(1), xb[1][29]); SB();
afh[0] = TR(lbA, 66048);
GLDS(lvoffA, lbase, 0);
gt2 = __builtin_amdgcn_rcpf(gt2); PIN(gt2);
SB(); M32V(Y[0], AF(2), xb[0][30]); SB();
afl[1] = TR(lbA, 67584);
{ float v = T[14] * gt2; PIN(v); T[14] = v; }
gt0 = __builtin_amdgcn_exp2f(gt0); PIN(gt0);
gt1 = __builtin_fmaf(gt1, C1, C0); PIN(gt1);
SB(); M32V(Y[1], AF(2), xb[1][30]); SB();
afh[1] = TR(lbA, 68096);
gt0 = gt0 + 1.0f; PIN(gt0);
gt1 = T[16] * gt1; PIN(gt1);
gt2 = T[17] * T[17]; PIN(gt2);
SB(); M32V(Y[0], AF(3), xb[0][31]); SB();
afl[2] = TR(lbA, 69632);
GLDS(lvoffB, lbase, 1024);
gt0 = __builtin_amdgcn_rcpf(gt0); PIN(gt0);
gt2 = __builtin_fmaf(gt2, C1, C0); PIN(gt2);
SB(); M32V(Y[1], AF(3), xb[1][31]); SB();
afh[2] = TR(lbA, 70144);
lbase += lstep;
SB();
asm volatile("s_waitcnt vmcnt(12)\n\ts_barrier" ::: "memory");
SB(); M32V(Y[0], AF(0), xb[0][32]); SB();
afl[3] = TR(lbA, 71680);
{ float v = T[15] * gt0; PIN(v); T[15] = v; }
gt1 = __builtin_amdgcn_exp2f(gt1); PIN(gt1);
gt2 = T[17] * gt2; PIN(gt2);
SB(); M32V(Y[1], AF(0), xb[1][32]); SB();
afh[3] = TR(lbA, 72192);
gt1 = gt1 + 1.0f; PIN(gt1);
gt2 = __builtin_amdgcn_exp2f(gt2); PIN(gt2);
SB(); M32V(Y[0], AF(1), xb[0][33]); SB();
afl[0] = TR(lbA, 0);
SETM0(wv2k + 57344);
gt1 = __builtin_amdgcn_rcpf(gt1); PIN(gt1);
gt2 = gt2 + 1.0f; PIN(gt2);
SB(); M32V(Y[1], AF(1), xb[1][33]); SB();
afh[0] = TR(lbA, 512);
GLDS(lvoffA, lbase, 0);
{ float v = T[16] * gt1; PIN(v); T[16] = v; }
gt0 = T[18] * T[18]; PIN(gt0);
SB(); M32V(Y[0], AF(2), xb[0][34]); SB();
afl[1] = TR(lbA, 2048);
gt2 = __builtin_amdgcn_rcpf(gt2); PIN(gt2);
gt0 = __builtin_fmaf(gt0, C1, C0); PIN(gt0);
gt1 = T[19] * T[19]; PIN(gt1);
SB(); M32V(Y[1], AF(2), xb[1][34]); SB();
afh[1] = TR(lbA, 2560);
{ float v = T[17] * gt2; PIN(v); T[17] = v; }
gt0 = T[18] * gt0; PIN(gt0);
gt1 = __builtin_fmaf(gt1, C1, C0); PIN(gt1);
SB(); M32V(Y[0], AF(3), xb[0][35]); SB();
afl[2] = TR(lbA, 4096);
GLDS(lvoffB, lbase, 1024);
gt0 = __builtin_amdgcn_exp2f(gt0); PIN(gt0);
gt1 = T[19] * gt1; PIN(gt1);
SB(); M32V(Y[1], AF(3), xb[1][35]); SB();
afh[2] = TR(lbA, 4608);
lbase += lstep;
}
{
const char* nbase = wub + 384; const unsigned nstride = 9216u;
SB();
asm volatile("s_waitcnt vmcnt(12)\n\ts_barrier" ::: "memory");
SB(); M32ZA(X[0], AF(0), xb[0][0]); SB();
afl[3] = TR(lbA, 6144);
gt0 = gt0 + 1.0f; PIN(gt0);
gt1 = __builtin_amdgcn_exp2f(gt1); PIN(gt1);
gt2 = T[20] * T[20]; PIN(gt2);
SB(); M32ZA(X[1], AF(0), xb[1][0]); SB();
afh[3] = TR(lbA, 6656);
gt0 = __builtin_amdgcn_rcpf(gt0); PIN(gt0);
gt1 = gt1 + 1.0f; PIN(gt1);
SB(); M32A(X[0], AF(1), xb[0][1]); SB();
afl[0] = TR(lbA, 8192);
SETM0(wv2k + 65536);
{ float v = T[18] * gt0; PIN(v); T[18] = v; }
gt1 = __builtin_amdgcn_rcpf(gt1); PIN(gt1);
SB(); M32A(X[1], AF(1), xb[1][1]); SB();
afh[0] = TR(lbA, 8704);
GLDS(lvoffA, lbase, 0);
{ float v = T[19] * gt1; PIN(v); T[19] = v; }
gt2 = __builtin_fmaf(gt2, C1, C0); PIN(gt2);
SB(); M32A(X[0], AF(2), xb[0][2]); SB();
afl[1] = TR(lbA, 10240);
gt2 = T[20] * gt2; PIN(gt2);
gt0 = T[21] * T[21]; PIN(gt0);
gt1 = T[22] * T[22]; PIN(gt1);
SB(); M32A(X[1], AF(2), xb[1][2]); SB();
afh[1] = TR(lbA, 10752);
gt2 = __builtin_amdgcn_exp2f(gt2); PIN(gt2);
gt0 = __builtin_fmaf(gt0, C1, C0); PIN(gt0);
SB(); M32A(X[0], AF(3), xb[0][3]); SB();
afl[2] = TR(lbA, 12288);
GLDS(lvoffB, lbase, 1024);
gt2 = gt2 + 1.0f; PIN(gt2);
gt0 = T[21] * gt0; PIN(gt0);
gt1 = __builtin_fmaf(gt1, C1, C0); PIN(gt1);
SB(); M32A(X[1], AF(3), xb[1][3]); SB();
afh[2] = TR(lbA, 12800);
lbase = nbase; lstep = 64 * nstride; lvoffA = rowA * nstride + c16P1; lvoffB = (rowA + 8) * nstride + c16P1 - 1024;
SB();
asm volatile("s_waitcnt vmcnt(12)\n\ts_barrier" ::: "memory");
SB(); M32A(X[0], AF(0), xb[0][4]); SB();
afl[3] = TR(lbA, 14336);
gt2 = __builtin_amdgcn_rcpf(gt2); PIN(gt2);
gt0 = __builtin_amdgcn_exp2f(gt0); PIN(gt0);
SB(); M32A(X[1], AF(0), xb[1][4]); SB();
afh[3] = TR(lbA, 14848);
{ float v = T[20] * gt2; PIN(v); T[20] = v; }
gt0 = gt0 + 1.0f; PIN(gt0);
gt1 = T[22] * gt1; PIN(gt1);
SB(); M32A(X[0], AF(1), xb[0][5]); SB();
afl[0] = TR(lbA, 16384);
SETM0(wv2k + 0);
gt0 = __builtin_amdgcn_rcpf(gt0); PIN(gt0);
gt2 = T[23] * T[23]; PIN(gt2);
SB(); M32A(X[1], AF(1), xb[1][5]); SB();
afh[0] = TR(lbA, 16896);
GLDS(lvoffA, lbase, 0);
{ float v = T[21] * gt0; PIN(v); T[21] = v; }
gt2 = __builtin_fmaf(gt2, C1, C0); PIN(gt2);
SB(); M32A(X[0], AF(2), xb[0][6]); SB();
afl[1] = TR(lbA, 18432);
gt1 = __builtin_amdgcn_exp2f(gt1); PIN(gt1);
gt2 = T[23] * gt2; PIN(gt2);
gt0 = T[24] * T[24]; PIN(gt0);
SB(); M32A(X[1], AF(2), xb[1][6]); SB();
afh[1] = TR(lbA, 18944);
gt1 = gt1 + 1.0f; PIN(gt1);
gt2 = __builtin_amdgcn_exp2f(gt2); PIN(gt2);
SB(); M32A(X[0], AF(3), xb[0][7]); SB();
afl[2] = TR(lbA, 20480);
GLDS(lvoffB, lbase, 1024);
gt1 = __builtin_amdgcn_rcpf(gt1); PIN(gt1);
gt2 = gt2 + 1.0f; PIN(gt2);
SB(); M32A(X[1], AF(3), xb[1][7]); SB();
afh[2] = TR(lbA, 20992);
lbase += lstep;
SB();
asm volatile("s_waitcnt vmcnt(12)\n\ts_barrier" ::: "memory");
SB(); M32A(X[0], AF(0), xb[0][8]); SB();
afl[3] = TR(lbA, 22528);
{ float v = T[22] * gt1; PIN(v); T[22] = v; }
gt2 = __builtin_amdgcn_rcpf(gt2); PIN(gt2);
gt0 = __builtin_fmaf(gt0, C1, C0); PIN(gt0);
SB(); M32A(X[1], AF(0), xb[1][8]); SB();
afh[3] = TR(lbA, 23040);
{ float v = T[23] * gt2; PIN(v); T[23] = v; }
gt0 = T[24] * gt0; PIN(gt0);
gt1 = T[25] * T[25]; PIN(gt1);
SB(); M32A(X[0], AF(1), xb[0][9]); SB();
afl[0] = TR(lbA, 24576);
SETM0(wv2k + 8192);
gt0 = __builtin_amdgcn_exp2f(gt0); PIN(gt0);
gt1 = __builtin_fmaf(gt1, C1, C0); PIN(gt1);
SB(); M32A(X[1], AF(1), xb[1][9]); SB();
afh[0] = TR(lbA, 25088);
GLDS(lvoffA, lbase, 0);
gt0 = gt0 + 1.0f; PIN(gt0);
gt1 = T[25] * gt1; PIN(gt1);
SB(); M32A(X[0], AF(2), xb[0][10]); SB();
afl[1] = TR(lbA, 26624);
gt0 = __builtin_amdgcn_rcpf(gt0); PIN(gt0);
gt1 = __builtin_amdgcn_exp2f(gt1); PIN(gt1);
SB(); M32A(X[1], AF(2), xb[1][10]); SB();
afh[1] = TR(lbA, 27136);
{ float v = T[24] * gt0; PIN(v); T[24] = v; }
gt1 = gt1 + 1.0f; PIN(gt1);
gt2 = T[26] * T[26]; PIN(gt2);
SB(); M32A(X[0], AF(3), xb[0][11]); SB();
afl[2] = TR(lbA, 28672);
GLDS(lvoffB, lbase, 1024);
gt1 = __builtin_amdgcn_rcpf(gt1); PIN(gt1);
gt2 = __builtin_fmaf(gt2, C1, C0); PIN(gt2);
SB(); M32A(X[1], AF(3), xb[1][11]); SB();
afh[2] = TR(lbA, 29184);
lbase += lstep;
SB();
asm volatile("s_waitcnt vmcnt(12)\n\ts_barrier" ::: "memory");
SB(); M32A(X[0], AF(0), xb[0][12]); SB();
afl[3] = TR(lbA, 30720);
{ float v = T[25] * gt1; PIN(v); T[25] = v; }
gt2 = T[26] * gt2; PIN(gt2);
gt0 = T[27] * T[27]; PIN(gt0);
{ float v = T[0] * Y[0][0]; PIN(v); T[0] = v; }
SB(); M32A(X[1], AF(0), xb[1][12]); SB();
afh[3] = TR(lbA, 31232);
gt2 = __builtin_amdgcn_exp2f(gt2); PIN(gt2);
gt0 = __builtin_fmaf(gt0, C1, C0); PIN(gt0);
SB(); M32A(X[0], AF(1), xb[0][13]); SB();
afl[0] = TR(lbA, 32768);
SETM0(wv2k + 16384);
gt2 = gt2 + 1.0f; PIN(gt2);
gt0 = T[27] * gt0; PIN(gt0);
gt1 = T[28] * T[28]; PIN(gt1);
SB(); M32A(X[1], AF(1), xb[1][13]); SB();
afh[0] = TR(lbA, 33280);
GLDS(lvoffA, lbase, 0);
gt2 = __builtin_amdgcn_rcpf(gt2); PIN(gt2);
SB(); M32A(X[0], AF(2), xb[0][14]); SB();
afl[1] = TR(lbA, 34816);
{ float v = T[26] * gt2; PIN(v); T[26] = v; }
gt0 = __builtin_amdgcn_exp2f(gt0); PIN(gt0);
gt1 = __builtin_fmaf(gt1, C1, C0); PIN(gt1);
SB(); M32A(X[1], AF(2), xb[1][14]); SB();
afh[1] = TR(lbA, 35328);
gt0 = gt0 + 1.0f; PIN(gt0);
gt1 = T[28] * gt1; PIN(gt1);
gt2 = T[29] * T[29]; PIN(gt2);
SB(); M32A(X[0], AF(3), xb[0][15]); SB();
afl[2] = TR(lbA, 36864);
GLDS(lvoffB, lbase, 1024);
gt0 = __builtin_amdgcn_rcpf(gt0); PIN(gt0);
gt2 = __builtin_fmaf(gt2, C1, C0); PIN(gt2);
SB(); M32A(X[1], AF(3), xb[1][15]); SB();
afh[2] = TR(lbA, 37376);
lbase += lstep;
SB();
asm volatile("s_waitcnt vmcnt(12)\n\ts_barrier" ::: "memory");
SB(); M32A(X[0], AF(0), xb[0][16]); SB();
afl[3] = TR(lbA, 38912);
{ float v = T[27] * gt0; PIN(v); T[27] = v; }
gt1 = __builtin_amdgcn_exp2f(gt1); PIN(gt1);
gt2 = T[29] * gt2; PIN(gt2);
SB(); M32A(X[1], AF(0), xb[1][16]); SB();
afh[3] = TR(lbA, 39424);
gt1 = gt1 + 1.0f; PIN(gt1);
gt2 = __builtin_amdgcn_exp2f(gt2); PIN(gt2);
SB(); M32A(X[0], AF(1), xb[0][17]); SB();
afl[0] = TR(lbA, 40960);
SETM0(wv2k + 24576);
gt1 = __builtin_amdgcn_rcpf(gt1); PIN(gt1);
gt2 = gt2 + 1.0f; PIN(gt2);
SB(); M32A(X[1], AF(1), xb[1][17]); SB();
afh[0] = TR(lbA, 41472);
GLDS(lvoffA, lbase, 0);
{ float v = T[28] * gt1; PIN(v); T[28] = v; }
gt0 = T[30] * T[30]; PIN(gt0);
SB(); M32A(X[0], AF(2), xb[0][18]); SB();
afl[1] = TR(lbA, 43008);
gt2 = __builtin_amdgcn_rcpf(gt2); PIN(gt2);
gt0 = __builtin_fmaf(gt0, C1, C0); PIN(gt0);
gt1 = T[31] * T[31]; PIN(gt1);
SB(); M32A(X[1], AF(2), xb[1][18]); SB();
afh[1] = TR(lbA, 43520);
{ float v = T[29] * gt2; PIN(v); T[29] = v; }
gt0 = T[30] * gt0; PIN(gt0);
gt1 = __builtin_fmaf(gt1, C1, C0); PIN(gt1);
SB(); M32A(X[0], AF(3), xb[0][19]); SB();
afl[2] = TR(lbA, 45056);
GLDS(lvoffB, lbase, 1024);
gt0 = __builtin_amdgcn_exp2f(gt0); PIN(gt0);
gt1 = T[31] * gt1; PIN(gt1);
SB(); M32A(X[1], AF(3), xb[1][19]); SB();
afh[2] = TR(lbA, 45568);
lbase += lstep;
SB();
asm volatile("s_waitcnt vmcnt(12)\n\ts_barrier" ::: "memory");
SB(); M32A(X[0], AF(0), xb[0][20]); SB();
afl[3] = TR(lbA, 47104);
gt0 = gt0 + 1.0f; PIN(gt0);
gt1 = __builtin_amdgcn_exp2f(gt1); PIN(gt1);
{ float v = T[1] * Y[0][1]; PIN(v); T[1] = v; }
SB(); M32A(X[1], AF(0), xb[1][20]); SB();
afh[3] = TR(lbA, 47616);
gt0 = __builtin_amdgcn_rcpf(gt0); PIN(gt0);
gt1 = gt1 + 1.0f; PIN(gt1);
SB(); M32A(X[0], AF(1), xb[0][21]); SB();
afl[0] = TR(lbA, 49152);
SETM0(wv2k + 32768);
LDW1(pw0, zvo, fOw);
{ float v = T[30] * gt0; PIN(v); T[30] = v; }
SB(); M32A(X[1], AF(1), xb[1][21]); SB();
afh[0] = TR(lbA, 49664);
GLDS(lvoffA, lbase, 0);
gt1 = __builtin_amdgcn_rcpf(gt1); PIN(gt1);
SB(); M32A(X[0], AF(2), xb[0][22]); SB();
afl[1] = TR(lbA, 51200);
{ float v = T[31] * gt1; PIN(v); T[31] = v; }
{ unsigned u = pk_bf16(T[0], T[1]); PIN(u); hh[0] = u; }
{ float v = T[2] * Y[0][2]; PIN(v); T[2] = v; }
{ float v = T[3] * Y[0][3]; PIN(v); T[3] = v; }
SB(); M32A(X[1], AF(2), xb[1][22]); SB();
afh[1] = TR(lbA, 51712);
{ unsigned u = pk_bf16(T[2], T[3]); PIN(u); hh[1] = u; }
{ float v = T[4] * Y[0][8]; PIN(v); T[4] = v; }
{ float v = T[5] * Y[0][9]; PIN(v); T[5] = v; }
SB(); M32A(X[0], AF(3), xb[0][23]); SB();
afl[2] = TR(lbA, 53248);
GLDS(lvoffB, lbase, 1024);
{ unsigned u = pk_bf16(T[4], T[5]); PIN(u); hh[2] = u; }
{ float v = T[6] * Y[0][10]; PIN(v); T[6] = v; }
{ float v = T[7] * Y[0][11]; PIN(v); T[7] = v; }
SB(); M32A(X[1], AF(3), xb[1][23]); SB();
afh[2] = TR(lbA, 53760);
lbase += lstep;
SB();
asm volatile("s_waitcnt vmcnt(13)\n\ts_barrier" ::: "memory");
SB(); M32A(X[0], AF(0), xb[0][24]); SB();
afl[3] = TR(lbA, 55296);
{ unsigned u = pk_bf16(T[6], T[7]); PIN(u); hh[3] = u; }
{ float v = T[8] * Y[0][4]; PIN(v); T[8] = v; }
{ float v = T[9] * Y[0][5]; PIN(v); T[9] = v; }
{ float v = T[10] * Y[0][6]; PIN(v); T[10] = v; }
SB(); M32A(X[1], AF(0), xb[1][24]); SB();
afh[3] = TR(lbA, 55808);
*(AS3 u32x4*)(uintptr_t)(hw_ad + 32768) = hh;
{ float v = T[11] * Y[0][7]; PIN(v); T[11] = v; }
SB(); M32A(X[0], AF(1), xb[0][25]); SB();
afl[0] = TR(lbA, 57344);
SETM0(wv2k + 40960);
ST41(hxvo, hh, hxw + 32768);
SB(); M32A(X[1], AF(1), xb[1][25]); SB();
afh[0] = TR(lbA, 57856);
GLDS(lvoffA, lbase, 0);
{ unsigned u = pk_bf16(T[8], T[9]); PIN(u); hh[0] = u; }
{ unsigned u = pk_bf16(T[10], T[11]); PIN(u); hh[1] = u; }
SB(); M32A(X[0], AF(2), xb[0][26]); SB();
afl[1] = TR(lbA, 59392);
{ float v = T[12] * Y[0][12]; PIN(v); T[12] = v; }
{ float v = T[13] * Y[0][13]; PIN(v); T[13] = v; }
{ float v = T[14] * Y[0][14]; PIN(v); T[14] = v; }
{ float v = T[15] * Y[0][15]; PIN(v); T[15] = v; }
SB(); M32A(X[1], AF(2), xb[1][26]); SB();
afh[1] = TR(lbA, 59904);
{ unsigned u = pk_bf16(T[12], T[13]); PIN(u); hh[2] = u; }
{ unsigned u = pk_bf16(T[14], T[15]); PIN(u); hh[3] = u; }
{ float v = T[16] * Y[1][0]; PIN(v); T[16] = v; }
SB(); M32A(X[0], AF(3), xb[0][27]); SB();
afl[2] = TR(lbA, 61440);
GLDS(lvoffB, lbase, 1024);
*(AS3 u32x4*)(uintptr_t)(hw_ad + 33280) = hh;
{ float v = T[17] * Y[1][1]; PIN(v); T[17] = v; }
SB(); M32A(X[1], AF(3), xb[1][27]); SB();
afh[2] = TR(lbA, 61952);
lbase += lstep;
SB();
asm volatile("s_waitcnt vmcnt(14)\n\ts_barrier" ::: "memory");
SB(); M32A(X[0], AF(0), xb[0][28]); SB();
afl[3] = TR(lbA, 63488);
ST41(hxvo, hh, hxw + 33280);
{ float v = T[18] * Y[1][2]; PIN(v); T[18] = v; }
SB(); M32A(X[1], AF(0), xb[1][28]); SB();
afh[3] = TR(lbA, 64000);
{ unsigned u = pk_bf16(T[16], T[17]); PIN(u); hh[0] = u; }
{ float v = T[19] * Y[1][3]; PIN(v); T[19] = v; }
{ float v = T[20] * Y[1][8]; PIN(v); T[20] = v; }
SB(); M32A(X[0], AF(1), xb[0][29]); SB();
afl[0] = TR(lbA, 65536);
SETM0(wv2k + 49152);
{ unsigned u = pk_bf16(T[18], T[19]); PIN(u); hh[1] = u; }
{ float v = T[21] * Y[1][9]; PIN(v); T[21] = v; }
{ float v = T[22] * Y[1][10]; PIN(v); T[22] = v; }
SB(); M32A(X[1], AF(1), xb[1][29]); SB();
afh[0] = TR(lbA, 66048);
GLDS(lvoffA, lbase, 0);
{ unsigned u = pk_bf16(T[20], T[21]); PIN(u); hh[2] = u; }
{ float v = T[23] * Y[1][11]; PIN(v); T[23] = v; }
SB(); M32V(X[0], AF(2), xb[0][30]); SB();
afl[1] = TR(lbA, 67584);
{ unsigned u = pk_bf16(T[22], T[23]); PIN(u); hh[3] = u; }
{ float v = T[24] * Y[1][4]; PIN(v); T[24] = v; }
{ float v = T[25] * Y[1][5]; PIN(v); T[25] = v; }
{ float v = T[26] * Y[1][6]; PIN(v); T[26] = v; }
SB(); M32V(X[1], AF(2), xb[1][30]); SB();
afh[1] = TR(lbA, 68096);
*(AS3 u32x4*)(uintptr_t)(hw_ad + 34816) = hh;
{ float v = T[27] * Y[1][7]; PIN(v); T[27] = v; }
SB(); M32V(X[0], AF(3), xb[0][31]); SB();
afl[2] = TR(lbA, 69632);
GLDS(lvoffB, lbase, 1024);
ST41(hxvo, hh, hxw + 34816);
SB(); M32V(X[1], AF(3), xb[1][31]); SB();
afh[2] = TR(lbA, 70144);
lbase += lstep;
SB();
asm volatile("s_waitcnt vmcnt(9)\n\ts_barrier" : "+v"(pw0) :: "memory");
SB(); M32V(X[0], AF(0), xb[0][32]); SB();
afl[3] = TR(lbA, 71680);
{ unsigned u = pk_bf16(T[24], T[25]); PIN(u); hh[0] = u; }
{ unsigned u = pk_bf16(T[26], T[27]); PIN(u); hh[1] = u; }
{ float v = T[28] * Y[1][12]; PIN(v); T[28] = v; }
{ float v = T[29] * Y[1][13]; PIN(v); T[29] = v; }
SB(); M32V(X[1], AF(0), xb[1][32]); SB();
afh[3] = TR(lbA, 72192);
{ unsigned u = pk_bf16(T[28], T[29]); PIN(u); hh[2] = u; }
{ float v = T[30] * Y[1][14]; PIN(v); T[30] = v; }
{ float v = T[31] * Y[1][15]; PIN(v); T[31] = v; }
SB(); M32V(X[0], AF(1), xb[0][33]); SB();
afl[0] = TR(lbA, 0);
SETM0(wv2k + 57344);
{ unsigned u = pk_bf16(T[30], T[31]); PIN(u); hh[3] = u; }
SB(); M32V(X[1], AF(1), xb[1][33]); SB();
afh[0] = TR(lbA, 512);
GLDS(lvoffA, lbase, 0);
*(AS3 u32x4*)(uintptr_t)(hw_ad + 35328) = hh;
SB(); M32V(X[0], AF(2), xb[0][34]); SB();
afl[1] = TR(lbA, 2048);
ST41(hxvo, hh, hxw + 35328);
SB(); M32V(X[1], AF(2), xb[1][34]); SB();
afh[1] = TR(lbA, 2560);
SB(); M32V(X[0], AF(3), xb[0][35]); SB();
afl[2] = TR(lbA, 4096);
GLDS(lvoffB, lbase, 1024);
SB(); M32V(X[1], AF(3), xb[1][35]); SB();
afh[2] = TR(lbA, 4608);
lbase += lstep;
}
STAMP(6); JIT(6);
  const unsigned ln2 = lane_id();
  const unsigned c16P2 = ((ln2 & 7) ^ (((ln2 >> 4) & 3) << 1)) * 16;
  const unsigned swl = ((ln2 >> 3) & 1) | (((ln2 >> 4) & 1) << 1);
  const unsigned lbB0 = (4 * (ln2 >> 4) + ((ln2 & 15) >> 2)) * 128 + (((unsigned)(2 * cg) ^ swl) * 32) + 8 * (ln2 & 3), lbB1 = lbB0 ^ 32;
  const unsigned pvo = ln2 * 16;
  const unsigned hr_ad = H_OFF + (th * 4) * 1024 + pvo;
  const unsigned svo = ((ln2 >> 4) & 1) * (18 * NSL * 1024) + ((ln2 >> 5) * 16 + (ln2 & 15)) * 16;
  const unsigned lbW0 = lbA ^ (unsigned)(64 * cg), lbW1 = lbW0 ^ 64u;

MNOPX(X);
if (nw > 0) {
u32x4 ob[36];
{
const char* nbase = wob; const unsigned nstride = 1152u;
SB();
asm volatile("s_waitcnt vmcnt(17)\n\ts_barrier" ::: "memory");
CHECK_GE(pw0, fOw, 16);
SB(); M32ZA(Y[0], AF(0), xb[0][0]); SB();
afl[3] = TR(lbA, 6144);
T[0] = X[0][0];
T[1] = X[0][1];
T[2] = X[0][2];
T[3] = X[0][3];
T[4] = X[0][8];
T[5] = X[0][9];
T[6] = X[0][10];
T[7] = X[0][11];
T[8] = X[0][4];
T[9] = X[0][5];
T[10] = X[0][6];
T[11] = X[0][7];
T[12] = X[0][12];
T[13] = X[0][13];
T[14] = X[0][14];
T[15] = X[0][15];
T[16] = X[1][0];
T[17] = X[1][1];
T[18] = X[1][2];
T[19] = X[1][3];
T[20] = X[1][8];
T[21] = X[1][9];
T[22] = X[1][10];
T[23] = X[1][11];
T[24] = X[1][4];
T[25] = X[1][5];
T[26] = X[1][6];
T[27] = X[1][7];
T[28] = X[1][12];
T[29] = X[1][13];
T[30] = X[1][14];
T[31] = X[1][15];
SB(); M32ZA(Y[1], AF(0), xb[1][0]); SB();
afh[3] = TR(lbA, 6656);
SB(); M32AS(Y[0], AF(1), xb[0][1]); SB();
afl[0] = TR(lbA, 8192);
SETM0(wv2k + 65536);
LDA1(ob[0], xo0, osrc + 0);
SB(); M32AS(Y[1], AF(1), xb[1][1]); SB();
afh[0] = TR(lbA, 8704);
GLDS(lvoffA, lbase, 0);
LDA1(ob[1], xo0, osrc + 1024);
SB(); M32AS(Y[0], AF(2), xb[0][2]); SB();
afl[1] = TR(lbA, 10240);
SB(); M32AS(Y[1], AF(2), xb[1][2]); SB();
afh[1] = TR(lbA, 10752);
LDA1(ob[2], xo0, osrc + 2048);
SB(); M32AS(Y[0], AF(3), xb[0][3]); SB();
afl[2] = TR(lbA, 12288);
GLDS(lvoffB, lbase, 1024);
LDA1(ob[3], xo0, osrc + 3072);
gt0 = T[0] * T[0]; PIN(gt0);
SB(); M32AS(Y[1], AF(3), xb[1][3]); SB();
afh[2] = TR(lbA, 12800);
lbase = nbase; lstep = 64 * nstride; lvoffA = rowA * nstride + c16P1; lvoffB = (rowA + 8) * nstride + c16P1 - 1024;
gt0 = __builtin_fmaf(gt0, C1, C0); PIN(gt0);
SB();
asm volatile("s_waitcnt vmcnt(21)\n\ts_barrier" ::: "memory");
SB(); M32AS(Y[0], AF(0), xb[0][4]); SB();
afl[3] = TR(lbA, 14336);
LDA1(ob[4], xo0, osrc + 4096);
gt0 = T[0] * gt0; PIN(gt0);
gt1 = T[1] * T[1]; PIN(gt1);
SB(); M32AS(Y[1], AF(0), xb[1][4]); SB();
afh[3] = TR(lbA, 14848);
LDA1(ob[5], xo0, osrc + 5120);
gt0 = __builtin_amdgcn_exp2f(gt0); PIN(gt0);
gt1 = __builtin_fmaf(gt1, C1, C0); PIN(gt1);
SB(); M32AS(Y[0], AF(1), xb[0][5]); SB();
afl[0] = TR(lbA, 16384);
SETM0(wv2k + 0);
gt0 = gt0 + 1.0f; PIN(gt0);
gt1 = T[1] * gt1; PIN(gt1);
gt2 = T[2] * T[2]; PIN(gt2);
SB(); M32AS(Y[1], AF(1), xb[1][5]); SB();
afh[0] = TR(lbA, 16896);
GLDS(lvoffA, lbase, 0);
LDA1(ob[6], xo0, osrc + 6144);
gt0 = __builtin_amdgcn_rcpf(gt0); PIN(gt0);
SB(); M32AS(Y[0], AF(2), xb[0][6]); SB();
afl[1] = TR(lbA, 18432);
LDA1(ob[7], xo0, osrc + 7168);
{ float v = T[0] * gt0; PIN(v); T[0] = v; }
gt1 = __builtin_amdgcn_exp2f(gt1); PIN(gt1);
gt2 = __builtin_fmaf(gt2, C1, C0); PIN(gt2);
SB(); M32AS(Y[1], AF(2), xb[1][6]); SB();
afh[1] = TR(lbA, 18944);
gt1 = gt1 + 1.0f; PIN(gt1);
gt2 = T[2] * gt2; PIN(gt2);
gt0 = T[3] * T[3]; PIN(gt0);
SB(); M32AS(Y[0], AF(3), xb[0][7]); SB();
afl[2] = TR(lbA, 20480);
GLDS(lvoffB, lbase, 1024);
LDA1(ob[8], xo0, osrc + 8192);
gt1 = __builtin_amdgcn_rcpf(gt1); PIN(gt1);
gt0 = __builtin_fmaf(gt0, C1, C0); PIN(gt0);
SB(); M32AS(Y[1], AF(3), xb[1][7]); SB();
afh[2] = TR(lbA, 20992);
lbase += lstep;
LDA1(ob[9], xo0, osrc + 9216);
{ float v = T[1] * gt1; PIN(v); T[1] = v; }
SB();
asm volatile("s_waitcnt vmcnt(27)\n\ts_barrier" ::: "memory");
SB(); M32AS(Y[0], AF(0), xb[0][8]); SB();
afl[3] = TR(lbA, 22528);
gt2 = __builtin_amdgcn_exp2f(gt2); PIN(gt2);
gt0 = T[3] * gt0; PIN(gt0);
gt1 = T[4] * T[4]; PIN(gt1);
SB(); M32AS(Y[1], AF(0), xb[1][8]); SB();
afh[3] = TR(lbA, 23040);
LDA1(ob[10], xo0, osrc + 10240);
gt2 = gt2 + 1.0f; PIN(gt2);
gt0 = __builtin_amdgcn_exp2f(gt0); PIN(gt0);
gt1 = __builtin_fmaf(gt1, C1, C0); PIN(gt1);
SB(); M32AS(Y[0], AF(1), xb[0][9]); SB();
afl[0] = TR(lbA, 24576);
SETM0(wv2k + 8192);
LDA1(ob[11], xo0, osrc + 11264);
gt2 = __builtin_amdgcn_rcpf(gt2); PIN(gt2);
gt0 = gt0 + 1.0f; PIN(gt0);
SB(); M32AS(Y[1], AF(1), xb[1][9]); SB();
afh[0] = TR(lbA, 25088);
GLDS(lvoffA, lbase, 0);
{ float v = T[2] * gt2; PIN(v); T[2] = v; }
gt0 = __builtin_amdgcn_rcpf(gt0); PIN(gt0);
gt1 = T[4] * gt1; PIN(gt1);
SB(); M32AS(Y[0], AF(2), xb[0][10]); SB();
afl[1] = TR(lbA, 26624);
LDA1(ob[12], xo0, osrc + 12288);
{ float v = T[3] * gt0; PIN(v); T[3] = v; }
gt1 = __builtin_amdgcn_exp2f(gt1); PIN(gt1);
gt2 = T[5] * T[5]; PIN(gt2);
SB(); M32AS(Y[1], AF(2), xb[1][10]); SB();
afh[1] = TR(lbA, 27136);
LDA1(ob[13], xo0, osrc + 13312);
gt1 = gt1 + 1.0f; PIN(gt1);
gt2 = __builtin_fmaf(gt2, C1, C0); PIN(gt2);
gt0 = T[6] * T[6]; PIN(gt0);
SB(); M32AS(Y[0], AF(3), xb[0][11]); SB();
afl[2] = TR(lbA, 28672);
GLDS(lvoffB, lbase, 1024);
gt1 = __builtin_amdgcn_rcpf(gt1); PIN(gt1);
gt2 = T[5] * gt2; PIN(gt2);
gt0 = __builtin_fmaf(gt0, C1, C0); PIN(gt0);
SB(); M32AS(Y[1], AF(3), xb[1][11]); SB();
afh[2] = TR(lbA, 29184);
lbase += lstep;
LDA1(ob[14], xo0, osrc + 14336);
{ float v = T[4] * gt1; PIN(v); T[4] = v; }
SB();
asm volatile("s_waitcnt vmcnt(22)\n\ts_barrier" ::: "memory");
SIGNAL_SET(fHme, 3, true);
SB(); M32AS(Y[0], AF(0), xb[0][12]); SB();
afl[3] = TR(lbA, 30720);
LDA1(ob[15], xo0, osrc + 15360);
gt2 = __builtin_amdgcn_exp2f(gt2); PIN(gt2);
gt0 = T[6] * gt0; PIN(gt0);
gt1 = T[7] * T[7]; PIN(gt1);
SB(); M32AS(Y[1], AF(0), xb[1][12]); SB();
afh[3] = TR(lbA, 31232);
gt2 = gt2 + 1.0f; PIN(gt2);
gt0 = __builtin_amdgcn_exp2f(gt0); PIN(gt0);
gt1 = __builtin_fmaf(gt1, C1, C0); PIN(gt1);
SB(); M32AS(Y[0], AF(1), xb[0][13]); SB();
afl[0] = TR(lbA, 32768);
SETM0(wv2k + 16384);
LDA1(ob[16], xo0, osrc + 16384);
gt2 = __builtin_amdgcn_rcpf(gt2); PIN(gt2);
gt0 = gt0 + 1.0f; PIN(gt0);
SB(); M32AS(Y[1], AF(1), xb[1][13]); SB();
afh[0] = TR(lbA, 33280);
GLDS(lvoffA, lbase, 0);
LDA1(ob[17], xo0, osrc + 17408);
{ float v = T[5] * gt2; PIN(v); T[5] = v; }
gt1 = T[7] * gt1; PIN(gt1);
SB(); M32AS(Y[0], AF(2), xb[0][14]); SB();
afl[1] = TR(lbA, 34816);
gt0 = __builtin_amdgcn_rcpf(gt0); PIN(gt0);
gt1 = __builtin_amdgcn_exp2f(gt1); PIN(gt1);
gt2 = T[8] * T[8]; PIN(gt2);
SB(); M32AS(Y[1], AF(2), xb[1][14]); SB();
afh[1] = TR(lbA, 35328);
LDA1(ob[18], xo0, osrc + 18432);
{ float v = T[6] * gt0; PIN(v); T[6] = v; }
gt1 = gt1 + 1.0f; PIN(gt1);
gt2 = __builtin_fmaf(gt2, C1, C0); PIN(gt2);
SB(); M32AS(Y[0], AF(3), xb[0][15]); SB();
afl[2] = TR(lbA, 36864);
GLDS(lvoffB, lbase, 1024);
LDA1(ob[19], xo0, osrc + 19456);
gt1 = __builtin_amdgcn_rcpf(gt1); PIN(gt1);
gt2 = T[8] * gt2; PIN(gt2);
SB(); M32AS(Y[1], AF(3), xb[1][15]); SB();
afh[2] = TR(lbA, 37376);
lbase += lstep;
{ float v = T[7] * gt1; PIN(v); T[7] = v; }
gt0 = T[9] * T[9]; PIN(gt0);
SB();
asm volatile("s_waitcnt vmcnt(35)\n\ts_barrier" ::: "memory");
SB(); M32AS(Y[0], AF(0), xb[0][16]); SB();
afl[3] = TR(lbA, 38912);
LDA1(ob[20], xo0, osrc + 20480);
gt2 = __builtin_amdgcn_exp2f(gt2); PIN(gt2);
gt0 = __builtin_fmaf(gt0, C1, C0); PIN(gt0);
gt1 = T[10] * T[10]; PIN(gt1);
SB(); M32AS(Y[1], AF(0), xb[1][16]); SB();
afh[3] = TR(lbA, 39424);
LDA1(ob[21], xo0, osrc + 21504);
gt2 = gt2 + 1.0f; PIN(gt2);
gt0 = T[9] * gt0; PIN(gt0);
gt1 = __builtin_fmaf(gt1, C1, C0); PIN(gt1);
SB(); M32AS(Y[0], AF(1), xb[0][17]); SB();
afl[0] = TR(lbA, 40960);
SETM0(wv2k + 24576);
gt2 = __builtin_amdgcn_rcpf(gt2); PIN(gt2);
gt0 = __builtin_amdgcn_exp2f(gt0); PIN(gt0);
gt1 = T[10] * gt1; PIN(gt1);
SB(); M32AS(Y[1], AF(1), xb[1][17]); SB();
afh[0] = TR(lbA, 41472);
GLDS(lvoffA, lbase, 0);
LDA1(ob[22], xo0, osrc + 22528);
{ float v = T[8] * gt2; PIN(v); T[8] = v; }
gt0 = gt0 + 1.0f; PIN(gt0);
SB(); M32AS(Y[0], AF(2), xb[0][18]); SB();
afl[1] = TR(lbA, 43008);
LDA1(ob[23], xo0, osrc + 23552);
gt0 = __builtin_amdgcn_rcpf(gt0); PIN(gt0);
gt1 = __builtin_amdgcn_exp2f(gt1); PIN(gt1);
SB(); M32AS(Y[1], AF(2), xb[1][18]); SB();
afh[1] = TR(lbA, 43520);
{ float v = T[9] * gt0; PIN(v); T[9] = v; }
gt1 = gt1 + 1.0f; PIN(gt1);
gt2 = T[11] * T[11]; PIN(gt2);
SB(); M32AS(Y[0], AF(3), xb[0][19]); SB();
afl[2] = TR(lbA, 45056);
GLDS(lvoffB, lbase, 1024);
LDA1(ob[24], xo0, osrc + 24576);
gt1 = __builtin_amdgcn_rcpf(gt1); PIN(gt1);
gt2 = __builtin_fmaf(gt2, C1, C0); PIN(gt2);
SB(); M32AS(Y[1], AF(3), xb[1][19]); SB();
afh[2] = TR(lbA, 45568);
lbase += lstep;
LDA1(ob[25], xo0, osrc + 25600);
{ float v = T[10] * gt1; PIN(v); T[10] = v; }
SB();
asm volatile("s_waitcnt vmcnt(40)\n\ts_barrier" ::: "memory");
SB(); M32AS(Y[0], AF(0), xb[0][20]); SB();
afl[3] = TR(lbA, 47104);
gt2 = T[11] * gt2; PIN(gt2);
gt0 = T[12] * T[12]; PIN(gt0);
gt1 = T[13] * T[13]; PIN(gt1);
SB(); M32AS(Y[1], AF(0), xb[1][20]); SB();
afh[3] = TR(lbA, 47616);
LDA1(ob[26], xo0, osrc + 26624);
gt2 = __builtin_amdgcn_exp2f(gt2); PIN(gt2);
gt0 = __builtin_fmaf(gt0, C1, C0); PIN(gt0);
gt1 = __builtin_fmaf(gt1, C1, C0); PIN(gt1);
SB(); M32AS(Y[0], AF(1), xb[0][21]); SB();
afl[0] = TR(lbA, 49152);
SETM0(wv2k + 32768);
LDA1(ob[27], xo0, osrc + 27648);
gt2 = gt2 + 1.0f; PIN(gt2);
gt0 = T[12] * gt0; PIN(gt0);
gt1 = T[13] * gt1; PIN(gt1);
SB(); M32AS(Y[1], AF(1), xb[1][21]); SB();
afh[0] = TR(lbA, 49664);
GLDS(lvoffA, lbase, 0);
gt2 = __builtin_amdgcn_rcpf(gt2); PIN(gt2);
gt0 = __builtin_amdgcn_exp2f(gt0); PIN(gt0);
SB(); M32AS(Y[0], AF(2), xb[0][22]); SB();
afl[1] = TR(lbA, 51200);
LDA1(ob[28], xo0, osrc + 28672);
{ float v = T[11] * gt2; PIN(v); T[11] = v; }
gt0 = gt0 + 1.0f; PIN(gt0);
gt1 = __builtin_amdgcn_exp2f(gt1); PIN(gt1);
SB(); M32AS(Y[1], AF(2), xb[1][22]); SB();
afh[1] = TR(lbA, 51712);
LDA1(ob[29], xo0, osrc + 29696);
gt0 = __builtin_amdgcn_rcpf(gt0); PIN(gt0);
gt1 = gt1 + 1.0f; PIN(gt1);
gt2 = T[14] * T[14]; PIN(gt2);
SB(); M32AS(Y[0], AF(3), xb[0][23]); SB();
afl[2] = TR(lbA, 53248);
GLDS(lvoffB, lbase, 1024);
{ float v = T[12] * gt0; PIN(v); T[12] = v; }
gt1 = __builtin_amdgcn_rcpf(gt1); PIN(gt1);
gt2 = __builtin_fmaf(gt2, C1, C0); PIN(gt2);
SB(); M32AS(Y[1], AF(3), xb[1][23]); SB();
afh[2] = TR(lbA, 53760);
lbase += lstep;
LDA1(ob[30], xo0, osrc + 30720);
{ float v = T[13] * gt1; PIN(v); T[13] = v; }
SB();
asm volatile("s_waitcnt vmcnt(43)\n\ts_barrier" ::: "memory");
SB(); M32AS(Y[0], AF(0), xb[0][24]); SB();
afl[3] = TR(lbA, 55296);
LDA1(ob[31], xo0, osrc + 31744);
gt2 = T[14] * gt2; PIN(gt2);
gt0 = T[15] * T[15]; PIN(gt0);
gt1 = T[16] * T[16]; PIN(gt1);
SB(); M32AS(Y[1], AF(0), xb[1][24]); SB();
afh[3] = TR(lbA, 55808);
gt2 = __builtin_amdgcn_exp2f(gt2); PIN(gt2);
gt0 = __builtin_fmaf(gt0, C1, C0); PIN(gt0);
gt1 = __builtin_fmaf(gt1, C1, C0); PIN(gt1);
SB(); M32AS(Y[0], AF(1), xb[0][25]); SB();
afl[0] = TR(lbA, 57344);
SETM0(wv2k + 40960);
LDA1(ob[32], xo0, osrc + 32768);
gt2 = gt2 + 1.0f; PIN(gt2);
gt0 = T[15] * gt0; PIN(gt0);
gt1 = T[16] * gt1; PIN(gt1);
SB(); M32AS(Y[1], AF(1), xb[1][25]); SB();
afh[0] = TR(lbA, 57856);
GLDS(lvoffA, lbase, 0);
LDA1(ob[33], xo0, osrc + 33792);
gt2 = __builtin_amdgcn_rcpf(gt2); PIN(gt2);
SB(); M32AS(Y[0], AF(2), xb[0][26]); SB();
afl[1] = TR(lbA, 59392);
{ float v = T[14] * gt2; PIN(v); T[14] = v; }
gt0 = __builtin_amdgcn_exp2f(gt0); PIN(gt0);
gt1 = __builtin_amdgcn_exp2f(gt1); PIN(gt1);
SB(); M32AS(Y[1], AF(2), xb[1][26]); SB();
afh[1] = TR(lbA, 59904);
LDA1(ob[34], xo0, osrc + 34816);
gt0 = gt0 + 1.0f; PIN(gt0);
gt1 = gt1 + 1.0f; PIN(gt1);
gt2 = T[17] * T[17]; PIN(gt2);
SB(); M32AS(Y[0], AF(3), xb[0][27]); SB();
afl[2] = TR(lbA, 61440);
GLDS(lvoffB, lbase, 1024);
LDA1(ob[35], xo0, osrc + 35840);
gt0 = __builtin_amdgcn_rcpf(gt0); PIN(gt0);
gt2 = __builtin_fmaf(gt2, C1, C0); PIN(gt2);
SB(); M32AS(Y[1], AF(3), xb[1][27]); SB();
afh[2] = TR(lbA, 61952);
lbase += lstep;
{ float v = T[15] * gt0; PIN(v); T[15] = v; }
gt2 = T[17] * gt2; PIN(gt2);
SB();
asm volatile("s_waitcnt vmcnt(45)\n\ts_barrier" ::: "memory");
SB(); M32AS(Y[0], AF(0), xb[0][28]); SB();
afl[3] = TR(lbA, 63488);
gt1 = __builtin_amdgcn_rcpf(gt1); PIN(gt1);
gt2 = __builtin_amdgcn_exp2f(gt2); PIN(gt2);
gt0 = T[18] * T[18]; PIN(gt0);
SB(); M32AS(Y[1], AF(0), xb[1][28]); SB();
afh[3] = TR(lbA, 64000);
{ float v = T[16] * gt1; PIN(v); T[16] = v; }
gt2 = gt2 + 1.0f; PIN(gt2);
gt0 = __builtin_fmaf(gt0, C1, C0); PIN(gt0);
SB(); M32AS(Y[0], AF(1), xb[0][29]); SB();
afl[0] = TR(lbA, 65536);
SETM0(wv2k + 49152);
gt2 = __builtin_amdgcn_rcpf(gt2); PIN(gt2);
gt0 = T[18] * gt0; PIN(gt0);
gt1 = T[19] * T[19]; PIN(gt1);
SB(); M32AS(Y[1], AF(1), xb[1][29]); SB();
afh[0] = TR(lbA, 66048);
GLDS(lvoffA, lbase, 0);
{ float v = T[17] * gt2; PIN(v); T[17] = v; }
gt0 = __builtin_amdgcn_exp2f(gt0); PIN(gt0);
gt1 = __builtin_fmaf(gt1, C1, C0); PIN(gt1);
SB(); M32VS(Y[0], AF(2), xb[0][30]); SB();
afl[1] = TR(lbA, 67584);
gt0 = gt0 + 1.0f; PIN(gt0);
gt1 = T[19] * gt1; PIN(gt1);
gt2 = T[20] * T[20]; PIN(gt2);
SB(); M32VS(Y[1], AF(2), xb[1][30]); SB();
afh[1] = TR(lbA, 68096);
gt0 = __builtin_amdgcn_rcpf(gt0); PIN(gt0);
gt1 = __builtin_amdgcn_exp2f(gt1); PIN(gt1);
gt2 = __builtin_fmaf(gt2, C1, C0); PIN(gt2);
SB(); M32VS(Y[0], AF(3), xb[0][31]); SB();
afl[2] = TR(lbA, 69632);
GLDS(lvoffB, lbase, 1024);
{ float v = T[18] * gt0; PIN(v); T[18] = v; }
gt1 = gt1 + 1.0f; PIN(gt1);
gt2 = T[20] * gt2; PIN(gt2);
SB(); M32VS(Y[1], AF(3), xb[1][31]); SB();
afh[2] = TR(lbA, 70144);
lbase += lstep;
gt1 = __builtin_amdgcn_rcpf(gt1); PIN(gt1);
SB();
asm volatile("s_waitcnt vmcnt(40)\n\ts_barrier" ::: "memory");
SB(); M32VS(Y[0], AF(0), xb[0][32]); SB();
afl[3] = TR(lbA, 71680);
{ float v = T[19] * gt1; PIN(v); T[19] = v; }
gt2 = __builtin_amdgcn_exp2f(gt2); PIN(gt2);
gt0 = T[21] * T[21]; PIN(gt0);
SB(); M32VS(Y[1], AF(0), xb[1][32]); SB();
afh[3] = TR(lbA, 72192);
gt2 = gt2 + 1.0f; PIN(gt2);
gt0 = __builtin_fmaf(gt0, C1, C0); PIN(gt0);
gt1 = T[22] * T[22]; PIN(gt1);
SB(); M32VS(Y[0], AF(1), xb[0][33]); SB();
afl[0] = TR(lbW0, 0);
SETM0(wv2k + 57344);
gt2 = __builtin_amdgcn_rcpf(gt2); PIN(gt2);
gt0 = T[21] * gt0; PIN(gt0);
gt1 = __builtin_fmaf(gt1, C1, C0); PIN(gt1);
SB(); M32VS(Y[1], AF(1), xb[1][33]); SB();
afh[0] = TR(lbW0, 512);
GLDS(lvoffA, lbase, 0);
{ float v = T[20] * gt2; PIN(v); T[20] = v; }
gt0 = __builtin_amdgcn_exp2f(gt0); PIN(gt0);
gt1 = T[22] * gt1; PIN(gt1);
SB(); M32VS(Y[0], AF(2), xb[0][34]); SB();
afl[1] = TR(lbW1, 0);
gt0 = gt0 + 1.0f; PIN(gt0);
gt1 = __builtin_amdgcn_exp2f(gt1); PIN(gt1);
gt2 = T[23] * T[23]; PIN(gt2);
SB(); M32VS(Y[1], AF(2), xb[1][34]); SB();
afh[1] = TR(lbW1, 512);
gt0 = __builtin_amdgcn_rcpf(gt0); PIN(gt0);
gt1 = gt1 + 1.0f; PIN(gt1);
gt2 = __builtin_fmaf(gt2, C1, C0); PIN(gt2);
SB(); M32VS(Y[0], AF(3), xb[0][35]); SB();
afl[2] = TR(lbW0, 2048);
GLDS(lvoffB, lbase, 1024);
{ float v = T[21] * gt0; PIN(v); T[21] = v; }
gt1 = __builtin_amdgcn_rcpf(gt1); PIN(gt1);
gt2 = T[23] * gt2; PIN(gt2);
SB(); M32VS(Y[1], AF(3), xb[1][35]); SB();
afh[2] = TR(lbW0, 2560);
lbase += lstep;
{ float v = T[22] * gt1; PIN(v); T[22] = v; }
gt0 = T[24] * T[24]; PIN(gt0);
}
STAMP(7);
{
SB();
asm volatile("s_waitcnt vmcnt(34)\n\ts_barrier" : "+a"(ob[0]), "+a"(ob[1]), "+a"(ob[2]), "+a"(ob[3]) :: "memory");
SB(); M32ZA(X[0], AF(0), ob[0]); SB();
afl[3] = TR(lbW1, 2048);
afh[3] = TR(lbW1, 2560);
gt2 = __builtin_amdgcn_exp2f(gt2); PIN(gt2);
gt0 = __builtin_fmaf(gt0, C1, C0); PIN(gt0);
SB(); M32ZA(X[1], AF(1), ob[0]); SB();
afl[0] = TR(lbW0, 4096);
afh[0] = TR(lbW0, 4608);
gt2 = gt2 + 1.0f; PIN(gt2);
gt0 = T[24] * gt0; PIN(gt0);
gt1 = T[25] * T[25]; PIN(gt1);
SB(); M32A(X[0], AF(2), ob[1]); SB();
afl[1] = TR(lbW1, 4096);
afh[1] = TR(lbW1, 4608);
SETM0(wv2k + 65536);
gt2 = __builtin_amdgcn_rcpf(gt2); PIN(gt2);
SB(); M32A(X[1], AF(3), ob[1]); SB();
afl[2] = TR(lbW0, 6144);
afh[2] = TR(lbW0, 6656);
GLDS(lvoffA, lbase, 0);
{ float v = T[23] * gt2; PIN(v); T[23] = v; }
gt1 = __builtin_fmaf(gt1, C1, C0); PIN(gt1);
SB(); M32A(X[0], AF(0), ob[2]); SB();
afl[3] = TR(lbW1, 6144);
afh[3] = TR(lbW1, 6656);
gt0 = __builtin_amdgcn_exp2f(gt0); PIN(gt0);
gt1 = T[25] * gt1; PIN(gt1);
SB(); M32A(X[1], AF(1), ob[2]); SB();
afl[0] = TR(lbW0, 8192);
afh[0] = TR(lbW0, 8704);
gt0 = gt0 + 1.0f; PIN(gt0);
gt1 = __builtin_amdgcn_exp2f(gt1); PIN(gt1);
SB(); M32A(X[0], AF(2), ob[3]); SB();
afl[1] = TR(lbW1, 8192);
afh[1] = TR(lbW1, 8704);
GLDS(lvoffB, lbase, 1024);
gt0 = __builtin_amdgcn_rcpf(gt0); PIN(gt0);
SB(); M32A(X[1], AF(3), ob[3]); SB();
afl[2] = TR(lbW0, 10240);
afh[2] = TR(lbW0, 10752);
lbase = wdb + 369152; lvoffA = rowA * 1152 + c16P2; lvoffB = (rowA + 8) * 1152 + c16P2 - 1024;
SB();
asm volatile("s_waitcnt vmcnt(29)\n\ts_barrier" : "+a"(ob[4]), "+a"(ob[5]), "+a"(ob[6]), "+a"(ob[7]) :: "memory");
SB(); M32A(X[0], AF(0), ob[4]); SB();
afl[3] = TR(lbW1, 10240);
afh[3] = TR(lbW1, 10752);
{ float v = T[24] * gt0; PIN(v); T[24] = v; }
gt1 = gt1 + 1.0f; PIN(gt1);
gt2 = T[26] * T[26]; PIN(gt2);
SB(); M32A(X[1], AF(1), ob[4]); SB();
afl[0] = TR(lbW0, 12288);
afh[0] = TR(lbW0, 12800);
gt1 = __builtin_amdgcn_rcpf(gt1); PIN(gt1);
gt2 = __builtin_fmaf(gt2, C1, C0); PIN(gt2);
SB(); M32A(X[0], AF(2), ob[5]); SB();
afl[1] = TR(lbW1, 12288);
afh[1] = TR(lbW1, 12800);
SETM0(wv2k + 0);
{ float v = T[25] * gt1; PIN(v); T[25] = v; }
gt2 = T[26] * gt2; PIN(gt2);
SB(); M32A(X[1], AF(3), ob[5]); SB();
afl[2] = TR(lbW0, 14336);
afh[2] = TR(lbW0, 14848);
GLDS(lvoffA, lbase, 0);
gt2 = __builtin_amdgcn_exp2f(gt2); PIN(gt2);
SB(); M32A(X[0], AF(0), ob[6]); SB();
afl[3] = TR(lbW1, 14336);
afh[3] = TR(lbW1, 14848);
gt2 = gt2 + 1.0f; PIN(gt2);
gt0 = T[27] * T[27]; PIN(gt0);
gt1 = T[28] * T[28]; PIN(gt1);
SB(); M32A(X[1], AF(1), ob[6]); SB();
afl[0] = TR(lbW0, 16384);
afh[0] = TR(lbW0, 16896);
gt2 = __builtin_amdgcn_rcpf(gt2); PIN(gt2);
gt0 = __builtin_fmaf(gt0, C1, C0); PIN(gt0);
SB(); M32A(X[0], AF(2), ob[7]); SB();
afl[1] = TR(lbW1, 16384);
afh[1] = TR(lbW1, 16896);
GLDS(lvoffB, lbase, 1024);
{ float v = T[26] * gt2; PIN(v); T[26] = v; }
gt0 = T[27] * gt0; PIN(gt0);
SB(); M32A(X[1], AF(3), ob[7]); SB();
afl[2] = TR(lbW0, 18432);
afh[2] = TR(lbW0, 18944);
lbase = wdb + 442880;
SB();
asm volatile("s_waitcnt vmcnt(24)\n\ts_barrier" : "+a"(ob[8]), "+a"(ob[9]), "+a"(ob[10]), "+a"(ob[11]) :: "memory");
SB(); M32A(X[0], AF(0), ob[8]); SB();
afl[3] = TR(lbW1, 18432);
afh[3] = TR(lbW1, 18944);
gt0 = __builtin_amdgcn_exp2f(gt0); PIN(gt0);
gt1 = __builtin_fmaf(gt1, C1, C0); PIN(gt1);
SB(); M32A(X[1], AF(1), ob[8]); SB();
afl[0] = TR(lbW0, 20480);
afh[0] = TR(lbW0, 20992);
gt0 = gt0 + 1.0f; PIN(gt0);
gt1 = T[28] * gt1; PIN(gt1);
gt2 = T[29] * T[29]; PIN(gt2);
SB(); M32A(X[0], AF(2), ob[9]); SB();
afl[1] = TR(lbW1, 20480);
afh[1] = TR(lbW1, 20992);
SETM0(wv2k + 8192);
gt0 = __builtin_amdgcn_rcpf(gt0); PIN(gt0);
SB(); M32A(X[1], AF(3), ob[9]); SB();
afl[2] = TR(lbW0, 22528);
afh[2] = TR(lbW0, 23040);
GLDS(lvoffA, lbase, 0);
{ float v = T[27] * gt0; PIN(v); T[27] = v; }
gt2 = __builtin_fmaf(gt2, C1, C0); PIN(gt2);
SB(); M32A(X[0], AF(0), ob[10]); SB();
afl[3] = TR(lbW1, 22528);
afh[3] = TR(lbW1, 23040);
gt1 = __builtin_amdgcn_exp2f(gt1); PIN(gt1);
gt2 = T[29] * gt2; PIN(gt2);
SB(); M32A(X[1], AF(1), ob[10]); SB();
afl[0] = TR(lbW0, 24576);
afh[0] = TR(lbW0, 25088);
gt1 = gt1 + 1.0f; PIN(gt1);
gt2 = __builtin_amdgcn_exp2f(gt2); PIN(gt2);
SB(); M32A(X[0], AF(2), ob[11]); SB();
afl[1] = TR(lbW1, 24576);
afh[1] = TR(lbW1, 25088);
GLDS(lvoffB, lbase, 1024);
gt1 = __builtin_amdgcn_rcpf(gt1); PIN(gt1);
SB(); M32A(X[1], AF(3), ob[11]); SB();
afl[2] = TR(lbW0, 26624);
afh[2] = TR(lbW0, 27136);
lbase = wdb + 516608;
SB();
asm volatile("s_waitcnt vmcnt(18)\n\ts_barrier" : "+a"(ob[12]), "+a"(ob[13]), "+a"(ob[14]), "+a"(ob[15]) :: "memory");
SB(); M32A(X[0], AF(0), ob[12]); SB();
afl[3] = TR(lbW1, 26624);
afh[3] = TR(lbW1, 27136);
{ float v = T[28] * gt1; PIN(v); T[28] = v; }
gt2 = gt2 + 1.0f; PIN(gt2);
gt0 = T[30] * T[30]; PIN(gt0);
SB(); M32A(X[1], AF(1), ob[12]); SB();
afl[0] = TR(lbW0, 28672);
afh[0] = TR(lbW0, 29184);
gt2 = __builtin_amdgcn_rcpf(gt2); PIN(gt2);
gt0 = __builtin_fmaf(gt0, C1, C0); PIN(gt0);
SB(); M32A(X[0], AF(2), ob[13]); SB();
afl[1] = TR(lbW1, 28672);
afh[1] = TR(lbW1, 29184);
SETM0(wv2k + 16384);
{ float v = T[29] * gt2; PIN(v); T[29] = v; }
gt0 = T[30] * gt0; PIN(gt0);
SB(); M32A(X[1], AF(3), ob[13]); SB();
afl[2] = TR(lbW0, 30720);
afh[2] = TR(lbW0, 31232);
GLDS(lvoffA, lbase, 0);
gt0 = __builtin_amdgcn_exp2f(gt0); PIN(gt0);
SB(); M32A(X[0], AF(0), ob[14]); SB();
afl[3] = TR(lbW1, 30720);
afh[3] = TR(lbW1, 31232);
gt0 = gt0 + 1.0f; PIN(gt0);
gt1 = T[31] * T[31]; PIN(gt1);
{ float v = T[0] * Y[0][0]; PIN(v); T[0] = v; }
SB(); M32A(X[1], AF(1), ob[14]); SB();
afl[0] = TR(lbW0, 32768);
afh[0] = TR(lbW0, 33280);
gt0 = __builtin_amdgcn_rcpf(gt0); PIN(gt0);
gt1 = __builtin_fmaf(gt1, C1, C0); PIN(gt1);
SB(); M32A(X[0], AF(2), ob[15]); SB();
afl[1] = TR(lbW1, 32768);
afh[1] = TR(lbW1, 33280);
GLDS(lvoffB, lbase, 1024);
{ float v = T[30] * gt0; PIN(v); T[30] = v; }
gt1 = T[31] * gt1; PIN(gt1);
SB(); M32A(X[1], AF(3), ob[15]); SB();
afl[2] = TR(lbW0, 34816);
afh[2] = TR(lbW0, 35328);
lbase = wdb + 590336;
SB();
asm volatile("s_waitcnt vmcnt(13)\n\ts_barrier" : "+a"(ob[16]), "+a"(ob[17]), "+a"(ob[18]), "+a"(ob[19]) :: "memory");
SB(); M32A(X[0], AF(0), ob[16]); SB();
afl[3] = TR(lbW1, 34816);
afh[3] = TR(lbW1, 35328);
gt1 = __builtin_amdgcn_exp2f(gt1); PIN(gt1);
{ float v = T[1] * Y[0][1]; PIN(v); T[1] = v; }
SB(); M32A(X[1], AF(1), ob[16]); SB();
afl[0] = TR(lbW0, 36864);
afh[0] = TR(lbW0, 37376);
gt1 = gt1 + 1.0f; PIN(gt1);
{ unsigned u = pk_bf16(T[0], T[1]); PIN(u); hh[0] = u; }
{ float v = T[2] * Y[0][2]; PIN(v); T[2] = v; }
SB(); M32A(X[0], AF(2), ob[17]); SB();
afl[1] = TR(lbW1, 36864);
afh[1] = TR(lbW1, 37376);
SETM0(wv2k + 24576);
gt1 = __builtin_amdgcn_rcpf(gt1); PIN(gt1);
SB(); M32A(X[1], AF(3), ob[17]); SB();
afl[2] = TR(lbW0, 38912);
afh[2] = TR(lbW0, 39424);
GLDS(lvoffA, lbase, 0);
{ float v = T[31] * gt1; PIN(v); T[31] = v; }
{ float v = T[3] * Y[0][3]; PIN(v); T[3] = v; }
SB(); M32A(X[0], AF(0), ob[18]); SB();
afl[3] = TR(lbW1, 38912);
afh[3] = TR(lbW1, 39424);
{ unsigned u = pk_bf16(T[2], T[3]); PIN(u); hh[1] = u; }
{ float v = T[4] * Y[0][8]; PIN(v); T[4] = v; }
{ float v = T[5] * Y[0][9]; PIN(v); T[5] = v; }
SB(); M32A(X[1], AF(1), ob[18]); SB();
afl[0] = TR(lbW0, 40960);
afh[0] = TR(lbW0, 41472);
{ unsigned u = pk_bf16(T[4], T[5]); PIN(u); hh[2] = u; }
{ float v = T[6] * Y[0][10]; PIN(v); T[6] = v; }
{ float v = T[7] * Y[0][11]; PIN(v); T[7] = v; }
SB(); M32A(X[0], AF(2), ob[19]); SB();
afl[1] = TR(lbW1, 40960);
afh[1] = TR(lbW1, 41472);
GLDS(lvoffB, lbase, 1024);
{ unsigned u = pk_bf16(T[6], T[7]); PIN(u); hh[3] = u; }
{ float v = T[8] * Y[0][4]; PIN(v); T[8] = v; }
SB(); M32A(X[1], AF(3), ob[19]); SB();
afl[2] = TR(lbW0, 43008);
afh[2] = TR(lbW0, 43520);
lbase = wdb + 369280;
SB();
asm volatile("s_waitcnt vmcnt(12)\n\ts_barrier" : "+a"(ob[20]), "+a"(ob[21]), "+a"(ob[22]), "+a"(ob[23]) :: "memory");
SB(); M32A(X[0], AF(0), ob[20]); SB();
afl[3] = TR(lbW1, 43008);
afh[3] = TR(lbW1, 43520);
*(AS3 u32x4*)(uintptr_t)(hw_ad + 49152) = hh;
{ float v = T[9] * Y[0][5]; PIN(v); T[9] = v; }
SB(); M32A(X[1], AF(1), ob[20]); SB();
afl[0] = TR(lbW0, 45056);
afh[0] = TR(lbW0, 45568);
ST41(hxvo, hh, hxw + 49152);
SB(); M32A(X[0], AF(2), ob[21]); SB();
afl[1] = TR(lbW1, 45056);
afh[1] = TR(lbW1, 45568);
SETM0(wv2k + 32768);
{ unsigned u = pk_bf16(T[8], T[9]); PIN(u); hh[0] = u; }
{ float v = T[10] * Y[0][6]; PIN(v); T[10] = v; }
SB(); M32A(X[1], AF(3), ob[21]); SB();
afl[2] = TR(lbW0, 47104);
afh[2] = TR(lbW0, 47616);
GLDS(lvoffA, lbase, 0);
{ float v = T[11] * Y[0][7]; PIN(v); T[11] = v; }
{ float v = T[12] * Y[0][12]; PIN(v); T[12] = v; }
SB(); M32A(X[0], AF(0), ob[22]); SB();
afl[3] = TR(lbW1, 47104);
afh[3] = TR(lbW1, 47616);
{ unsigned u = pk_bf16(T[10], T[11]); PIN(u); hh[1] = u; }
{ float v = T[13] * Y[0][13]; PIN(v); T[13] = v; }
{ float v = T[14] * Y[0][14]; PIN(v); T[14] = v; }
SB(); M32A(X[1], AF(1), ob[22]); SB();
afl[0] = TR(lbW0, 49152);
afh[0] = TR(lbW0, 49664);
{ unsigned u = pk_bf16(T[12], T[13]); PIN(u); hh[2] = u; }
{ float v = T[15] * Y[0][15]; PIN(v); T[15] = v; }
{ float v = T[16] * Y[1][0]; PIN(v); T[16] = v; }
SB(); M32A(X[0], AF(2), ob[23]); SB();
afl[1] = TR(lbW1, 49152);
afh[1] = TR(lbW1, 49664);
GLDS(lvoffB, lbase, 1024);
{ unsigned u = pk_bf16(T[14], T[15]); PIN(u); hh[3] = u; }
{ float v = T[17] * Y[1][1]; PIN(v); T[17] = v; }
SB(); M32A(X[1], AF(3), ob[23]); SB();
afl[2] = TR(lbW0, 51200);
afh[2] = TR(lbW0, 51712);
lbase = wdb + 443008;
SB();
asm volatile("s_waitcnt vmcnt(13)\n\ts_barrier" : "+a"(ob[24]), "+a"(ob[25]), "+a"(ob[26]), "+a"(ob[27]) :: "memory");
SB(); M32A(X[0], AF(0), ob[24]); SB();
afl[3] = TR(lbW1, 51200);
afh[3] = TR(lbW1, 51712);
*(AS3 u32x4*)(uintptr_t)(hw_ad + 49664) = hh;
{ float v = T[18] * Y[1][2]; PIN(v); T[18] = v; }
SB(); M32A(X[1], AF(1), ob[24]); SB();
afl[0] = TR(lbW0, 53248);
afh[0] = TR(lbW0, 53760);
ST41(hxvo, hh, hxw + 49664);
SB(); M32A(X[0], AF(2), ob[25]); SB();
afl[1] = TR(lbW1, 53248);
afh[1] = TR(lbW1, 53760);
SETM0(wv2k + 40960);
{ unsigned u = pk_bf16(T[16], T[17]); PIN(u); hh[0] = u; }
{ float v = T[19] * Y[1][3]; PIN(v); T[19] = v; }
SB(); M32A(X[1], AF(3), ob[25]); SB();
afl[2] = TR(lbW0, 55296);
afh[2] = TR(lbW0, 55808);
GLDS(lvoffA, lbase, 0);
{ unsigned u = pk_bf16(T[18], T[19]); PIN(u); hh[1] = u; }
{ float v = T[20] * Y[1][8]; PIN(v); T[20] = v; }
SB(); M32A(X[0], AF(0), ob[26]); SB();
afl[3] = TR(lbW1, 55296);
afh[3] = TR(lbW1, 55808);
{ float v = T[21] * Y[1][9]; PIN(v); T[21] = v; }
{ float v = T[22] * Y[1][10]; PIN(v); T[22] = v; }
{ float v = T[23] * Y[1][11]; PIN(v); T[23] = v; }
SB(); M32A(X[1], AF(1), ob[26]); SB();
afl[0] = TR(lbW0, 57344);
afh[0] = TR(lbW0, 57856);
{ unsigned u = pk_bf16(T[20], T[21]); PIN(u); hh[2] = u; }
{ unsigned u = pk_bf16(T[22], T[23]); PIN(u); hh[3] = u; }
{ float v = T[24] * Y[1][4]; PIN(v); T[24] = v; }
SB(); M32A(X[0], AF(2), ob[27]); SB();
afl[1] = TR(lbW1, 57344);
afh[1] = TR(lbW1, 57856);
GLDS(lvoffB, lbase, 1024);
*(AS3 u32x4*)(uintptr_t)(hw_ad + 51200) = hh;
SB(); M32A(X[1], AF(3), ob[27]); SB();
afl[2] = TR(lbW0, 59392);
afh[2] = TR(lbW0, 59904);
lbase = wdb + 516736;
SB();
asm volatile("s_waitcnt vmcnt(14)\n\ts_barrier" : "+a"(ob[28]), "+a"(ob[29]), "+a"(ob[30]), "+a"(ob[31]) :: "memory");
SB(); M32A(X[0], AF(0), ob[28]); SB();
afl[3] = TR(lbW1, 59392);
afh[3] = TR(lbW1, 59904);
ST41(hxvo, hh, hxw + 51200);
SB(); M32A(X[1], AF(1), ob[28]); SB();
afl[0] = TR(lbW0, 61440);
afh[0] = TR(lbW0, 61952);
{ float v = T[25] * Y[1][5]; PIN(v); T[25] = v; }
{ float v = T[26] * Y[1][6]; PIN(v); T[26] = v; }
{ float v = T[27] * Y[1][7]; PIN(v); T[27] = v; }
SB(); M32A(X[0], AF(2), ob[29]); SB();
afl[1] = TR(lbW1, 61440);
afh[1] = TR(lbW1, 61952);
SETM0(wv2k + 49152);
{ unsigned u = pk_bf16(T[24], T[25]); PIN(u); hh[0] = u; }
{ unsigned u = pk_bf16(T[26], T[27]); PIN(u); hh[1] = u; }
SB(); M32A(X[1], AF(3), ob[29]); SB();
afl[2] = TR(lbW0, 63488);
afh[2] = TR(lbW0, 64000);
GLDS(lvoffA, lbase, 0);
{ float v = T[28] * Y[1][12]; PIN(v); T[28] = v; }
{ float v = T[29] * Y[1][13]; PIN(v); T[29] = v; }
SB(); M32A(X[0], AF(0), ob[30]); SB();
afl[3] = TR(lbW1, 63488);
afh[3] = TR(lbW1, 64000);
{ unsigned u = pk_bf16(T[28], T[29]); PIN(u); hh[2] = u; }
{ float v = T[30] * Y[1][14]; PIN(v); T[30] = v; }
{ float v = T[31] * Y[1][15]; PIN(v); T[31] = v; }
SB(); M32A(X[1], AF(1), ob[30]); SB();
afl[0] = TR(lbW0, 65536);
afh[0] = TR(lbW0, 66048);
{ unsigned u = pk_bf16(T[30], T[31]); PIN(u); hh[3] = u; }
SB(); M32A(X[0], AF(2), ob[31]); SB();
afl[1] = TR(lbW1, 65536);
afh[1] = TR(lbW1, 66048);
GLDS(lvoffB, lbase, 1024);
*(AS3 u32x4*)(uintptr_t)(hw_ad + 51712) = hh;
SB(); M32A(X[1], AF(3), ob[31]); SB();
afl[2] = TR(lbW0, 67584);
afh[2] = TR(lbW0, 68096);
lbase = wdb + 590464;
SB();
asm volatile("s_waitcnt vmcnt(15)\n\ts_barrier" : "+a"(ob[32]), "+a"(ob[33]), "+a"(ob[34]), "+a"(ob[35]) :: "memory");
SB(); M32A(X[0], AF(0), ob[32]); SB();
afl[3] = TR(lbW1, 67584);
afh[3] = TR(lbW1, 68096);
ST41(hxvo, hh, hxw + 51712);
SB(); M32A(X[1], AF(1), ob[32]); SB();
afl[0] = TR(lbW0, 69632);
afh[0] = TR(lbW0, 70144);
SB(); M32A(X[0], AF(2), ob[33]); SB();
afl[1] = TR(lbW1, 69632);
afh[1] = TR(lbW1, 70144);
SETM0(wv2k + 57344);
SB(); M32A(X[1], AF(3), ob[33]); SB();
afl[2] = TR(lbW0, 71680);
afh[2] = TR(lbW0, 72192);
GLDS(lvoffA, lbase, 0);
SB(); M32A(X[0], AF(0), ob[34]); SB();
afl[3] = TR(lbW1, 71680);
afh[3] = TR(lbW1, 72192);
SB(); M32A(X[1], AF(1), ob[34]); SB();
afl[0] = TR(lbB0, 0);
afh[0] = TR(lbB0, 2048);
SB(); M32A(X[0], AF(2), ob[35]); SB();
afl[1] = TR(lbB1, 0);
afh[1] = TR(lbB1, 2048);
GLDS(lvoffB, lbase, 1024);
SB(); M32A(X[1], AF(3), ob[35]); SB();
afl[2] = TR(lbB0, 4096);
afh[2] = TR(lbB0, 6144);
lbase = wdb + 512;
}
MNOP();
{ unsigned u = pk_bf16(X[0][0], X[0][1]); PIN(u); hh[0] = u; }
{ unsigned u = pk_bf16(X[0][2], X[0][3]); PIN(u); hh[1] = u; }
{ unsigned u = pk_bf16(X[0][8], X[0][9]); PIN(u); hh[2] = u; }
{ unsigned u = pk_bf16(X[0][10], X[0][11]); PIN(u); hh[3] = u; }
ST41(svo, hh, sbase0 + (wp0) * 18432 + 0);
{ unsigned u = pk_bf16(X[0][4], X[0][5]); PIN(u); hh[0] = u; }
{ unsigned u = pk_bf16(X[0][6], X[0][7]); PIN(u); hh[1] = u; }
{ unsigned u = pk_bf16(X[0][12], X[0][13]); PIN(u); hh[2] = u; }
{ unsigned u = pk_bf16(X[0][14], X[0][15]); PIN(u); hh[3] = u; }
ST41(svo, hh, sbase0 + (wp0) * 18432 + 512);
{ unsigned u = pk_bf16(X[1][0], X[1][1]); PIN(u); hh[0] = u; }
{ unsigned u = pk_bf16(X[1][2], X[1][3]); PIN(u); hh[1] = u; }
{ unsigned u = pk_bf16(X[1][8], X[1][9]); PIN(u); hh[2] = u; }
{ unsigned u = pk_bf16(X[1][10], X[1][11]); PIN(u); hh[3] = u; }
ST41(svo, hh, sbase0 + (wp0) * 18432 + 9216);
{ unsigned u = pk_bf16(X[1][4], X[1][5]); PIN(u); hh[0] = u; }
{ unsigned u = pk_bf16(X[1][6], X[1][7]); PIN(u); hh[1] = u; }
{ unsigned u = pk_bf16(X[1][12], X[1][13]); PIN(u); hh[2] = u; }
{ unsigned u = pk_bf16(X[1][14], X[1][15]); PIN(u); hh[3] = u; }
ST41(svo, hh, sbase0 + (wp0) * 18432 + 9728);
} else {
{
SB();
asm volatile("s_waitcnt vmcnt(17)\n\ts_barrier" ::: "memory");
SB(); M32ZA(Y[0], AF(0), xb[0][0]); SB();
afl[3] = TR(lbA, 6144);
T[0] = X[0][0];
T[1] = X[0][1];
T[2] = X[0][2];
T[3] = X[0][3];
T[4] = X[0][8];
T[5] = X[0][9];
T[6] = X[0][10];
T[7] = X[0][11];
T[8] = X[0][4];
T[9] = X[0][5];
T[10] = X[0][6];
T[11] = X[0][7];
T[12] = X[0][12];
T[13] = X[0][13];
T[14] = X[0][14];
T[15] = X[0][15];
T[16] = X[1][0];
T[17] = X[1][1];
T[18] = X[1][2];
T[19] = X[1][3];
T[20] = X[1][8];
T[21] = X[1][9];
T[22] = X[1][10];
T[23] = X[1][11];
T[24] = X[1][4];
T[25] = X[1][5];
T[26] = X[1][6];
T[27] = X[1][7];
T[28] = X[1][12];
T[29] = X[1][13];
T[30] = X[1][14];
T[31] = X[1][15];
SB(); M32ZA(Y[1], AF(0), xb[1][0]); SB();
afh[3] = TR(lbA, 6656);
SB(); M32A(Y[0], AF(1), xb[0][1]); SB();
afl[0] = TR(lbA, 8192);
SETM0(wv2k + 65536);
SB(); M32A(Y[1], AF(1), xb[1][1]); SB();
afh[0] = TR(lbA, 8704);
GLDS(lvoffA, lbase, 0);
SB(); M32A(Y[0], AF(2), xb[0][2]); SB();
afl[1] = TR(lbA, 10240);
SB(); M32A(Y[1], AF(2), xb[1][2]); SB();
afh[1] = TR(lbA, 10752);
SB(); M32A(Y[0], AF(3), xb[0][3]); SB();
afl[2] = TR(lbA, 12288);
GLDS(lvoffB, lbase, 1024);
gt0 = T[0] * T[0]; PIN(gt0);
SB(); M32A(Y[1], AF(3), xb[1][3]); SB();
afh[2] = TR(lbA, 12800);
lbase = wdb + 369152; lvoffA = rowA * 1152 + c16P2; lvoffB = (rowA + 8) * 1152 + c16P2 - 1024;
gt0 = __builtin_fmaf(gt0, C1, C0); PIN(gt0);
gt1 = T[1] * T[1]; PIN(gt1);
SB();
asm volatile("s_waitcnt vmcnt(17)\n\ts_barrier" ::: "memory");
SB(); M32A(Y[0], AF(0), xb[0][4]); SB();
afl[3] = TR(lbA, 14336);
gt0 = T[0] * gt0; PIN(gt0);
gt1 = __builtin_fmaf(gt1, C1, C0); PIN(gt1);
SB(); M32A(Y[1], AF(0), xb[1][4]); SB();
afh[3] = TR(lbA, 14848);
gt0 = __builtin_amdgcn_exp2f(gt0); PIN(gt0);
gt1 = T[1] * gt1; PIN(gt1);
gt2 = T[2] * T[2]; PIN(gt2);
SB(); M32A(Y[0], AF(1), xb[0][5]); SB();
afl[0] = TR(lbA, 16384);
SETM0(wv2k + 0);
gt0 = gt0 + 1.0f; PIN(gt0);
gt1 = __builtin_amdgcn_exp2f(gt1); PIN(gt1);
gt2 = __builtin_fmaf(gt2, C1, C0); PIN(gt2);
SB(); M32A(Y[1], AF(1), xb[1][5]); SB();
afh[0] = TR(lbA, 16896);
GLDS(lvoffA, lbase, 0);
gt0 = __builtin_amdgcn_rcpf(gt0); PIN(gt0);
gt1 = gt1 + 1.0f; PIN(gt1);
gt2 = T[2] * gt2; PIN(gt2);
SB(); M32A(Y[0], AF(2), xb[0][6]); SB();
afl[1] = TR(lbA, 18432);
{ float v = T[0] * gt0; PIN(v); T[0] = v; }
gt1 = __builtin_amdgcn_rcpf(gt1); PIN(gt1);
gt2 = __builtin_amdgcn_exp2f(gt2); PIN(gt2);
SB(); M32A(Y[1], AF(2), xb[1][6]); SB();
afh[1] = TR(lbA, 18944);
{ float v = T[1] * gt1; PIN(v); T[1] = v; }
gt2 = gt2 + 1.0f; PIN(gt2);
gt0 = T[3] * T[3]; PIN(gt0);
SB(); M32A(Y[0], AF(3), xb[0][7]); SB();
afl[2] = TR(lbA, 20480);
GLDS(lvoffB, lbase, 1024);
gt2 = __builtin_amdgcn_rcpf(gt2); PIN(gt2);
gt0 = __builtin_fmaf(gt0, C1, C0); PIN(gt0);
gt1 = T[4] * T[4]; PIN(gt1);
SB(); M32A(Y[1], AF(3), xb[1][7]); SB();
afh[2] = TR(lbA, 20992);
lbase = wdb + 442880;
{ float v = T[2] * gt2; PIN(v); T[2] = v; }
gt0 = T[3] * gt0; PIN(gt0);
gt1 = __builtin_fmaf(gt1, C1, C0); PIN(gt1);
SB();
asm volatile("s_waitcnt vmcnt(17)\n\ts_barrier" ::: "memory");
SB(); M32A(Y[0], AF(0), xb[0][8]); SB();
afl[3] = TR(lbA, 22528);
gt0 = __builtin_amdgcn_exp2f(gt0); PIN(gt0);
gt1 = T[4] * gt1; PIN(gt1);
gt2 = T[5] * T[5]; PIN(gt2);
SB(); M32A(Y[1], AF(0), xb[1][8]); SB();
afh[3] = TR(lbA, 23040);
gt0 = gt0 + 1.0f; PIN(gt0);
gt1 = __builtin_amdgcn_exp2f(gt1); PIN(gt1);
gt2 = __builtin_fmaf(gt2, C1, C0); PIN(gt2);
SB(); M32A(Y[0], AF(1), xb[0][9]); SB();
afl[0] = TR(lbA, 24576);
SETM0(wv2k + 8192);
gt0 = __builtin_amdgcn_rcpf(gt0); PIN(gt0);
gt1 = gt1 + 1.0f; PIN(gt1);
gt2 = T[5] * gt2; PIN(gt2);
SB(); M32A(Y[1], AF(1), xb[1][9]); SB();
afh[0] = TR(lbA, 25088);
GLDS(lvoffA, lbase, 0);
{ float v = T[3] * gt0; PIN(v); T[3] = v; }
gt1 = __builtin_amdgcn_rcpf(gt1); PIN(gt1);
SB(); M32A(Y[0], AF(2), xb[0][10]); SB();
afl[1] = TR(lbA, 26624);
{ float v = T[4] * gt1; PIN(v); T[4] = v; }
gt2 = __builtin_amdgcn_exp2f(gt2); PIN(gt2);
gt0 = T[6] * T[6]; PIN(gt0);
SB(); M32A(Y[1], AF(2), xb[1][10]); SB();
afh[1] = TR(lbA, 27136);
gt2 = gt2 + 1.0f; PIN(gt2);
gt0 = __builtin_fmaf(gt0, C1, C0); PIN(gt0);
gt1 = T[7] * T[7]; PIN(gt1);
SB(); M32A(Y[0], AF(3), xb[0][11]); SB();
afl[2] = TR(lbA, 28672);
GLDS(lvoffB, lbase, 1024);
gt2 = __builtin_amdgcn_rcpf(gt2); PIN(gt2);
gt0 = T[6] * gt0; PIN(gt0);
gt1 = __builtin_fmaf(gt1, C1, C0); PIN(gt1);
SB(); M32A(Y[1], AF(3), xb[1][11]); SB();
afh[2] = TR(lbA, 29184);
lbase = wdb + 516608;
{ float v = T[5] * gt2; PIN(v); T[5] = v; }
gt0 = __builtin_amdgcn_exp2f(gt0); PIN(gt0);
SB();
asm volatile("s_waitcnt vmcnt(7)\n\ts_barrier" ::: "memory");
SIGNAL_SET(fHme, 3, true);
SB(); M32A(Y[0], AF(0), xb[0][12]); SB();
afl[3] = TR(lbA, 30720);
gt0 = gt0 + 1.0f; PIN(gt0);
gt1 = T[7] * gt1; PIN(gt1);
gt2 = T[8] * T[8]; PIN(gt2);
SB(); M32A(Y[1], AF(0), xb[1][12]); SB();
afh[3] = TR(lbA, 31232);
gt0 = __builtin_amdgcn_rcpf(gt0); PIN(gt0);
gt1 = __builtin_amdgcn_exp2f(gt1); PIN(gt1);
gt2 = __builtin_fmaf(gt2, C1, C0); PIN(gt2);
SB(); M32A(Y[0], AF(1), xb[0][13]); SB();
afl[0] = TR(lbA, 32768);
SETM0(wv2k + 16384);
{ float v = T[6] * gt0; PIN(v); T[6] = v; }
gt1 = gt1 + 1.0f; PIN(gt1);
gt2 = T[8] * gt2; PIN(gt2);
SB(); M32A(Y[1], AF(1), xb[1][13]); SB();
afh[0] = TR(lbA, 33280);
GLDS(lvoffA, lbase, 0);
gt1 = __builtin_amdgcn_rcpf(gt1); PIN(gt1);
gt2 = __builtin_amdgcn_exp2f(gt2); PIN(gt2);
SB(); M32A(Y[0], AF(2), xb[0][14]); SB();
afl[1] = TR(lbA, 34816);
{ float v = T[7] * gt1; PIN(v); T[7] = v; }
gt2 = gt2 + 1.0f; PIN(gt2);
gt0 = T[9] * T[9]; PIN(gt0);
SB(); M32A(Y[1], AF(2), xb[1][14]); SB();
afh[1] = TR(lbA, 35328);
gt2 = __builtin_amdgcn_rcpf(gt2); PIN(gt2);
gt0 = __builtin_fmaf(gt0, C1, C0); PIN(gt0);
gt1 = T[10] * T[10]; PIN(gt1);
SB(); M32A(Y[0], AF(3), xb[0][15]); SB();
afl[2] = TR(lbA, 36864);
GLDS(lvoffB, lbase, 1024);
{ float v = T[8] * gt2; PIN(v); T[8] = v; }
gt0 = T[9] * gt0; PIN(gt0);
gt1 = __builtin_fmaf(gt1, C1, C0); PIN(gt1);
SB(); M32A(Y[1], AF(3), xb[1][15]); SB();
afh[2] = TR(lbA, 37376);
lbase = wdb + 590336;
gt0 = __builtin_amdgcn_exp2f(gt0); PIN(gt0);
gt1 = T[10] * gt1; PIN(gt1);
SB();
asm volatile("s_waitcnt vmcnt(15)\n\ts_barrier" ::: "memory");
SB(); M32A(Y[0], AF(0), xb[0][16]); SB();
afl[3] = TR(lbA, 38912);
gt0 = gt0 + 1.0f; PIN(gt0);
gt1 = __builtin_amdgcn_exp2f(gt1); PIN(gt1);
gt2 = T[11] * T[11]; PIN(gt2);
SB(); M32A(Y[1], AF(0), xb[1][16]); SB();
afh[3] = TR(lbA, 39424);
gt0 = __builtin_amdgcn_rcpf(gt0); PIN(gt0);
gt1 = gt1 + 1.0f; PIN(gt1);
gt2 = __builtin_fmaf(gt2, C1, C0); PIN(gt2);
SB(); M32A(Y[0], AF(1), xb[0][17]); SB();
afl[0] = TR(lbA, 40960);
SETM0(wv2k + 24576);
{ float v = T[9] * gt0; PIN(v); T[9] = v; }
gt1 = __builtin_amdgcn_rcpf(gt1); PIN(gt1);
gt2 = T[11] * gt2; PIN(gt2);
SB(); M32A(Y[1], AF(1), xb[1][17]); SB();
afh[0] = TR(lbA, 41472);
GLDS(lvoffA, lbase, 0);
{ float v = T[10] * gt1; PIN(v); T[10] = v; }
gt2 = __builtin_amdgcn_exp2f(gt2); PIN(gt2);
gt0 = T[12] * T[12]; PIN(gt0);
SB(); M32A(Y[0], AF(2), xb[0][18]); SB();
afl[1] = TR(lbA, 43008);
gt2 = gt2 + 1.0f; PIN(gt2);
gt0 = __builtin_fmaf(gt0, C1, C0); PIN(gt0);
gt1 = T[13] * T[13]; PIN(gt1);
SB(); M32A(Y[1], AF(2), xb[1][18]); SB();
afh[1] = TR(lbA, 43520);
gt2 = __builtin_amdgcn_rcpf(gt2); PIN(gt2);
gt0 = T[12] * gt0; PIN(gt0);
gt1 = __builtin_fmaf(gt1, C1, C0); PIN(gt1);
SB(); M32A(Y[0], AF(3), xb[0][19]); SB();
afl[2] = TR(lbA, 45056);
GLDS(lvoffB, lbase, 1024);
{ float v = T[11] * gt2; PIN(v); T[11] = v; }
gt0 = __builtin_amdgcn_exp2f(gt0); PIN(gt0);
gt1 = T[13] * gt1; PIN(gt1);
SB(); M32A(Y[1], AF(3), xb[1][19]); SB();
afh[2] = TR(lbA, 45568);
lbase = wdb + 369280;
gt0 = gt0 + 1.0f; PIN(gt0);
gt1 = __builtin_amdgcn_exp2f(gt1); PIN(gt1);
SB();
asm volatile("s_waitcnt vmcnt(14)\n\ts_barrier" ::: "memory");
SB(); M32A(Y[0], AF(0), xb[0][20]); SB();
afl[3] = TR(lbA, 47104);
gt0 = __builtin_amdgcn_rcpf(gt0); PIN(gt0);
gt1 = gt1 + 1.0f; PIN(gt1);
gt2 = T[14] * T[14]; PIN(gt2);
SB(); M32A(Y[1], AF(0), xb[1][20]); SB();
afh[3] = TR(lbA, 47616);
{ float v = T[12] * gt0; PIN(v); T[12] = v; }
gt1 = __builtin_amdgcn_rcpf(gt1); PIN(gt1);
gt2 = __builtin_fmaf(gt2, C1, C0); PIN(gt2);
SB(); M32A(Y[0], AF(1), xb[0][21]); SB();
afl[0] = TR(lbA, 49152);
SETM0(wv2k + 32768);
{ float v = T[13] * gt1; PIN(v); T[13] = v; }
gt2 = T[14] * gt2; PIN(gt2);
gt0 = T[15] * T[15]; PIN(gt0);
SB(); M32A(Y[1], AF(1), xb[1][21]); SB();
afh[0] = TR(lbA, 49664);
GLDS(lvoffA, lbase, 0);
gt2 = __builtin_amdgcn_exp2f(gt2); PIN(gt2);
gt0 = __builtin_fmaf(gt0, C1, C0); PIN(gt0);
gt1 = T[16] * T[16]; PIN(gt1);
SB(); M32A(Y[0], AF(2), xb[0][22]); SB();
afl[1] = TR(lbA, 51200);
gt2 = gt2 + 1.0f; PIN(gt2);
gt0 = T[15] * gt0; PIN(gt0);
gt1 = __builtin_fmaf(gt1, C1, C0); PIN(gt1);
SB(); M32A(Y[1], AF(2), xb[1][22]); SB();
afh[1] = TR(lbA, 51712);
gt2 = __builtin_amdgcn_rcpf(gt2); PIN(gt2);
gt0 = __builtin_amdgcn_exp2f(gt0); PIN(gt0);
gt1 = T[16] * gt1; PIN(gt1);
SB(); M32A(Y[0], AF(3), xb[0][23]); SB();
afl[2] = TR(lbA, 53248);
GLDS(lvoffB, lbase, 1024);
{ float v = T[14] * gt2; PIN(v); T[14] = v; }
gt0 = gt0 + 1.0f; PIN(gt0);
gt1 = __builtin_amdgcn_exp2f(gt1); PIN(gt1);
SB(); M32A(Y[1], AF(3), xb[1][23]); SB();
afh[2] = TR(lbA, 53760);
lbase = wdb + 443008;
gt0 = __builtin_amdgcn_rcpf(gt0); PIN(gt0);
gt1 = gt1 + 1.0f; PIN(gt1);
SB();
asm volatile("s_waitcnt vmcnt(12)\n\ts_barrier" ::: "memory");
SB(); M32A(Y[0], AF(0), xb[0][24]); SB();
afl[3] = TR(lbA, 55296);
{ float v = T[15] * gt0; PIN(v); T[15] = v; }
gt1 = __builtin_amdgcn_rcpf(gt1); PIN(gt1);
gt2 = T[17] * T[17]; PIN(gt2);
SB(); M32A(Y[1], AF(0), xb[1][24]); SB();
afh[3] = TR(lbA, 55808);
{ float v = T[16] * gt1; PIN(v); T[16] = v; }
gt2 = __builtin_fmaf(gt2, C1, C0); PIN(gt2);
gt0 = T[18] * T[18]; PIN(gt0);
SB(); M32A(Y[0], AF(1), xb[0][25]); SB();
afl[0] = TR(lbA, 57344);
SETM0(wv2k + 40960);
gt2 = T[17] * gt2; PIN(gt2);
gt0 = __builtin_fmaf(gt0, C1, C0); PIN(gt0);
gt1 = T[19] * T[19]; PIN(gt1);
SB(); M32A(Y[1], AF(1), xb[1][25]); SB();
afh[0] = TR(lbA, 57856);
GLDS(lvoffA, lbase, 0);
gt2 = __builtin_amdgcn_exp2f(gt2); PIN(gt2);
gt0 = T[18] * gt0; PIN(gt0);
gt1 = __builtin_fmaf(gt1, C1, C0); PIN(gt1);
SB(); M32A(Y[0], AF(2), xb[0][26]); SB();
afl[1] = TR(lbA, 59392);
gt2 = gt2 + 1.0f; PIN(gt2);
gt0 = __builtin_amdgcn_exp2f(gt0); PIN(gt0);
gt1 = T[19] * gt1; PIN(gt1);
SB(); M32A(Y[1], AF(2), xb[1][26]); SB();
afh[1] = TR(lbA, 59904);
gt2 = __builtin_amdgcn_rcpf(gt2); PIN(gt2);
gt0 = gt0 + 1.0f; PIN(gt0);
gt1 = __builtin_amdgcn_exp2f(gt1); PIN(gt1);
SB(); M32A(Y[0], AF(3), xb[0][27]); SB();
afl[2] = TR(lbA, 61440);
GLDS(lvoffB, lbase, 1024);
{ float v = T[17] * gt2; PIN(v); T[17] = v; }
gt0 = __builtin_amdgcn_rcpf(gt0); PIN(gt0);
gt1 = gt1 + 1.0f; PIN(gt1);
SB(); M32A(Y[1], AF(3), xb[1][27]); SB();
afh[2] = TR(lbA, 61952);
lbase = wdb + 516736;
{ float v = T[18] * gt0; PIN(v); T[18] = v; }
gt1 = __builtin_amdgcn_rcpf(gt1); PIN(gt1);
SB();
asm volatile("s_waitcnt vmcnt(12)\n\ts_barrier" ::: "memory");
SB(); M32A(Y[0], AF(0), xb[0][28]); SB();
afl[3] = TR(lbA, 63488);
{ float v = T[19] * gt1; PIN(v); T[19] = v; }
gt2 = T[20] * T[20]; PIN(gt2);
gt0 = T[21] * T[21]; PIN(gt0);
SB(); M32A(Y[1], AF(0), xb[1][28]); SB();
afh[3] = TR(lbA, 64000);
gt2 = __builtin_fmaf(gt2, C1, C0); PIN(gt2);
gt0 = __builtin_fmaf(gt0, C1, C0); PIN(gt0);
gt1 = T[22] * T[22]; PIN(gt1);
SB(); M32A(Y[0], AF(1), xb[0][29]); SB();
afl[0] = TR(lbA, 65536);
SETM0(wv2k + 49152);
gt2 = T[20] * gt2; PIN(gt2);
gt0 = T[21] * gt0; PIN(gt0);
gt1 = __builtin_fmaf(gt1, C1, C0); PIN(gt1);
SB(); M32A(Y[1], AF(1), xb[1][29]); SB();
afh[0] = TR(lbA, 66048);
GLDS(lvoffA, lbase, 0);
gt2 = __builtin_amdgcn_exp2f(gt2); PIN(gt2);
gt0 = __builtin_amdgcn_exp2f(gt0); PIN(gt0);
SB(); M32V(Y[0], AF(2), xb[0][30]); SB();
afl[1] = TR(lbA, 67584);
gt2 = gt2 + 1.0f; PIN(gt2);
gt0 = gt0 + 1.0f; PIN(gt0);
gt1 = T[22] * gt1; PIN(gt1);
SB(); M32V(Y[1], AF(2), xb[1][30]); SB();
afh[1] = TR(lbA, 68096);
gt2 = __builtin_amdgcn_rcpf(gt2); PIN(gt2);
gt0 = __builtin_amdgcn_rcpf(gt0); PIN(gt0);
gt1 = __builtin_amdgcn_exp2f(gt1); PIN(gt1);
SB(); M32V(Y[0], AF(3), xb[0][31]); SB();
afl[2] = TR(lbA, 69632);
GLDS(lvoffB, lbase, 1024);
{ float v = T[20] * gt2; PIN(v); T[20] = v; }
{ float v = T[21] * gt0; PIN(v); T[21] = v; }
gt1 = gt1 + 1.0f; PIN(gt1);
SB(); M32V(Y[1], AF(3), xb[1][31]); SB();
afh[2] = TR(lbA, 70144);
lbase = wdb + 590464;
gt1 = __builtin_amdgcn_rcpf(gt1); PIN(gt1);
gt2 = T[23] * T[23]; PIN(gt2);
SB();
asm volatile("s_waitcnt vmcnt(12)\n\ts_barrier" ::: "memory");
SB(); M32V(Y[0], AF(0), xb[0][32]); SB();
afl[3] = TR(lbA, 71680);
{ float v = T[22] * gt1; PIN(v); T[22] = v; }
gt2 = __builtin_fmaf(gt2, C1, C0); PIN(gt2);
gt0 = T[24] * T[24]; PIN(gt0);
SB(); M32V(Y[1], AF(0), xb[1][32]); SB();
afh[3] = TR(lbA, 72192);
gt2 = T[23] * gt2; PIN(gt2);
gt0 = __builtin_fmaf(gt0, C1, C0); PIN(gt0);
gt1 = T[25] * T[25]; PIN(gt1);
SB(); M32V(Y[0], AF(1), xb[0][33]); SB();
afl[0] = TR(lbB0, 0);
SETM0(wv2k + 57344);
gt2 = __builtin_amdgcn_exp2f(gt2); PIN(gt2);
gt0 = T[24] * gt0; PIN(gt0);
gt1 = __builtin_fmaf(gt1, C1, C0); PIN(gt1);
SB(); M32V(Y[1], AF(1), xb[1][33]); SB();
afh[0] = TR(lbB0, 2048);
GLDS(lvoffA, lbase, 0);
gt2 = gt2 + 1.0f; PIN(gt2);
gt0 = __builtin_amdgcn_exp2f(gt0); PIN(gt0);
gt1 = T[25] * gt1; PIN(gt1);
SB(); M32V(Y[0], AF(2), xb[0][34]); SB();
afl[1] = TR(lbB1, 0);
gt2 = __builtin_amdgcn_rcpf(gt2); PIN(gt2);
gt0 = gt0 + 1.0f; PIN(gt0);
gt1 = __builtin_amdgcn_exp2f(gt1); PIN(gt1);
SB(); M32V(Y[1], AF(2), xb[1][34]); SB();
afh[1] = TR(lbB1, 2048);
{ float v = T[23] * gt2; PIN(v); T[23] = v; }
gt0 = __builtin_amdgcn_rcpf(gt0); PIN(gt0);
gt1 = gt1 + 1.0f; PIN(gt1);
SB(); M32V(Y[0], AF(3), xb[0][35]); SB();
afl[2] = TR(lbB0, 4096);
GLDS(lvoffB, lbase, 1024);
{ float v = T[24] * gt0; PIN(v); T[24] = v; }
gt1 = __builtin_amdgcn_rcpf(gt1); PIN(gt1);
gt2 = T[26] * T[26]; PIN(gt2);
SB(); M32V(Y[1], AF(3), xb[1][35]); SB();
afh[2] = TR(lbB0, 6144);
lbase = wdb + 512;
{ float v = T[25] * gt1; PIN(v); T[25] = v; }
gt2 = __builtin_fmaf(gt2, C1, C0); PIN(gt2);
gt0 = T[27] * T[27]; PIN(gt0);
}
STAMP(7);
MNOP();
gt2 = T[26] * gt2; PIN(gt2);
gt2 = __builtin_amdgcn_exp2f(gt2); PIN(gt2);
gt2 = gt2 + 1.0f; PIN(gt2);
gt2 = __builtin_amdgcn_rcpf(gt2); PIN(gt2);
{ float v = T[26] * gt2; PIN(v); T[26] = v; }
gt0 = __builtin_fmaf(gt0, C1, C0); PIN(gt0);
gt0 = T[27] * gt0; PIN(gt0);
gt0 = __builtin_amdgcn_exp2f(gt0); PIN(gt0);
gt0 = gt0 + 1.0f; PIN(gt0);
gt0 = __builtin_amdgcn_rcpf(gt0); PIN(gt0);
{ float v = T[27] * gt0; PIN(v); T[27] = v; }
gt1 = T[28] * T[28]; PIN(gt1);
gt1 = __builtin_fmaf(gt1, C1, C0); PIN(gt1);
gt1 = T[28] * gt1; PIN(gt1);
gt1 = __builtin_amdgcn_exp2f(gt1); PIN(gt1);
gt1 = gt1 + 1.0f; PIN(gt1);
gt1 = __builtin_amdgcn_rcpf(gt1); PIN(gt1);
{ float v = T[28] * gt1; PIN(v); T[28] = v; }
gt2 = T[29] * T[29]; PIN(gt2);
gt2 = __builtin_fmaf(gt2, C1, C0); PIN(gt2);
gt2 = T[29] * gt2; PIN(gt2);
gt2 = __builtin_amdgcn_exp2f(gt2); PIN(gt2);
gt2 = gt2 + 1.0f; PIN(gt2);
gt2 = __builtin_amdgcn_rcpf(gt2); PIN(gt2);
{ float v = T[29] * gt2; PIN(v); T[29] = v; }
gt0 = T[30] * T[30]; PIN(gt0);
gt0 = __builtin_fmaf(gt0, C1, C0); PIN(gt0);
gt0 = T[30] * gt0; PIN(gt0);
gt0 = __builtin_amdgcn_exp2f(gt0); PIN(gt0);
gt0 = gt0 + 1.0f; PIN(gt0);
gt0 = __builtin_amdgcn_rcpf(gt0); PIN(gt0);
{ float v = T[30] * gt0; PIN(v); T[30] = v; }
gt1 = T[31] * T[31]; PIN(gt1);
gt1 = __builtin_fmaf(gt1, C1, C0); PIN(gt1);
gt1 = T[31] * gt1; PIN(gt1);
gt1 = __builtin_amdgcn_exp2f(gt1); PIN(gt1);
gt1 = gt1 + 1.0f; PIN(gt1);
gt1 = __builtin_amdgcn_rcpf(gt1); PIN(gt1);
{ float v = T[31] * gt1; PIN(v); T[31] = v; }
{ float v = T[0] * Y[0][0]; PIN(v); T[0] = v; }
{ float v = T[1] * Y[0][1]; PIN(v); T[1] = v; }
{ unsigned u = pk_bf16(T[0], T[1]); PIN(u); hh[0] = u; }
{ float v = T[2] * Y[0][2]; PIN(v); T[2] = v; }
{ float v = T[3] * Y[0][3]; PIN(v); T[3] = v; }
{ unsigned u = pk_bf16(T[2], T[3]); PIN(u); hh[1] = u; }
{ float v = T[4] * Y[0][8]; PIN(v); T[4] = v; }
{ float v = T[5] * Y[0][9]; PIN(v); T[5] = v; }
{ unsigned u = pk_bf16(T[4], T[5]); PIN(u); hh[2] = u; }
{ float v = T[6] * Y[0][10]; PIN(v); T[6] = v; }
{ float v = T[7] * Y[0][11]; PIN(v); T[7] = v; }
{ unsigned u = pk_bf16(T[6], T[7]); PIN(u); hh[3] = u; }
*(AS3 u32x4*)(uintptr_t)(hw_ad + 49152) = hh;
ST41(hxvo, hh, hxw + 49152);
{ float v = T[8] * Y[0][4]; PIN(v); T[8] = v; }
{ float v = T[9] * Y[0][5]; PIN(v); T[9] = v; }
{ unsigned u = pk_bf16(T[8], T[9]); PIN(u); hh[0] = u; }
{ float v = T[10] * Y[0][6]; PIN(v); T[10] = v; }
{ float v = T[11] * Y[0][7]; PIN(v); T[11] = v; }
{ unsigned u = pk_bf16(T[10], T[11]); PIN(u); hh[1] = u; }
{ float v = T[12] * Y[0][12]; PIN(v); T[12] = v; }
{ float v = T[13] * Y[0][13]; PIN(v); T[13] = v; }
{ unsigned u = pk_bf16(T[12], T[13]); PIN(u); hh[2] = u; }
{ float v = T[14] * Y[0][14]; PIN(v); T[14] = v; }
{ float v = T[15] * Y[0][15]; PIN(v); T[15] = v; }
{ unsigned u = pk_bf16(T[14], T[15]); PIN(u); hh[3] = u; }
*(AS3 u32x4*)(uintptr_t)(hw_ad + 49664) = hh;
ST41(hxvo, hh, hxw + 49664);
{ float v = T[16] * Y[1][0]; PIN(v); T[16] = v; }
{ float v = T[17] * Y[1][1]; PIN(v); T[17] = v; }
{ unsigned u = pk_bf16(T[16], T[17]); PIN(u); hh[0] = u; }
{ float v = T[18] * Y[1][2]; PIN(v); T[18] = v; }
{ float v = T[19] * Y[1][3]; PIN(v); T[19] = v; }
{ unsigned u = pk_bf16(T[18], T[19]); PIN(u); hh[1] = u; }
{ float v = T[20] * Y[1][8]; PIN(v); T[20] = v; }
{ float v = T[21] * Y[1][9]; PIN(v); T[21] = v; }
{ unsigned u = pk_bf16(T[20], T[21]); PIN(u); hh[2] = u; }
{ float v = T[22] * Y[1][10]; PIN(v); T[22] = v; }
{ float v = T[23] * Y[1][11]; PIN(v); T[23] = v; }
{ unsigned u = pk_bf16(T[22], T[23]); PIN(u); hh[3] = u; }
*(AS3 u32x4*)(uintptr_t)(hw_ad + 51200) = hh;
ST41(hxvo, hh, hxw + 51200);
{ float v = T[24] * Y[1][4]; PIN(v); T[24] = v; }
{ float v = T[25] * Y[1][5]; PIN(v); T[25] = v; }
{ unsigned u = pk_bf16(T[24], T[25]); PIN(u); hh[0] = u; }
{ float v = T[26] * Y[1][6]; PIN(v); T[26] = v; }
{ float v = T[27] * Y[1][7]; PIN(v); T[27] = v; }
{ unsigned u = pk_bf16(T[26], T[27]); PIN(u); hh[1] = u; }
{ float v = T[28] * Y[1][12]; PIN(v); T[28] = v; }
{ float v = T[29] * Y[1][13]; PIN(v); T[29] = v; }
{ unsigned u = pk_bf16(T[28], T[29]); PIN(u); hh[2] = u; }
{ float v = T[30] * Y[1][14]; PIN(v); T[30] = v; }
{ float v = T[31] * Y[1][15]; PIN(v); T[31] = v; }
{ unsigned u = pk_bf16(T[30], T[31]); PIN(u); hh[3] = u; }
*(AS3 u32x4*)(uintptr_t)(hw_ad + 51712) = hh;
ST41(hxvo, hh, hxw + 51712);
}
asm volatile("s_waitcnt lgkmcnt(0)\n\ts_barrier" ::: "memory");
STAMP(8); JIT(8);
u32x4 hb[4][18];
f32x4 P[2][4], R[2][4], Q[2][4];
hb[0][0] = *(AS3 u32x4*)(uintptr_t)(hr_ad + 0);
hb[1][0] = *(AS3 u32x4*)(uintptr_t)(hr_ad + 1024);
hb[2][0] = *(AS3 u32x4*)(uintptr_t)(hr_ad + 2048);
hb[3][0] = *(AS3 u32x4*)(uintptr_t)(hr_ad + 3072);
hb[0][1] = *(AS3 u32x4*)(uintptr_t)(hr_ad + 8192);
hb[1][1] = *(AS3 u32x4*)(uintptr_t)(hr_ad + 9216);
hb[2][1] = *(AS3 u32x4*)(uintptr_t)(hr_ad + 10240);
hb[3][1] = *(AS3 u32x4*)(uintptr_t)(hr_ad + 11264);
SB();
asm volatile("s_waitcnt vmcnt(12)\n\ts_barrier" ::: "memory");
SB(); M16ZA(P[0][0], AF(0), hb[0][0]); SB();
afl[3] = TR(lbB1, 4096);
SB(); M16ZA(P[0][1], AF(0), hb[1][0]); SB();
afh[3] = TR(lbB1, 6144);
SB(); M16ZA(P[0][2], AF(0), hb[2][0]); SB();
LDW1(pw0, zvo, fHpa);
SB(); M16ZA(P[0][3], AF(0), hb[3][0]); SB();
hb[0][2] = *(AS3 u32x4*)(uintptr_t)(hr_ad + 16384);
SB(); M16ZA(P[1][0], AF(1), hb[0][0]); SB();
afl[0] = TR(lbB0, 8192);
SB(); M16ZA(P[1][1], AF(1), hb[1][0]); SB();
afh[0] = TR(lbB0, 10240);
SB(); M16ZA(P[1][2], AF(1), hb[2][0]); SB();
SETM0(wv2k + 65536);
SB(); M16ZA(P[1][3], AF(1), hb[3][0]); SB();
GLDS(lvoffA, lbase, 0);
hb[1][2] = *(AS3 u32x4*)(uintptr_t)(hr_ad + 17408);
SB(); M16A(P[0][0], AF(2), hb[0][1]); SB();
afl[1] = TR(lbB1, 8192);
hb[2][2] = *(AS3 u32x4*)(uintptr_t)(hr_ad + 18432);
SB(); M16A(P[0][1], AF(2), hb[1][1]); SB();
afh[1] = TR(lbB1, 10240);
hb[3][2] = *(AS3 u32x4*)(uintptr_t)(hr_ad + 19456);
SB(); M16A(P[0][2], AF(2), hb[2][1]); SB();
hb[0][3] = *(AS3 u32x4*)(uintptr_t)(hr_ad + 24576);
SB(); M16A(P[0][3], AF(2), hb[3][1]); SB();
hb[1][3] = *(AS3 u32x4*)(uintptr_t)(hr_ad + 25600);
SB(); M16A(P[1][0], AF(3), hb[0][1]); SB();
afl[2] = TR(lbB0, 12288);
hb[2][3] = *(AS3 u32x4*)(uintptr_t)(hr_ad + 26624);
SB(); M16A(P[1][1], AF(3), hb[1][1]); SB();
afh[2] = TR(lbB0, 14336);
hb[3][3] = *(AS3 u32x4*)(uintptr_t)(hr_ad + 27648);
SB(); M16A(P[1][2], AF(3), hb[2][1]); SB();
GLDS(lvoffB, lbase, 1024);
SB(); M16A(P[1][3], AF(3), hb[3][1]); SB();
SB();
asm volatile("s_waitcnt vmcnt(13)\n\ts_barrier" ::: "memory");
SB(); M16A(P[0][0], AF(0), hb[0][2]); SB();
afl[3] = TR(lbB1, 12288);
SB(); M16A(P[0][1], AF(0), hb[1][2]); SB();
afh[3] = TR(lbB1, 14336);
SB(); M16A(P[0][2], AF(0), hb[2][2]); SB();
lbase = wdb + 74240;
SB(); M16A(P[0][3], AF(0), hb[3][2]); SB();
hb[0][4] = *(AS3 u32x4*)(uintptr_t)(hr_ad + 32768);
SB(); M16A(P[1][0], AF(1), hb[0][2]); SB();
afl[0] = TR(lbB0, 16384);
SB(); M16A(P[1][1], AF(1), hb[1][2]); SB();
afh[0] = TR(lbB0, 18432);
SB(); M16A(P[1][2], AF(1), hb[2][2]); SB();
SETM0(wv2k + 0);
SB(); M16A(P[1][3], AF(1), hb[3][2]); SB();
GLDS(lvoffA, lbase, 0);
hb[1][4] = *(AS3 u32x4*)(uintptr_t)(hr_ad + 33792);
SB(); M16A(P[0][0], AF(2), hb[0][3]); SB();
afl[1] = TR(lbB1, 16384);
hb[2][4] = *(AS3 u32x4*)(uintptr_t)(hr_ad + 34816);
SB(); M16A(P[0][1], AF(2), hb[1][3]); SB();
afh[1] = TR(lbB1, 18432);
hb[3][4] = *(AS3 u32x4*)(uintptr_t)(hr_ad + 35840);
SB(); M16A(P[0][2], AF(2), hb[2][3]); SB();
hb[0][5] = *(AS3 u32x4*)(uintptr_t)(hr_ad + 40960);
SB(); M16A(P[0][3], AF(2), hb[3][3]); SB();
hb[1][5] = *(AS3 u32x4*)(uintptr_t)(hr_ad + 41984);
SB(); M16A(P[1][0], AF(3), hb[0][3]); SB();
afl[2] = TR(lbB0, 20480);
hb[2][5] = *(AS3 u32x4*)(uintptr_t)(hr_ad + 43008);
SB(); M16A(P[1][1], AF(3), hb[1][3]); SB();
afh[2] = TR(lbB0, 22528);
hb[3][5] = *(AS3 u32x4*)(uintptr_t)(hr_ad + 44032);
SB(); M16A(P[1][2], AF(3), hb[2][3]); SB();
GLDS(lvoffB, lbase, 1024);
SB(); M16A(P[1][3], AF(3), hb[3][3]); SB();
SB();
asm volatile("s_waitcnt vmcnt(4)\n\ts_barrier" : "+v"(pw0) :: "memory");
SIGNAL_SET(fHme, 4, true);
SIGNAL_ADD(fPw, nw, true);
CHECK_GE(pw0, fHpa, 4);
SB(); M16A(P[0][0], AF(0), hb[0][4]); SB();
afl[3] = TR(lbB1, 20480);
SB(); M16A(P[0][1], AF(0), hb[1][4]); SB();
afh[3] = TR(lbB1, 22528);
SB(); M16A(P[0][2], AF(0), hb[2][4]); SB();
lbase = wdb + 147968;
SB(); M16A(P[0][3], AF(0), hb[3][4]); SB();
hb[0][6] = *(AS3 u32x4*)(uintptr_t)(hr_ad + 49152);
asm volatile("s_mov_b32 m0, %0\n\ts_nop 0" :: "s"(wv4k + 139264) : "memory"); GLDS1(pvo, hxr4 + 0, 0); GLDS1(pvo, hxr4 + 0, 1024); GLDS1(pvo, hxr4 + 0, 2048); GLDS1(pvo, hxr4 + 0, 3072);
SB(); M16A(P[1][0], AF(1), hb[0][4]); SB();
afl[0] = TR(lbB0, 24576);
SB(); M16A(P[1][1], AF(1), hb[1][4]); SB();
afh[0] = TR(lbB0, 26624);
SB(); M16A(P[1][2], AF(1), hb[2][4]); SB();
SETM0(wv2k + 8192);
SB(); M16A(P[1][3], AF(1), hb[3][4]); SB();
GLDS(lvoffA, lbase, 0);
hb[1][6] = *(AS3 u32x4*)(uintptr_t)(hr_ad + 50176);
SB(); M16A(P[0][0], AF(2), hb[0][5]); SB();
afl[1] = TR(lbB1, 24576);
hb[2][6] = *(AS3 u32x4*)(uintptr_t)(hr_ad + 51200);
SB(); M16A(P[0][1], AF(2), hb[1][5]); SB();
afh[1] = TR(lbB1, 26624);
hb[3][6] = *(AS3 u32x4*)(uintptr_t)(hr_ad + 52224);
SB(); M16A(P[0][2], AF(2), hb[2][5]); SB();
hb[0][7] = *(AS3 u32x4*)(uintptr_t)(hr_ad + 57344);
SB(); M16A(P[0][3], AF(2), hb[3][5]); SB();
hb[1][7] = *(AS3 u32x4*)(uintptr_t)(hr_ad + 58368);
SB(); M16A(P[1][0], AF(3), hb[0][5]); SB();
afl[2] = TR(lbB0, 28672);
hb[2][7] = *(AS3 u32x4*)(uintptr_t)(hr_ad + 59392);
SB(); M16A(P[1][1], AF(3), hb[1][5]); SB();
afh[2] = TR(lbB0, 30720);
hb[3][7] = *(AS3 u32x4*)(uintptr_t)(hr_ad + 60416);
SB(); M16A(P[1][2], AF(3), hb[2][5]); SB();
GLDS(lvoffB, lbase, 1024);
SB(); M16A(P[1][3], AF(3), hb[3][5]); SB();
SB();
asm volatile("s_waitcnt vmcnt(17)\n\ts_barrier" ::: "memory");
SB(); M16A(P[0][0], AF(0), hb[0][6]); SB();
afl[3] = TR(lbB1, 28672);
SB(); M16A(P[0][1], AF(0), hb[1][6]); SB();
afh[3] = TR(lbB1, 30720);
SB(); M16A(P[0][2], AF(0), hb[2][6]); SB();
lbase = wdb + 221696;
SB(); M16A(P[0][3], AF(0), hb[3][6]); SB();
asm volatile("s_mov_b32 m0, %0\n\ts_nop 0" :: "s"(wv4k + 73728) : "memory"); GLDS1(pvo, hxr4 + 16384, 0); GLDS1(pvo, hxr4 + 16384, 1024); GLDS1(pvo, hxr4 + 16384, 2048); GLDS1(pvo, hxr4 + 16384, 3072);
SB(); M16A(P[1][0], AF(1), hb[0][6]); SB();
afl[0] = TR(lbB0, 32768);
SB(); M16A(P[1][1], AF(1), hb[1][6]); SB();
afh[0] = TR(lbB0, 34816);
SB(); M16A(P[1][2], AF(1), hb[2][6]); SB();
SETM0(wv2k + 16384);
SB(); M16A(P[1][3], AF(1), hb[3][6]); SB();
GLDS(lvoffA, lbase, 0);
SB(); M16A(P[0][0], AF(2), hb[0][7]); SB();
afl[1] = TR(lbB1, 32768);
SB(); M16A(P[0][1], AF(2), hb[1][7]); SB();
afh[1] = TR(lbB1, 34816);
SB(); M16A(P[0][2], AF(2), hb[2][7]); SB();
SB(); M16A(P[0][3], AF(2), hb[3][7]); SB();
SB(); M16A(P[1][0], AF(3), hb[0][7]); SB();
afl[2] = TR(lbB0, 36864);
SB(); M16A(P[1][1], AF(3), hb[1][7]); SB();
afh[2] = TR(lbB0, 38912);
SB(); M16A(P[1][2], AF(3), hb[2][7]); SB();
GLDS(lvoffB, lbase, 1024);
SB(); M16A(P[1][3], AF(3), hb[3][7]); SB();
SB();
asm volatile("s_waitcnt vmcnt(21)\n\ts_barrier" ::: "memory");
SB(); M16ZA(R[0][0], AF(0), hb[0][0]); SB();
afl[3] = TR(lbB1, 36864);
SB(); M16ZA(R[0][1], AF(0), hb[1][0]); SB();
afh[3] = TR(lbB1, 38912);
SB(); M16ZA(R[0][2], AF(0), hb[2][0]); SB();
lbase = wdb + 640;
SB(); M16ZA(R[0][3], AF(0), hb[3][0]); SB();
asm volatile("s_mov_b32 m0, %0\n\ts_nop 0" :: "s"(wv4k + 90112) : "memory"); GLDS1(pvo, hxr4 + 32768, 0); GLDS1(pvo, hxr4 + 32768, 1024); GLDS1(pvo, hxr4 + 32768, 2048); GLDS1(pvo, hxr4 + 32768, 3072);
SB(); M16ZA(R[1][0], AF(1), hb[0][0]); SB();
afl[0] = TR(lbB0, 40960);
SB(); M16ZA(R[1][1], AF(1), hb[1][0]); SB();
afh[0] = TR(lbB0, 43008);
SB(); M16ZA(R[1][2], AF(1), hb[2][0]); SB();
SETM0(wv2k + 24576);
SB(); M16ZA(R[1][3], AF(1), hb[3][0]); SB();
GLDS(lvoffA, lbase, 0);
SB(); M16A(R[0][0], AF(2), hb[0][1]); SB();
afl[1] = TR(lbB1, 40960);
SB(); M16A(R[0][1], AF(2), hb[1][1]); SB();
afh[1] = TR(lbB1, 43008);
SB(); M16A(R[0][2], AF(2), hb[2][1]); SB();
SB(); M16A(R[0][3], AF(2), hb[3][1]); SB();
SB(); M16A(R[1][0], AF(3), hb[0][1]); SB();
afl[2] = TR(lbB0, 45056);
SB(); M16A(R[1][1], AF(3), hb[1][1]); SB();
afh[2] = TR(lbB0, 47104);
SB(); M16A(R[1][2], AF(3), hb[2][1]); SB();
GLDS(lvoffB, lbase, 1024);
SB(); M16A(R[1][3], AF(3), hb[3][1]); SB();
SB();
asm volatile("s_waitcnt vmcnt(25)\n\ts_barrier" ::: "memory");
SB(); M16A(R[0][0], AF(0), hb[0][2]); SB();
afl[3] = TR(lbB1, 45056);
SB(); M16A(R[0][1], AF(0), hb[1][2]); SB();
afh[3] = TR(lbB1, 47104);
SB(); M16A(R[0][2], AF(0), hb[2][2]); SB();
lbase = wdb + 74368;
SB(); M16A(R[0][3], AF(0), hb[3][2]); SB();
asm volatile("s_mov_b32 m0, %0\n\ts_nop 0" :: "s"(wv4k + 106496) : "memory"); GLDS1(pvo, hxr4 + 49152, 0); GLDS1(pvo, hxr4 + 49152, 1024); GLDS1(pvo, hxr4 + 49152, 2048); GLDS1(pvo, hxr4 + 49152, 3072);
SB(); M16A(R[1][0], AF(1), hb[0][2]); SB();
afl[0] = TR(lbB0, 49152);
SB(); M16A(R[1][1], AF(1), hb[1][2]); SB();
afh[0] = TR(lbB0, 51200);
SB(); M16A(R[1][2], AF(1), hb[2][2]); SB();
SETM0(wv2k + 32768);
SB(); M16A(R[1][3], AF(1), hb[3][2]); SB();
GLDS(lvoffA, lbase, 0);
SB(); M16A(R[0][0], AF(2), hb[0][3]); SB();
afl[1] = TR(lbB1, 49152);
SB(); M16A(R[0][1], AF(2), hb[1][3]); SB();
afh[1] = TR(lbB1, 51200);
SB(); M16A(R[0][2], AF(2), hb[2][3]); SB();
SB(); M16A(R[0][3], AF(2), hb[3][3]); SB();
SB(); M16A(R[1][0], AF(3), hb[0][3]); SB();
afl[2] = TR(lbB0, 53248);
SB(); M16A(R[1][1], AF(3), hb[1][3]); SB();
afh[2] = TR(lbB0, 55296);
SB(); M16A(R[1][2], AF(3), hb[2][3]); SB();
GLDS(lvoffB, lbase, 1024);
SB(); M16A(R[1][3], AF(3), hb[3][3]); SB();
SB();
asm volatile("s_waitcnt vmcnt(29)\n\ts_barrier" ::: "memory");
SB(); M16A(R[0][0], AF(0), hb[0][4]); SB();
afl[3] = TR(lbB1, 53248);
SB(); M16A(R[0][1], AF(0), hb[1][4]); SB();
afh[3] = TR(lbB1, 55296);
SB(); M16A(R[0][2], AF(0), hb[2][4]); SB();
lbase = wdb + 148096;
SB(); M16A(R[0][3], AF(0), hb[3][4]); SB();
SB(); M16A(R[1][0], AF(1), hb[0][4]); SB();
afl[0] = TR(lbB0, 57344);
SB(); M16A(R[1][1], AF(1), hb[1][4]); SB();
afh[0] = TR(lbB0, 59392);
SB(); M16A(R[1][2], AF(1), hb[2][4]); SB();
SETM0(wv2k + 40960);
SB(); M16A(R[1][3], AF(1), hb[3][4]); SB();
GLDS(lvoffA, lbase, 0);
SB(); M16A(R[0][0], AF(2), hb[0][5]); SB();
afl[1] = TR(lbB1, 57344);
SB(); M16A(R[0][1], AF(2), hb[1][5]); SB();
afh[1] = TR(lbB1, 59392);
SB(); M16A(R[0][2], AF(2), hb[2][5]); SB();
SB(); M16A(R[0][3], AF(2), hb[3][5]); SB();
SB(); M16A(R[1][0], AF(3), hb[0][5]); SB();
afl[2] = TR(lbB0, 61440);
SB(); M16A(R[1][1], AF(3), hb[1][5]); SB();
afh[2] = TR(lbB0, 63488);
SB(); M16A(R[1][2], AF(3), hb[2][5]); SB();
GLDS(lvoffB, lbase, 1024);
SB(); M16A(R[1][3], AF(3), hb[3][5]); SB();
SB();
asm volatile("s_waitcnt vmcnt(22)\n\ts_barrier" ::: "memory");
SB(); M16A(R[0][0], AF(0), hb[0][6]); SB();
afl[3] = TR(lbB1, 61440);
SB(); M16A(R[0][1], AF(0), hb[1][6]); SB();
afh[3] = TR(lbB1, 63488);
SB(); M16A(R[0][2], AF(0), hb[2][6]); SB();
lbase = wdb + 221824;
SB(); M16A(R[0][3], AF(0), hb[3][6]); SB();
hb[0][8] = *(AS3 u32x4*)(uintptr_t)(hr_ad + 65536);
SB(); M16A(R[1][0], AF(1), hb[0][6]); SB();
afl[0] = TR(lbB0, 65536);
SB(); M16A(R[1][1], AF(1), hb[1][6]); SB();
afh[0] = TR(lbB0, 67584);
SB(); M16A(R[1][2], AF(1), hb[2][6]); SB();
SETM0(wv2k + 49152);
SB(); M16A(R[1][3], AF(1), hb[3][6]); SB();
GLDS(lvoffA, lbase, 0);
hb[1][8] = *(AS3 u32x4*)(uintptr_t)(hr_ad + 66560);
SB(); M16A(R[0][0], AF(2), hb[0][7]); SB();
afl[1] = TR(lbB1, 65536);
hb[2][8] = *(AS3 u32x4*)(uintptr_t)(hr_ad + 67584);
SB(); M16A(R[0][1], AF(2), hb[1][7]); SB();
afh[1] = TR(lbB1, 67584);
hb[3][8] = *(AS3 u32x4*)(uintptr_t)(hr_ad + 68608);
SB(); M16A(R[0][2], AF(2), hb[2][7]); SB();
hb[0][9] = *(AS3 u32x4*)(uintptr_t)(hr_ad + 73728);
SB(); M16A(R[0][3], AF(2), hb[3][7]); SB();
hb[1][9] = *(AS3 u32x4*)(uintptr_t)(hr_ad + 74752);
SB(); M16A(R[1][0], AF(3), hb[0][7]); SB();
afl[2] = TR(lbB0, 69632);
hb[2][9] = *(AS3 u32x4*)(uintptr_t)(hr_ad + 75776);
SB(); M16A(R[1][1], AF(3), hb[1][7]); SB();
afh[2] = TR(lbB0, 71680);
hb[3][9] = *(AS3 u32x4*)(uintptr_t)(hr_ad + 76800);
SB(); M16A(R[1][2], AF(3), hb[2][7]); SB();
GLDS(lvoffB, lbase, 1024);
SB(); M16A(R[1][3], AF(3), hb[3][7]); SB();
SB();
asm volatile("s_waitcnt vmcnt(18)\n\ts_barrier" ::: "memory");
SB(); M16A(P[0][0], AF(0), hb[0][8]); SB();
afl[3] = TR(lbB1, 69632);
SB(); M16A(P[0][1], AF(0), hb[1][8]); SB();
afh[3] = TR(lbB1, 71680);
SB(); M16A(P[0][2], AF(0), hb[2][8]); SB();
lbase = wdb + 295424;
SB(); M16A(P[0][3], AF(0), hb[3][8]); SB();
hb[0][10] = *(AS3 u32x4*)(uintptr_t)(hr_ad + 0);
SB(); M16A(P[1][0], AF(1), hb[0][8]); SB();
afl[0] = TR(lbB0, 0);
SB(); M16A(P[1][1], AF(1), hb[1][8]); SB();
afh[0] = TR(lbB0, 2048);
SB(); M16A(P[1][2], AF(1), hb[2][8]); SB();
SETM0(wv2k + 57344);
SB(); M16A(P[1][3], AF(1), hb[3][8]); SB();
GLDS(lvoffA, lbase, 0);
hb[1][10] = *(AS3 u32x4*)(uintptr_t)(hr_ad + 1024);
SB(); M16A(P[0][0], AF(2), hb[0][9]); SB();
afl[1] = TR(lbB1, 0);
hb[2][10] = *(AS3 u32x4*)(uintptr_t)(hr_ad + 2048);
SB(); M16A(P[0][1], AF(2), hb[1][9]); SB();
afh[1] = TR(lbB1, 2048);
hb[3][10] = *(AS3 u32x4*)(uintptr_t)(hr_ad + 3072);
SB(); M16A(P[0][2], AF(2), hb[2][9]); SB();
hb[0][11] = *(AS3 u32x4*)(uintptr_t)(hr_ad + 8192);
SB(); M16A(P[0][3], AF(2), hb[3][9]); SB();
hb[1][11] = *(AS3 u32x4*)(uintptr_t)(hr_ad + 9216);
SB(); M16A(P[1][0], AF(3), hb[0][9]); SB();
afl[2] = TR(lbB0, 4096);
hb[2][11] = *(AS3 u32x4*)(uintptr_t)(hr_ad + 10240);
SB(); M16A(P[1][1], AF(3), hb[1][9]); SB();
afh[2] = TR(lbB0, 6144);
hb[3][11] = *(AS3 u32x4*)(uintptr_t)(hr_ad + 11264);
SB(); M16A(P[1][2], AF(3), hb[2][9]); SB();
GLDS(lvoffB, lbase, 1024);
SB(); M16A(P[1][3], AF(3), hb[3][9]); SB();
SB();
asm volatile("s_waitcnt vmcnt(14)\n\ts_barrier" ::: "memory");
SB(); M16A(P[0][0], AF(0), hb[0][10]); SB();
afl[3] = TR(lbB1, 4096);
SB(); M16A(P[0][1], AF(0), hb[1][10]); SB();
afh[3] = TR(lbB1, 6144);
SB(); M16A(P[0][2], AF(0), hb[2][10]); SB();
lbase = wdb + 295552;
LDW1(pw1, zvo, fHpa);
SB(); M16A(P[0][3], AF(0), hb[3][10]); SB();
hb[0][12] = *(AS3 u32x4*)(uintptr_t)(hr_ad + 16384);
SB(); M16A(P[1][0], AF(1), hb[0][10]); SB();
afl[0] = TR(lbB0, 8192);
SB(); M16A(P[1][1], AF(1), hb[1][10]); SB();
afh[0] = TR(lbB0, 10240);
SB(); M16A(P[1][2], AF(1), hb[2][10]); SB();
SETM0(wv2k + 65536);
SB(); M16A(P[1][3], AF(1), hb[3][10]); SB();
GLDS(lvoffA, lbase, 0);
hb[1][12] = *(AS3 u32x4*)(uintptr_t)(hr_ad + 17408);
SB(); M16A(P[0][0], AF(2), hb[0][11]); SB();
afl[1] = TR(lbB1, 8192);
hb[2][12] = *(AS3 u32x4*)(uintptr_t)(hr_ad + 18432);
SB(); M16A(P[0][1], AF(2), hb[1][11]); SB();
afh[1] = TR(lbB1, 10240);
hb[3][12] = *(AS3 u32x4*)(uintptr_t)(hr_ad + 19456);
SB(); M16A(P[0][2], AF(2), hb[2][11]); SB();
hb[0][13] = *(AS3 u32x4*)(uintptr_t)(hr_ad + 24576);
SB(); M16A(P[0][3], AF(2), hb[3][11]); SB();
hb[1][13] = *(AS3 u32x4*)(uintptr_t)(hr_ad + 25600);
SB(); M16A(P[1][0], AF(3), hb[0][11]); SB();
afl[2] = TR(lbB0, 12288);
hb[2][13] = *(AS3 u32x4*)(uintptr_t)(hr_ad + 26624);
SB(); M16A(P[1][1], AF(3), hb[1][11]); SB();
afh[2] = TR(lbB0, 14336);
hb[3][13] = *(AS3 u32x4*)(uintptr_t)(hr_ad + 27648);
SB(); M16A(P[1][2], AF(3), hb[2][11]); SB();
GLDS(lvoffB, lbase, 1024);
SB(); M16A(P[1][3], AF(3), hb[3][11]); SB();
SB();
asm volatile("s_waitcnt vmcnt(11)\n\ts_barrier" ::: "memory");
SB(); M16A(P[0][0], AF(0), hb[0][12]); SB();
afl[3] = TR(lbB1, 12288);
SB(); M16A(P[0][1], AF(0), hb[1][12]); SB();
afh[3] = TR(lbB1, 14336);
SB(); M16A(P[0][2], AF(0), hb[2][12]); SB();
lbase = wdb + 369408;
SB(); M16A(P[0][3], AF(0), hb[3][12]); SB();
hb[0][14] = *(AS3 u32x4*)(uintptr_t)(hr_ad + 32768);
SB(); M16A(P[1][0], AF(1), hb[0][12]); SB();
afl[0] = TR(lbB0, 16384);
SB(); M16A(P[1][1], AF(1), hb[1][12]); SB();
afh[0] = TR(lbB0, 18432);
SB(); M16A(P[1][2], AF(1), hb[2][12]); SB();
SETM0(wv2k + 0);
SB(); M16A(P[1][3], AF(1), hb[3][12]); SB();
GLDS(lvoffA, lbase, 0);
hb[1][14] = *(AS3 u32x4*)(uintptr_t)(hr_ad + 33792);
SB(); M16A(P[0][0], AF(2), hb[0][13]); SB();
afl[1] = TR(lbB1, 16384);
hb[2][14] = *(AS3 u32x4*)(uintptr_t)(hr_ad + 34816);
SB(); M16A(P[0][1], AF(2), hb[1][13]); SB();
afh[1] = TR(lbB1, 18432);
hb[3][14] = *(AS3 u32x4*)(uintptr_t)(hr_ad + 35840);
SB(); M16A(P[0][2], AF(2), hb[2][13]); SB();
hb[0][15] = *(AS3 u32x4*)(uintptr_t)(hr_ad + 40960);
SB(); M16A(P[0][3], AF(2), hb[3][13]); SB();
hb[1][15] = *(AS3 u32x4*)(uintptr_t)(hr_ad + 41984);
SB(); M16A(P[1][0], AF(3), hb[0][13]); SB();
afl[2] = TR(lbB0, 20480);
hb[2][15] = *(AS3 u32x4*)(uintptr_t)(hr_ad + 43008);
SB(); M16A(P[1][1], AF(3), hb[1][13]); SB();
afh[2] = TR(lbB0, 22528);
hb[3][15] = *(AS3 u32x4*)(uintptr_t)(hr_ad + 44032);
SB(); M16A(P[1][2], AF(3), hb[2][13]); SB();
GLDS(lvoffB, lbase, 1024);
SB(); M16A(P[1][3], AF(3), hb[3][13]); SB();
SB();
asm volatile("s_waitcnt vmcnt(4)\n\ts_barrier" : "+v"(pw1) :: "memory");
CHECK_GE(pw1, fHpa, 5);
SB(); M16A(P[0][0], AF(0), hb[0][14]); SB();
afl[3] = TR(lbB1, 20480);
SB(); M16A(P[0][1], AF(0), hb[1][14]); SB();
afh[3] = TR(lbB1, 22528);
SB(); M16A(P[0][2], AF(0), hb[2][14]); SB();
lbase = wdb + 443136;
SB(); M16A(P[0][3], AF(0), hb[3][14]); SB();
asm volatile("s_mov_b32 m0, %0\n\ts_nop 0" :: "s"(wv4k + 122880) : "memory"); GLDS1(pvo, hxr4 + 65536, 0); GLDS1(pvo, hxr4 + 65536, 1024); GLDS1(pvo, hxr4 + 65536, 2048); GLDS1(pvo, hxr4 + 65536, 3072);
SB(); M16A(P[1][0], AF(1), hb[0][14]); SB();
afl[0] = TR(lbB0, 24576);
SB(); M16A(P[1][1], AF(1), hb[1][14]); SB();
afh[0] = TR(lbB0, 26624);
SB(); M16A(P[1][2], AF(1), hb[2][14]); SB();
SETM0(wv2k + 8192);
SB(); M16A(P[1][3], AF(1), hb[3][14]); SB();
GLDS(lvoffA, lbase, 0);
SB(); M16V(P[0][0], AF(2), hb[0][15]); SB();
afl[1] = TR(lbB1, 24576);
SB(); M16V(P[0][1], AF(2), hb[1][15]); SB();
afh[1] = TR(lbB1, 26624);
SB(); M16V(P[0][2], AF(2), hb[2][15]); SB();
SB(); M16V(P[0][3], AF(2), hb[3][15]); SB();
SB(); M16V(P[1][0], AF(3), hb[0][15]); SB();
afl[2] = TR(lbB0, 28672);
SB(); M16V(P[1][1], AF(3), hb[1][15]); SB();
afh[2] = TR(lbB0, 30720);
SB(); M16V(P[1][2], AF(3), hb[2][15]); SB();
GLDS(lvoffB, lbase, 1024);
SB(); M16V(P[1][3], AF(3), hb[3][15]); SB();
SB();
asm volatile("s_waitcnt vmcnt(17)\n\ts_barrier" ::: "memory");
SB(); M16A(R[0][0], AF(0), hb[0][8]); SB();
afl[3] = TR(lbB1, 28672);
SB(); M16A(R[0][1], AF(0), hb[1][8]); SB();
afh[3] = TR(lbB1, 30720);
SB(); M16A(R[0][2], AF(0), hb[2][8]); SB();
lbase = wdb + 516864;
SB(); M16A(R[0][3], AF(0), hb[3][8]); SB();
SB(); M16A(R[1][0], AF(1), hb[0][8]); SB();
afl[0] = TR(lbB0, 32768);
SB(); M16A(R[1][1], AF(1), hb[1][8]); SB();
afh[0] = TR(lbB0, 34816);
SB(); M16A(R[1][2], AF(1), hb[2][8]); SB();
SETM0(wv2k + 16384);
SB(); M16A(R[1][3], AF(1), hb[3][8]); SB();
GLDS(lvoffA, lbase, 0);
SB(); M16A(R[0][0], AF(2), hb[0][9]); SB();
afl[1] = TR(lbB1, 32768);
SB(); M16A(R[0][1], AF(2), hb[1][9]); SB();
afh[1] = TR(lbB1, 34816);
SB(); M16A(R[0][2], AF(2), hb[2][9]); SB();
SB(); M16A(R[0][3], AF(2), hb[3][9]); SB();
SB(); M16A(R[1][0], AF(3), hb[0][9]); SB();
afl[2] = TR(lbB0, 36864);
SB(); M16A(R[1][1], AF(3), hb[1][9]); SB();
afh[2] = TR(lbB0, 38912);
SB(); M16A(R[1][2], AF(3), hb[2][9]); SB();
GLDS(lvoffB, lbase, 1024);
SB(); M16A(R[1][3], AF(3), hb[3][9]); SB();
SB();
asm volatile("s_waitcnt vmcnt(17)\n\ts_barrier" ::: "memory");
SB(); M16A(R[0][0], AF(0), hb[0][10]); SB();
afl[3] = TR(lbB1, 36864);
SB(); M16A(R[0][1], AF(0), hb[1][10]); SB();
afh[3] = TR(lbB1, 38912);
SB(); M16A(R[0][2], AF(0), hb[2][10]); SB();
lbase = wdb + 590592;
SB(); M16A(R[0][3], AF(0), hb[3][10]); SB();
SB(); M16A(R[1][0], AF(1), hb[0][10]); SB();
afl[0] = TR(lbB0, 40960);
SB(); M16A(R[1][1], AF(1), hb[1][10]); SB();
afh[0] = TR(lbB0, 43008);
SB(); M16A(R[1][2], AF(1), hb[2][10]); SB();
SETM0(wv2k + 24576);
SB(); M16A(R[1][3], AF(1), hb[3][10]); SB();
GLDS(lvoffA, lbase, 0);
SB(); M16A(R[0][0], AF(2), hb[0][11]); SB();
afl[1] = TR(lbB1, 40960);
SB(); M16A(R[0][1], AF(2), hb[1][11]); SB();
afh[1] = TR(lbB1, 43008);
SB(); M16A(R[0][2], AF(2), hb[2][11]); SB();
SB(); M16A(R[0][3], AF(2), hb[3][11]); SB();
SB(); M16A(R[1][0], AF(3), hb[0][11]); SB();
afl[2] = TR(lbB0, 45056);
SB(); M16A(R[1][1], AF(3), hb[1][11]); SB();
afh[2] = TR(lbB0, 47104);
SB(); M16A(R[1][2], AF(3), hb[2][11]); SB();
GLDS(lvoffB, lbase, 1024);
SB(); M16A(R[1][3], AF(3), hb[3][11]); SB();
SB();
asm volatile("s_waitcnt vmcnt(17)\n\ts_barrier" ::: "memory");
SB(); M16A(R[0][0], AF(0), hb[0][12]); SB();
afl[3] = TR(lbB1, 45056);
SB(); M16A(R[0][1], AF(0), hb[1][12]); SB();
afh[3] = TR(lbB1, 47104);
SB(); M16A(R[0][2], AF(0), hb[2][12]); SB();
lbase = wdb + 768;
SB(); M16A(R[0][3], AF(0), hb[3][12]); SB();
SB(); M16A(R[1][0], AF(1), hb[0][12]); SB();
afl[0] = TR(lbB0, 49152);
SB(); M16A(R[1][1], AF(1), hb[1][12]); SB();
afh[0] = TR(lbB0, 51200);
SB(); M16A(R[1][2], AF(1), hb[2][12]); SB();
SETM0(wv2k + 32768);
SB(); M16A(R[1][3], AF(1), hb[3][12]); SB();
GLDS(lvoffA, lbase, 0);
SB(); M16A(R[0][0], AF(2), hb[0][13]); SB();
afl[1] = TR(lbB1, 49152);
SB(); M16A(R[0][1], AF(2), hb[1][13]); SB();
afh[1] = TR(lbB1, 51200);
SB(); M16A(R[0][2], AF(2), hb[2][13]); SB();
SB(); M16A(R[0][3], AF(2), hb[3][13]); SB();
SB(); M16A(R[1][0], AF(3), hb[0][13]); SB();
afl[2] = TR(lbB0, 53248);
SB(); M16A(R[1][1], AF(3), hb[1][13]); SB();
afh[2] = TR(lbB0, 55296);
SB(); M16A(R[1][2], AF(3), hb[2][13]); SB();
GLDS(lvoffB, lbase, 1024);
SB(); M16A(R[1][3], AF(3), hb[3][13]); SB();
SB();
asm volatile("s_waitcnt vmcnt(8)\n\ts_barrier" ::: "memory");
SB(); M16A(R[0][0], AF(0), hb[0][14]); SB();
afl[3] = TR(lbB1, 53248);
SB(); M16A(R[0][1], AF(0), hb[1][14]); SB();
afh[3] = TR(lbB1, 55296);
SB(); M16A(R[0][2], AF(0), hb[2][14]); SB();
lbase = wdb + 74496;
SB(); M16A(R[0][3], AF(0), hb[3][14]); SB();
hb[0][16] = *(AS3 u32x4*)(uintptr_t)(hr_ad + 49152);
SB(); M16A(R[1][0], AF(1), hb[0][14]); SB();
afl[0] = TR(lbB0, 57344);
SB(); M16A(R[1][1], AF(1), hb[1][14]); SB();
afh[0] = TR(lbB0, 59392);
SB(); M16A(R[1][2], AF(1), hb[2][14]); SB();
SETM0(wv2k + 40960);
SB(); M16A(R[1][3], AF(1), hb[3][14]); SB();
GLDS(lvoffA, lbase, 0);
hb[1][16] = *(AS3 u32x4*)(uintptr_t)(hr_ad + 50176);
SB(); M16V(R[0][0], AF(2), hb[0][15]); SB();
afl[1] = TR(lbB1, 57344);
hb[2][16] = *(AS3 u32x4*)(uintptr_t)(hr_ad + 51200);
SB(); M16V(R[0][1], AF(2), hb[1][15]); SB();
afh[1] = TR(lbB1, 59392);
hb[3][16] = *(AS3 u32x4*)(uintptr_t)(hr_ad + 52224);
SB(); M16V(R[0][2], AF(2), hb[2][15]); SB();
hb[0][17] = *(AS3 u32x4*)(uintptr_t)(hr_ad + 57344);
SB(); M16V(R[0][3], AF(2), hb[3][15]); SB();
hb[1][17] = *(AS3 u32x4*)(uintptr_t)(hr_ad + 58368);
SB(); M16V(R[1][0], AF(3), hb[0][15]); SB();
afl[2] = TR(lbB0, 61440);
hb[2][17] = *(AS3 u32x4*)(uintptr_t)(hr_ad + 59392);
SB(); M16V(R[1][1], AF(3), hb[1][15]); SB();
afh[2] = TR(lbB0, 63488);
hb[3][17] = *(AS3 u32x4*)(uintptr_t)(hr_ad + 60416);
SB(); M16V(R[1][2], AF(3), hb[2][15]); SB();
GLDS(lvoffB, lbase, 1024);
SB(); M16V(R[1][3], AF(3), hb[3][15]); SB();
SB();
asm volatile("s_waitcnt vmcnt(16)\n\ts_barrier" ::: "memory");
SB(); M16V(P[0][0], AF(0), hb[0][16]); SB();
afl[3] = TR(lbB1, 61440);
SB(); M16V(P[0][1], AF(0), hb[1][16]); SB();
afh[3] = TR(lbB1, 63488);
SB(); M16V(P[0][2], AF(0), hb[2][16]); SB();
lbase = wdb + 148224;
SB(); M16V(P[0][3], AF(0), hb[3][16]); SB();
SB(); M16V(P[1][0], AF(1), hb[0][16]); SB();
afl[0] = TR(lbB0, 65536);
SB(); M16V(P[1][1], AF(1), hb[1][16]); SB();
afh[0] = TR(lbB0, 67584);
SB(); M16V(P[1][2], AF(1), hb[2][16]); SB();
SETM0(wv2k + 49152);
SB(); M16V(P[1][3], AF(1), hb[3][16]); SB();
GLDS(lvoffA, lbase, 0);
SB(); M16V(P[0][0], AF(2), hb[0][17]); SB();
afl[1] = TR(lbB1, 65536);
SB(); M16V(P[0][1], AF(2), hb[1][17]); SB();
afh[1] = TR(lbB1, 67584);
SB(); M16V(P[0][2], AF(2), hb[2][17]); SB();
SB(); M16V(P[0][3], AF(2), hb[3][17]); SB();
SB(); M16V(P[1][0], AF(3), hb[0][17]); SB();
afl[2] = TR(lbB0, 69632);
SB(); M16V(P[1][1], AF(3), hb[1][17]); SB();
afh[2] = TR(lbB0, 71680);
SB(); M16V(P[1][2], AF(3), hb[2][17]); SB();
GLDS(lvoffB, lbase, 1024);
SB(); M16V(P[1][3], AF(3), hb[3][17]); SB();
SB();
asm volatile("s_waitcnt vmcnt(16)\n\ts_barrier" ::: "memory");
SB(); M16V(R[0][0], AF(0), hb[0][16]); SB();
afl[3] = TR(lbB1, 69632);
SB(); M16V(R[0][1], AF(0), hb[1][16]); SB();
afh[3] = TR(lbB1, 71680);
SB(); M16V(R[0][2], AF(0), hb[2][16]); SB();
lbase = wdb + 221952;
SB(); M16V(R[0][3], AF(0), hb[3][16]); SB();
SB(); M16V(R[1][0], AF(1), hb[0][16]); SB();
afl[0] = TR(lbB0, 0);
SB(); M16V(R[1][1], AF(1), hb[1][16]); SB();
afh[0] = TR(lbB0, 2048);
SB(); M16V(R[1][2], AF(1), hb[2][16]); SB();
SETM0(wv2k + 57344);
SB(); M16V(R[1][3], AF(1), hb[3][16]); SB();
GLDS(lvoffA, lbase, 0);
SB(); M16V(R[0][0], AF(2), hb[0][17]); SB();
afl[1] = TR(lbB1, 0);
SB(); M16V(R[0][1], AF(2), hb[1][17]); SB();
afh[1] = TR(lbB1, 2048);
SB(); M16V(R[0][2], AF(2), hb[2][17]); SB();
{ unsigned u = pk_bf16(P[0][0][0], P[0][0][1]); PIN(u); hh[0] = u; }
{ unsigned u = pk_bf16(P[0][0][2], P[0][0][3]); PIN(u); hh[1] = u; }
SB(); M16V(R[0][3], AF(2), hb[3][17]); SB();
{ unsigned u = pk_bf16(P[1][0][0], P[1][0][1]); PIN(u); hh[2] = u; }
{ unsigned u = pk_bf16(P[1][0][2], P[1][0][3]); PIN(u); hh[3] = u; }
SB(); M16V(R[1][0], AF(3), hb[0][17]); SB();
afl[2] = TR(lbB0, 4096);
SB(); M16V(R[1][1], AF(3), hb[1][17]); SB();
afh[2] = TR(lbB0, 6144);
SB(); M16V(R[1][2], AF(3), hb[2][17]); SB();
GLDS(lvoffB, lbase, 1024);
SB(); M16V(R[1][3], AF(3), hb[3][17]); SB();
STAMP(9); JIT(9);
SB();
asm volatile("s_waitcnt vmcnt(12)\n\ts_barrier" ::: "memory");
SB(); M16ZA(Q[0][0], AF(0), hb[0][0]); SB();
afl[3] = TR(lbB1, 4096);
SB(); M16ZA(Q[0][1], AF(0), hb[1][0]); SB();
afh[3] = TR(lbB1, 6144);
SB(); M16ZA(Q[0][2], AF(0), hb[2][0]); SB();
lbase = wdb + 295680;
SB(); M16ZA(Q[0][3], AF(0), hb[3][0]); SB();
PST(pvo, hh, pbase0 + 73728);
SB(); M16ZA(Q[1][0], AF(1), hb[0][0]); SB();
afl[0] = TR(lbB0, 8192);
SB(); M16ZA(Q[1][1], AF(1), hb[1][0]); SB();
afh[0] = TR(lbB0, 10240);
SB(); M16ZA(Q[1][2], AF(1), hb[2][0]); SB();
SETM0(wv2k + 65536);
{ unsigned u = pk_bf16(P[0][1][0], P[0][1][1]); PIN(u); hh[0] = u; }
SB(); M16ZA(Q[1][3], AF(1), hb[3][0]); SB();
GLDS(lvoffA, lbase, 0);
SB(); M16A(Q[0][0], AF(2), hb[0][1]); SB();
afl[1] = TR(lbB1, 8192);
SB(); M16A(Q[0][1], AF(2), hb[1][1]); SB();
afh[1] = TR(lbB1, 10240);
SB(); M16A(Q[0][2], AF(2), hb[2][1]); SB();
{ unsigned u = pk_bf16(P[0][1][2], P[0][1][3]); PIN(u); hh[1] = u; }
{ unsigned u = pk_bf16(P[1][1][0], P[1][1][1]); PIN(u); hh[2] = u; }
SB(); M16A(Q[0][3], AF(2), hb[3][1]); SB();
{ unsigned u = pk_bf16(P[1][1][2], P[1][1][3]); PIN(u); hh[3] = u; }
SB(); M16A(Q[1][0], AF(3), hb[0][1]); SB();
afl[2] = TR(lbB0, 12288);
SB(); M16A(Q[1][1], AF(3), hb[1][1]); SB();
afh[2] = TR(lbB0, 14336);
SB(); M16A(Q[1][2], AF(3), hb[2][1]); SB();
GLDS(lvoffB, lbase, 1024);
SB(); M16A(Q[1][3], AF(3), hb[3][1]); SB();
SB();
asm volatile("s_waitcnt vmcnt(13)\n\ts_barrier" ::: "memory");
SB(); M16A(Q[0][0], AF(0), hb[0][2]); SB();
afl[3] = TR(lbB1, 12288);
SB(); M16A(Q[0][1], AF(0), hb[1][2]); SB();
afh[3] = TR(lbB1, 14336);
SB(); M16A(Q[0][2], AF(0), hb[2][2]); SB();
lbase = wdb + 369536;
SB(); M16A(Q[0][3], AF(0), hb[3][2]); SB();
PST(pvo, hh, pbase0 + 239616);
SB(); M16A(Q[1][0], AF(1), hb[0][2]); SB();
afl[0] = TR(lbB0, 16384);
SB(); M16A(Q[1][1], AF(1), hb[1][2]); SB();
afh[0] = TR(lbB0, 18432);
SB(); M16A(Q[1][2], AF(1), hb[2][2]); SB();
SETM0(wv2k + 0);
{ unsigned u = pk_bf16(P[0][2][0], P[0][2][1]); PIN(u); hh[0] = u; }
SB(); M16A(Q[1][3], AF(1), hb[3][2]); SB();
GLDS(lvoffA, lbase, 0);
SB(); M16A(Q[0][0], AF(2), hb[0][3]); SB();
afl[1] = TR(lbB1, 16384);
SB(); M16A(Q[0][1], AF(2), hb[1][3]); SB();
afh[1] = TR(lbB1, 18432);
SB(); M16A(Q[0][2], AF(2), hb[2][3]); SB();
{ unsigned u = pk_bf16(P[0][2][2], P[0][2][3]); PIN(u); hh[1] = u; }
{ unsigned u = pk_bf16(P[1][2][0], P[1][2][1]); PIN(u); hh[2] = u; }
SB(); M16A(Q[0][3], AF(2), hb[3][3]); SB();
{ unsigned u = pk_bf16(P[1][2][2], P[1][2][3]); PIN(u); hh[3] = u; }
SB(); M16A(Q[1][0], AF(3), hb[0][3]); SB();
afl[2] = TR(lbB0, 20480);
SB(); M16A(Q[1][1], AF(3), hb[1][3]); SB();
afh[2] = TR(lbB0, 22528);
SB(); M16A(Q[1][2], AF(3), hb[2][3]); SB();
GLDS(lvoffB, lbase, 1024);
SB(); M16A(Q[1][3], AF(3), hb[3][3]); SB();
SB();
asm volatile("s_waitcnt vmcnt(14)\n\ts_barrier" ::: "memory");
SB(); M16A(Q[0][0], AF(0), hb[0][4]); SB();
afl[3] = TR(lbB1, 20480);
SB(); M16A(Q[0][1], AF(0), hb[1][4]); SB();
afh[3] = TR(lbB1, 22528);
SB(); M16A(Q[0][2], AF(0), hb[2][4]); SB();
lbase = wdb + 443264;
SB(); M16A(Q[0][3], AF(0), hb[3][4]); SB();
PST(pvo, hh, pbase0 + 405504);
SB(); M16A(Q[1][0], AF(1), hb[0][4]); SB();
afl[0] = TR(lbB0, 24576);
SB(); M16A(Q[1][1], AF(1), hb[1][4]); SB();
afh[0] = TR(lbB0, 26624);
SB(); M16A(Q[1][2], AF(1), hb[2][4]); SB();
SETM0(wv2k + 8192);
{ unsigned u = pk_bf16(P[0][3][0], P[0][3][1]); PIN(u); hh[0] = u; }
SB(); M16A(Q[1][3], AF(1), hb[3][4]); SB();
GLDS(lvoffA, lbase, 0);
SB(); M16A(Q[0][0], AF(2), hb[0][5]); SB();
afl[1] = TR(lbB1, 24576);
SB(); M16A(Q[0][1], AF(2), hb[1][5]); SB();
afh[1] = TR(lbB1, 26624);
SB(); M16A(Q[0][2], AF(2), hb[2][5]); SB();
{ unsigned u = pk_bf16(P[0][3][2], P[0][3][3]); PIN(u); hh[1] = u; }
{ unsigned u = pk_bf16(P[1][3][0], P[1][3][1]); PIN(u); hh[2] = u; }
SB(); M16A(Q[0][3], AF(2), hb[3][5]); SB();
{ unsigned u = pk_bf16(P[1][3][2], P[1][3][3]); PIN(u); hh[3] = u; }
SB(); M16A(Q[1][0], AF(3), hb[0][5]); SB();
afl[2] = TR(lbB0, 28672);
SB(); M16A(Q[1][1], AF(3), hb[1][5]); SB();
afh[2] = TR(lbB0, 30720);
SB(); M16A(Q[1][2], AF(3), hb[2][5]); SB();
GLDS(lvoffB, lbase, 1024);
SB(); M16A(Q[1][3], AF(3), hb[3][5]); SB();
SB();
asm volatile("s_waitcnt vmcnt(15)\n\ts_barrier" ::: "memory");
SB(); M16A(Q[0][0], AF(0), hb[0][6]); SB();
afl[3] = TR(lbB1, 28672);
SB(); M16A(Q[0][1], AF(0), hb[1][6]); SB();
afh[3] = TR(lbB1, 30720);
SB(); M16A(Q[0][2], AF(0), hb[2][6]); SB();
lbase = wdb + 516992;
SB(); M16A(Q[0][3], AF(0), hb[3][6]); SB();
PST(pvo, hh, pbase0 + 571392);
SB(); M16A(Q[1][0], AF(1), hb[0][6]); SB();
afl[0] = TR(lbB0, 32768);
SB(); M16A(Q[1][1], AF(1), hb[1][6]); SB();
afh[0] = TR(lbB0, 34816);
SB(); M16A(Q[1][2], AF(1), hb[2][6]); SB();
SETM0(wv2k + 16384);
{ unsigned u = pk_bf16(R[0][0][0], R[0][0][1]); PIN(u); hh[0] = u; }
SB(); M16A(Q[1][3], AF(1), hb[3][6]); SB();
GLDS(lvoffA, lbase, 0);
SB(); M16A(Q[0][0], AF(2), hb[0][7]); SB();
afl[1] = TR(lbB1, 32768);
SB(); M16A(Q[0][1], AF(2), hb[1][7]); SB();
afh[1] = TR(lbB1, 34816);
SB(); M16A(Q[0][2], AF(2), hb[2][7]); SB();
{ unsigned u = pk_bf16(R[0][0][2], R[0][0][3]); PIN(u); hh[1] = u; }
{ unsigned u = pk_bf16(R[1][0][0], R[1][0][1]); PIN(u); hh[2] = u; }
SB(); M16A(Q[0][3], AF(2), hb[3][7]); SB();
{ unsigned u = pk_bf16(R[1][0][2], R[1][0][3]); PIN(u); hh[3] = u; }
SB(); M16A(Q[1][0], AF(3), hb[0][7]); SB();
afl[2] = TR(lbB0, 36864);
SB(); M16A(Q[1][1], AF(3), hb[1][7]); SB();
afh[2] = TR(lbB0, 38912);
SB(); M16A(Q[1][2], AF(3), hb[2][7]); SB();
GLDS(lvoffB, lbase, 1024);
SB(); M16A(Q[1][3], AF(3), hb[3][7]); SB();
SB();
asm volatile("s_waitcnt vmcnt(16)\n\ts_barrier" ::: "memory");
SB(); M16A(Q[0][0], AF(0), hb[0][8]); SB();
afl[3] = TR(lbB1, 36864);
SB(); M16A(Q[0][1], AF(0), hb[1][8]); SB();
afh[3] = TR(lbB1, 38912);
SB(); M16A(Q[0][2], AF(0), hb[2][8]); SB();
lbase = wdb + 590720;
SB(); M16A(Q[0][3], AF(0), hb[3][8]); SB();
PST(pvo, hh, pbase0 + 92160);
SB(); M16A(Q[1][0], AF(1), hb[0][8]); SB();
afl[0] = TR(lbB0, 40960);
SB(); M16A(Q[1][1], AF(1), hb[1][8]); SB();
afh[0] = TR(lbB0, 43008);
SB(); M16A(Q[1][2], AF(1), hb[2][8]); SB();
SETM0(wv2k + 24576);
{ unsigned u = pk_bf16(R[0][1][0], R[0][1][1]); PIN(u); hh[0] = u; }
SB(); M16A(Q[1][3], AF(1), hb[3][8]); SB();
GLDS(lvoffA, lbase, 0);
SB(); M16A(Q[0][0], AF(2), hb[0][9]); SB();
afl[1] = TR(lbB1, 40960);
SB(); M16A(Q[0][1], AF(2), hb[1][9]); SB();
afh[1] = TR(lbB1, 43008);
SB(); M16A(Q[0][2], AF(2), hb[2][9]); SB();
{ unsigned u = pk_bf16(R[0][1][2], R[0][1][3]); PIN(u); hh[1] = u; }
{ unsigned u = pk_bf16(R[1][1][0], R[1][1][1]); PIN(u); hh[2] = u; }
SB(); M16A(Q[0][3], AF(2), hb[3][9]); SB();
{ unsigned u = pk_bf16(R[1][1][2], R[1][1][3]); PIN(u); hh[3] = u; }
SB(); M16A(Q[1][0], AF(3), hb[0][9]); SB();
afl[2] = TR(lbB0, 45056);
SB(); M16A(Q[1][1], AF(3), hb[1][9]); SB();
afh[2] = TR(lbB0, 47104);
SB(); M16A(Q[1][2], AF(3), hb[2][9]); SB();
GLDS(lvoffB, lbase, 1024);
SB(); M16A(Q[1][3], AF(3), hb[3][9]); SB();
SB();
asm volatile("s_waitcnt vmcnt(17)\n\ts_barrier" ::: "memory");
SB(); M16A(Q[0][0], AF(0), hb[0][10]); SB();
afl[3] = TR(lbB1, 45056);
SB(); M16A(Q[0][1], AF(0), hb[1][10]); SB();
afh[3] = TR(lbB1, 47104);
SB(); M16A(Q[0][2], AF(0), hb[2][10]); SB();
lbase = wdb + 896;
SB(); M16A(Q[0][3], AF(0), hb[3][10]); SB();
PST(pvo, hh, pbase0 + 258048);
SB(); M16A(Q[1][0], AF(1), hb[0][10]); SB();
afl[0] = TR(lbB0, 49152);
SB(); M16A(Q[1][1], AF(1), hb[1][10]); SB();
afh[0] = TR(lbB0, 51200);
SB(); M16A(Q[1][2], AF(1), hb[2][10]); SB();
SETM0(wv2k + 32768);
{ unsigned u = pk_bf16(R[0][2][0], R[0][2][1]); PIN(u); hh[0] = u; }
SB(); M16A(Q[1][3], AF(1), hb[3][10]); SB();
GLDS(lvoffA, lbase, 0);
SB(); M16A(Q[0][0], AF(2), hb[0][11]); SB();
afl[1] = TR(lbB1, 49152);
SB(); M16A(Q[0][1], AF(2), hb[1][11]); SB();
afh[1] = TR(lbB1, 51200);
SB(); M16A(Q[0][2], AF(2), hb[2][11]); SB();
{ unsigned u = pk_bf16(R[0][2][2], R[0][2][3]); PIN(u); hh[1] = u; }
{ unsigned u = pk_bf16(R[1][2][0], R[1][2][1]); PIN(u); hh[2] = u; }
SB(); M16A(Q[0][3], AF(2), hb[3][11]); SB();
{ unsigned u = pk_bf16(R[1][2][2], R[1][2][3]); PIN(u); hh[3] = u; }
SB(); M16A(Q[1][0], AF(3), hb[0][11]); SB();
afl[2] = TR(lbB0, 53248);
SB(); M16A(Q[1][1], AF(3), hb[1][11]); SB();
afh[2] = TR(lbB0, 55296);
SB(); M16A(Q[1][2], AF(3), hb[2][11]); SB();
GLDS(lvoffB, lbase, 1024);
SB(); M16A(Q[1][3], AF(3), hb[3][11]); SB();
SB();
asm volatile("s_waitcnt vmcnt(18)\n\ts_barrier" ::: "memory");
SB(); M16A(Q[0][0], AF(0), hb[0][12]); SB();
afl[3] = TR(lbB1, 53248);
SB(); M16A(Q[0][1], AF(0), hb[1][12]); SB();
afh[3] = TR(lbB1, 55296);
SB(); M16A(Q[0][2], AF(0), hb[2][12]); SB();
lbase = wdb + 74624;
SB(); M16A(Q[0][3], AF(0), hb[3][12]); SB();
PST(pvo, hh, pbase0 + 423936);
SB(); M16A(Q[1][0], AF(1), hb[0][12]); SB();
afl[0] = TR(lbB0, 57344);
SB(); M16A(Q[1][1], AF(1), hb[1][12]); SB();
afh[0] = TR(lbB0, 59392);
SB(); M16A(Q[1][2], AF(1), hb[2][12]); SB();
SETM0(wv2k + 40960);
{ unsigned u = pk_bf16(R[0][3][0], R[0][3][1]); PIN(u); hh[0] = u; }
SB(); M16A(Q[1][3], AF(1), hb[3][12]); SB();
GLDS(lvoffA, lbase, 0);
SB(); M16A(Q[0][0], AF(2), hb[0][13]); SB();
afl[1] = TR(lbB1, 57344);
SB(); M16A(Q[0][1], AF(2), hb[1][13]); SB();
afh[1] = TR(lbB1, 59392);
SB(); M16A(Q[0][2], AF(2), hb[2][13]); SB();
{ unsigned u = pk_bf16(R[0][3][2], R[0][3][3]); PIN(u); hh[1] = u; }
{ unsigned u = pk_bf16(R[1][3][0], R[1][3][1]); PIN(u); hh[2] = u; }
SB(); M16A(Q[0][3], AF(2), hb[3][13]); SB();
{ unsigned u = pk_bf16(R[1][3][2], R[1][3][3]); PIN(u); hh[3] = u; }
SB(); M16A(Q[1][0], AF(3), hb[0][13]); SB();
afl[2] = TR(lbB0, 61440);
SB(); M16A(Q[1][1], AF(3), hb[1][13]); SB();
afh[2] = TR(lbB0, 63488);
SB(); M16A(Q[1][2], AF(3), hb[2][13]); SB();
GLDS(lvoffB, lbase, 1024);
SB(); M16A(Q[1][3], AF(3), hb[3][13]); SB();
SB();
asm volatile("s_waitcnt vmcnt(18)\n\ts_barrier" ::: "memory");
SB(); M16A(Q[0][0], AF(0), hb[0][14]); SB();
afl[3] = TR(lbB1, 61440);
SB(); M16A(Q[0][1], AF(0), hb[1][14]); SB();
afh[3] = TR(lbB1, 63488);
SB(); M16A(Q[0][2], AF(0), hb[2][14]); SB();
lbase = wdb + 148352;
SB(); M16A(Q[0][3], AF(0), hb[3][14]); SB();
PST(pvo, hh, pbase0 + 589824);
SB(); M16A(Q[1][0], AF(1), hb[0][14]); SB();
afl[0] = TR(lbB0, 65536);
SB(); M16A(Q[1][1], AF(1), hb[1][14]); SB();
afh[0] = TR(lbB0, 67584);
SB(); M16A(Q[1][2], AF(1), hb[2][14]); SB();
SETM0(wv2k + 49152);
SB(); M16A(Q[1][3], AF(1), hb[3][14]); SB();
GLDS(lvoffA, lbase, 0);
SB(); M16V(Q[0][0], AF(2), hb[0][15]); SB();
afl[1] = TR(lbB1, 65536);
SB(); M16V(Q[0][1], AF(2), hb[1][15]); SB();
afh[1] = TR(lbB1, 67584);
SB(); M16V(Q[0][2], AF(2), hb[2][15]); SB();
SB(); M16V(Q[0][3], AF(2), hb[3][15]); SB();
SB(); M16V(Q[1][0], AF(3), hb[0][15]); SB();
afl[2] = TR(lbB0, 69632);
SB(); M16V(Q[1][1], AF(3), hb[1][15]); SB();
afh[2] = TR(lbB0, 71680);
SB(); M16V(Q[1][2], AF(3), hb[2][15]); SB();
GLDS(lvoffB, lbase, 1024);
SB(); M16V(Q[1][3], AF(3), hb[3][15]); SB();
SB();
asm volatile("s_waitcnt vmcnt(18)\n\ts_barrier" ::: "memory");
SB(); M16V(Q[0][0], AF(0), hb[0][16]); SB();
afl[3] = TR(lbB1, 69632);
SB(); M16V(Q[0][1], AF(0), hb[1][16]); SB();
afh[3] = TR(lbB1, 71680);
SB(); M16V(Q[0][2], AF(0), hb[2][16]); SB();
lbase = wdb + 222080;
SB(); M16V(Q[0][3], AF(0), hb[3][16]); SB();
SB(); M16V(Q[1][0], AF(1), hb[0][16]); SB();
afl[0] = TR(lbB0, 0);
SB(); M16V(Q[1][1], AF(1), hb[1][16]); SB();
afh[0] = TR(lbB0, 2048);
SB(); M16V(Q[1][2], AF(1), hb[2][16]); SB();
SETM0(wv2k + 57344);
SB(); M16V(Q[1][3], AF(1), hb[3][16]); SB();
GLDS(lvoffA, lbase, 0);
SB(); M16V(Q[0][0], AF(2), hb[0][17]); SB();
afl[1] = TR(lbB1, 0);
SB(); M16V(Q[0][1], AF(2), hb[1][17]); SB();
afh[1] = TR(lbB1, 2048);
SB(); M16V(Q[0][2], AF(2), hb[2][17]); SB();
SB(); M16V(Q[0][3], AF(2), hb[3][17]); SB();
SB(); M16V(Q[1][0], AF(3), hb[0][17]); SB();
afl[2] = TR(lbB0, 4096);
SB(); M16V(Q[1][1], AF(3), hb[1][17]); SB();
afh[2] = TR(lbB0, 6144);
SB(); M16V(Q[1][2], AF(3), hb[2][17]); SB();
GLDS(lvoffB, lbase, 1024);
SB(); M16V(Q[1][3], AF(3), hb[3][17]); SB();
SB();
asm volatile("s_waitcnt vmcnt(4)\n\ts_barrier" ::: "memory");
SIGNAL_ADD(fP, 1, true);
SB(); M16ZA(P[0][0], AF(0), hb[0][0]); SB();
afl[3] = TR(lbB1, 4096);
SB(); M16ZA(P[0][1], AF(0), hb[1][0]); SB();
afh[3] = TR(lbB1, 6144);
SB(); M16ZA(P[0][2], AF(0), hb[2][0]); SB();
lbase = wdb + 295808;
SB(); M16ZA(P[0][3], AF(0), hb[3][0]); SB();
SB(); M16ZA(P[1][0], AF(1), hb[0][0]); SB();
afl[0] = TR(lbB0, 8192);
SB(); M16ZA(P[1][1], AF(1), hb[1][0]); SB();
afh[0] = TR(lbB0, 10240);
SB(); M16ZA(P[1][2], AF(1), hb[2][0]); SB();
SETM0(wv2k + 65536);
SB(); M16ZA(P[1][3], AF(1), hb[3][0]); SB();
GLDS(lvoffA, lbase, 0);
SB(); M16A(P[0][0], AF(2), hb[0][1]); SB();
afl[1] = TR(lbB1, 8192);
SB(); M16A(P[0][1], AF(2), hb[1][1]); SB();
afh[1] = TR(lbB1, 10240);
SB(); M16A(P[0][2], AF(2), hb[2][1]); SB();
{ unsigned u = pk_bf16(Q[0][0][0], Q[0][0][1]); PIN(u); hh[0] = u; }
{ unsigned u = pk_bf16(Q[0][0][2], Q[0][0][3]); PIN(u); hh[1] = u; }
SB(); M16A(P[0][3], AF(2), hb[3][1]); SB();
{ unsigned u = pk_bf16(Q[1][0][0], Q[1][0][1]); PIN(u); hh[2] = u; }
{ unsigned u = pk_bf16(Q[1][0][2], Q[1][0][3]); PIN(u); hh[3] = u; }
SB(); M16A(P[1][0], AF(3), hb[0][1]); SB();
afl[2] = TR(lbB0, 12288);
SB(); M16A(P[1][1], AF(3), hb[1][1]); SB();
afh[2] = TR(lbB0, 14336);
SB(); M16A(P[1][2], AF(3), hb[2][1]); SB();
GLDS(lvoffB, lbase, 1024);
SB(); M16A(P[1][3], AF(3), hb[3][1]); SB();
SB();
asm volatile("s_waitcnt vmcnt(16)\n\ts_barrier" ::: "memory");
SB(); M16A(P[0][0], AF(0), hb[0][2]); SB();
afl[3] = TR(lbB1, 12288);
SB(); M16A(P[0][1], AF(0), hb[1][2]); SB();
afh[3] = TR(lbB1, 14336);
SB(); M16A(P[0][2], AF(0), hb[2][2]); SB();
lbase = wdb + 369664;
SB(); M16A(P[0][3], AF(0), hb[3][2]); SB();
PST(pvo, hh, pbase0 + 110592);
SB(); M16A(P[1][0], AF(1), hb[0][2]); SB();
afl[0] = TR(lbB0, 16384);
SB(); M16A(P[1][1], AF(1), hb[1][2]); SB();
afh[0] = TR(lbB0, 18432);
SB(); M16A(P[1][2], AF(1), hb[2][2]); SB();
SETM0(wv2k + 0);
{ unsigned u = pk_bf16(Q[0][1][0], Q[0][1][1]); PIN(u); hh[0] = u; }
SB(); M16A(P[1][3], AF(1), hb[3][2]); SB();
GLDS(lvoffA, lbase, 0);
SB(); M16A(P[0][0], AF(2), hb[0][3]); SB();
afl[1] = TR(lbB1, 16384);
SB(); M16A(P[0][1], AF(2), hb[1][3]); SB();
afh[1] = TR(lbB1, 18432);
SB(); M16A(P[0][2], AF(2), hb[2][3]); SB();
{ unsigned u = pk_bf16(Q[0][1][2], Q[0][1][3]); PIN(u); hh[1] = u; }
{ unsigned u = pk_bf16(Q[1][1][0], Q[1][1][1]); PIN(u); hh[2] = u; }
SB(); M16A(P[0][3], AF(2), hb[3][3]); SB();
{ unsigned u = pk_bf16(Q[1][1][2], Q[1][1][3]); PIN(u); hh[3] = u; }
SB(); M16A(P[1][0], AF(3), hb[0][3]); SB();
afl[2] = TR(lbB0, 20480);
SB(); M16A(P[1][1], AF(3), hb[1][3]); SB();
afh[2] = TR(lbB0, 22528);
SB(); M16A(P[1][2], AF(3), hb[2][3]); SB();
GLDS(lvoffB, lbase, 1024);
SB(); M16A(P[1][3], AF(3), hb[3][3]); SB();
SB();
asm volatile("s_waitcnt vmcnt(16)\n\ts_barrier" ::: "memory");
SB(); M16A(P[0][0], AF(0), hb[0][4]); SB();
afl[3] = TR(lbB1, 20480);
SB(); M16A(P[0][1], AF(0), hb[1][4]); SB();
afh[3] = TR(lbB1, 22528);
SB(); M16A(P[0][2], AF(0), hb[2][4]); SB();
lbase = wdb + 443392;
SB(); M16A(P[0][3], AF(0), hb[3][4]); SB();
PST(pvo, hh, pbase0 + 276480);
SB(); M16A(P[1][0], AF(1), hb[0][4]); SB();
afl[0] = TR(lbB0, 24576);
SB(); M16A(P[1][1], AF(1), hb[1][4]); SB();
afh[0] = TR(lbB0, 26624);
SB(); M16A(P[1][2], AF(1), hb[2][4]); SB();
SETM0(wv2k + 8192);
{ unsigned u = pk_bf16(Q[0][2][0], Q[0][2][1]); PIN(u); hh[0] = u; }
SB(); M16A(P[1][3], AF(1), hb[3][4]); SB();
GLDS(lvoffA, lbase, 0);
SB(); M16A(P[0][0], AF(2), hb[0][5]); SB();
afl[1] = TR(lbB1, 24576);
SB(); M16A(P[0][1], AF(2), hb[1][5]); SB();
afh[1] = TR(lbB1, 26624);
SB(); M16A(P[0][2], AF(2), hb[2][5]); SB();
{ unsigned u = pk_bf16(Q[0][2][2], Q[0][2][3]); PIN(u); hh[1] = u; }
{ unsigned u = pk_bf16(Q[1][2][0], Q[1][2][1]); PIN(u); hh[2] = u; }
SB(); M16A(P[0][3], AF(2), hb[3][5]); SB();
{ unsigned u = pk_bf16(Q[1][2][2], Q[1][2][3]); PIN(u); hh[3] = u; }
SB(); M16A(P[1][0], AF(3), hb[0][5]); SB();
afl[2] = TR(lbB0, 28672);
SB(); M16A(P[1][1], AF(3), hb[1][5]); SB();
afh[2] = TR(lbB0, 30720);
SB(); M16A(P[1][2], AF(3), hb[2][5]); SB();
GLDS(lvoffB, lbase, 1024);
SB(); M16A(P[1][3], AF(3), hb[3][5]); SB();
SB();
asm volatile("s_waitcnt vmcnt(16)\n\ts_barrier" ::: "memory");
SB(); M16A(P[0][0], AF(0), hb[0][6]); SB();
afl[3] = TR(lbB1, 28672);
SB(); M16A(P[0][1], AF(0), hb[1][6]); SB();
afh[3] = TR(lbB1, 30720);
SB(); M16A(P[0][2], AF(0), hb[2][6]); SB();
lbase = wdb + 517120;
SB(); M16A(P[0][3], AF(0), hb[3][6]); SB();
PST(pvo, hh, pbase0 + 442368);
SB(); M16A(P[1][0], AF(1), hb[0][6]); SB();
afl[0] = TR(lbB0, 32768);
SB(); M16A(P[1][1], AF(1), hb[1][6]); SB();
afh[0] = TR(lbB0, 34816);
SB(); M16A(P[1][2], AF(1), hb[2][6]); SB();
SETM0(wv2k + 16384);
{ unsigned u = pk_bf16(Q[0][3][0], Q[0][3][1]); PIN(u); hh[0] = u; }
SB(); M16A(P[1][3], AF(1), hb[3][6]); SB();
GLDS(lvoffA, lbase, 0);
SB(); M16A(P[0][0], AF(2), hb[0][7]); SB();
afl[1] = TR(lbB1, 32768);
SB(); M16A(P[0][1], AF(2), hb[1][7]); SB();
afh[1] = TR(lbB1, 34816);
SB(); M16A(P[0][2], AF(2), hb[2][7]); SB();
{ unsigned u = pk_bf16(Q[0][3][2], Q[0][3][3]); PIN(u); hh[1] = u; }
{ unsigned u = pk_bf16(Q[1][3][0], Q[1][3][1]); PIN(u); hh[2] = u; }
SB(); M16A(P[0][3], AF(2), hb[3][7]); SB();
{ unsigned u = pk_bf16(Q[1][3][2], Q[1][3][3]); PIN(u); hh[3] = u; }
SB(); M16A(P[1][0], AF(3), hb[0][7]); SB();
afl[2] = TR(lbB0, 36864);
SB(); M16A(P[1][1], AF(3), hb[1][7]); SB();
afh[2] = TR(lbB0, 38912);
SB(); M16A(P[1][2], AF(3), hb[2][7]); SB();
GLDS(lvoffB, lbase, 1024);
SB(); M16A(P[1][3], AF(3), hb[3][7]); SB();
SB();
asm volatile("s_waitcnt vmcnt(16)\n\ts_barrier" ::: "memory");
SB(); M16A(P[0][0], AF(0), hb[0][8]); SB();
afl[3] = TR(lbB1, 36864);
SB(); M16A(P[0][1], AF(0), hb[1][8]); SB();
afh[3] = TR(lbB1, 38912);
SB(); M16A(P[0][2], AF(0), hb[2][8]); SB();
lbase = wdb + 590848;
SB(); M16A(P[0][3], AF(0), hb[3][8]); SB();
PST(pvo, hh, pbase0 + 608256);
SB(); M16A(P[1][0], AF(1), hb[0][8]); SB();
afl[0] = TR(lbB0, 40960);
SB(); M16A(P[1][1], AF(1), hb[1][8]); SB();
afh[0] = TR(lbB0, 43008);
SB(); M16A(P[1][2], AF(1), hb[2][8]); SB();
SETM0(wv2k + 24576);
SB(); M16A(P[1][3], AF(1), hb[3][8]); SB();
GLDS(lvoffA, lbase, 0);
SB(); M16A(P[0][0], AF(2), hb[0][9]); SB();
afl[1] = TR(lbB1, 40960);
SB(); M16A(P[0][1], AF(2), hb[1][9]); SB();
afh[1] = TR(lbB1, 43008);
SB(); M16A(P[0][2], AF(2), hb[2][9]); SB();
SB(); M16A(P[0][3], AF(2), hb[3][9]); SB();
SB(); M16A(P[1][0], AF(3), hb[0][9]); SB();
afl[2] = TR(lbB0, 45056);
SB(); M16A(P[1][1], AF(3), hb[1][9]); SB();
afh[2] = TR(lbB0, 47104);
SB(); M16A(P[1][2], AF(3), hb[2][9]); SB();
GLDS(lvoffB, lbase, 1024);
SB(); M16A(P[1][3], AF(3), hb[3][9]); SB();
SB();
asm volatile("s_waitcnt vmcnt(16)\n\ts_barrier" ::: "memory");
SB(); M16A(P[0][0], AF(0), hb[0][10]); SB();
afl[3] = TR(lbB1, 45056);
SB(); M16A(P[0][1], AF(0), hb[1][10]); SB();
afh[3] = TR(lbB1, 47104);
SB(); M16A(P[0][2], AF(0), hb[2][10]); SB();
lbase = wdb + 1024;
SB(); M16A(P[0][3], AF(0), hb[3][10]); SB();
SB(); M16A(P[1][0], AF(1), hb[0][10]); SB();
afl[0] = TR(lbB0, 49152);
SB(); M16A(P[1][1], AF(1), hb[1][10]); SB();
afh[0] = TR(lbB0, 51200);
SB(); M16A(P[1][2], AF(1), hb[2][10]); SB();
SETM0(wv2k + 32768);
SB(); M16A(P[1][3], AF(1), hb[3][10]); SB();
GLDS(lvoffA, lbase, 0);
SB(); M16A(P[0][0], AF(2), hb[0][11]); SB();
afl[1] = TR(lbB1, 49152);
SB(); M16A(P[0][1], AF(2), hb[1][11]); SB();
afh[1] = TR(lbB1, 51200);
SB(); M16A(P[0][2], AF(2), hb[2][11]); SB();
SB(); M16A(P[0][3], AF(2), hb[3][11]); SB();
SB(); M16A(P[1][0], AF(3), hb[0][11]); SB();
afl[2] = TR(lbB0, 53248);
SB(); M16A(P[1][1], AF(3), hb[1][11]); SB();
afh[2] = TR(lbB0, 55296);
SB(); M16A(P[1][2], AF(3), hb[2][11]); SB();
GLDS(lvoffB, lbase, 1024);
SB(); M16A(P[1][3], AF(3), hb[3][11]); SB();
SB();
asm volatile("s_waitcnt vmcnt(16)\n\ts_barrier" ::: "memory");
SB(); M16A(P[0][0], AF(0), hb[0][12]); SB();
afl[3] = TR(lbB1, 53248);
SB(); M16A(P[0][1], AF(0), hb[1][12]); SB();
afh[3] = TR(lbB1, 55296);
SB(); M16A(P[0][2], AF(0), hb[2][12]); SB();
lbase = wdb + 74752;
SB(); M16A(P[0][3], AF(0), hb[3][12]); SB();
SB(); M16A(P[1][0], AF(1), hb[0][12]); SB();
afl[0] = TR(lbB0, 57344);
SB(); M16A(P[1][1], AF(1), hb[1][12]); SB();
afh[0] = TR(lbB0, 59392);
SB(); M16A(P[1][2], AF(1), hb[2][12]); SB();
SETM0(wv2k + 40960);
SB(); M16A(P[1][3], AF(1), hb[3][12]); SB();
GLDS(lvoffA, lbase, 0);
SB(); M16A(P[0][0], AF(2), hb[0][13]); SB();
afl[1] = TR(lbB1, 57344);
SB(); M16A(P[0][1], AF(2), hb[1][13]); SB();
afh[1] = TR(lbB1, 59392);
SB(); M16A(P[0][2], AF(2), hb[2][13]); SB();
SB(); M16A(P[0][3], AF(2), hb[3][13]); SB();
SB(); M16A(P[1][0], AF(3), hb[0][13]); SB();
afl[2] = TR(lbB0, 61440);
SB(); M16A(P[1][1], AF(3), hb[1][13]); SB();
afh[2] = TR(lbB0, 63488);
SB(); M16A(P[1][2], AF(3), hb[2][13]); SB();
GLDS(lvoffB, lbase, 1024);
SB(); M16A(P[1][3], AF(3), hb[3][13]); SB();
SB();
asm volatile("s_waitcnt vmcnt(16)\n\ts_barrier" ::: "memory");
SB(); M16A(P[0][0], AF(0), hb[0][14]); SB();
afl[3] = TR(lbB1, 61440);
SB(); M16A(P[0][1], AF(0), hb[1][14]); SB();
afh[3] = TR(lbB1, 63488);
SB(); M16A(P[0][2], AF(0), hb[2][14]); SB();
lbase = wdb + 148480;
SB(); M16A(P[0][3], AF(0), hb[3][14]); SB();
SB(); M16A(P[1][0], AF(1), hb[0][14]); SB();
afl[0] = TR(lbB0, 65536);
SB(); M16A(P[1][1], AF(1), hb[1][14]); SB();
afh[0] = TR(lbB0, 67584);
SB(); M16A(P[1][2], AF(1), hb[2][14]); SB();
SETM0(wv2k + 49152);
SB(); M16A(P[1][3], AF(1), hb[3][14]); SB();
GLDS(lvoffA, lbase, 0);
SB(); M16V(P[0][0], AF(2), hb[0][15]); SB();
afl[1] = TR(lbB1, 65536);
SB(); M16V(P[0][1], AF(2), hb[1][15]); SB();
afh[1] = TR(lbB1, 67584);
SB(); M16V(P[0][2], AF(2), hb[2][15]); SB();
SB(); M16V(P[0][3], AF(2), hb[3][15]); SB();
SB(); M16V(P[1][0], AF(3), hb[0][15]); SB();
afl[2] = TR(lbB0, 69632);
SB(); M16V(P[1][1], AF(3), hb[1][15]); SB();
afh[2] = TR(lbB0, 71680);
SB(); M16V(P[1][2], AF(3), hb[2][15]); SB();
GLDS(lvoffB, lbase, 1024);
SB(); M16V(P[1][3], AF(3), hb[3][15]); SB();
SB();
asm volatile("s_waitcnt vmcnt(15)\n\ts_barrier" ::: "memory");
SB(); M16V(P[0][0], AF(0), hb[0][16]); SB();
afl[3] = TR(lbB1, 69632);
SB(); M16V(P[0][1], AF(0), hb[1][16]); SB();
afh[3] = TR(lbB1, 71680);
SB(); M16V(P[0][2], AF(0), hb[2][16]); SB();
lbase = wdb + 222208;
SB(); M16V(P[0][3], AF(0), hb[3][16]); SB();
SB(); M16V(P[1][0], AF(1), hb[0][16]); SB();
afl[0] = TR(lbB0, 0);
SB(); M16V(P[1][1], AF(1), hb[1][16]); SB();
afh[0] = TR(lbB0, 2048);
SB(); M16V(P[1][2], AF(1), hb[2][16]); SB();
SETM0(wv2k + 57344);
SB(); M16V(P[1][3], AF(1), hb[3][16]); SB();
GLDS(lvoffA, lbase, 0);
SB(); M16V(P[0][0], AF(2), hb[0][17]); SB();
afl[1] = TR(lbB1, 0);
SB(); M16V(P[0][1], AF(2), hb[1][17]); SB();
afh[1] = TR(lbB1, 2048);
SB(); M16V(P[0][2], AF(2), hb[2][17]); SB();
SB(); M16V(P[0][3], AF(2), hb[3][17]); SB();
SB(); M16V(P[1][0], AF(3), hb[0][17]); SB();
afl[2] = TR(lbB0, 4096);
SB(); M16V(P[1][1], AF(3), hb[1][17]); SB();
afh[2] = TR(lbB0, 6144);
SB(); M16V(P[1][2], AF(3), hb[2][17]); SB();
GLDS(lvoffB, lbase, 1024);
SB(); M16V(P[1][3], AF(3), hb[3][17]); SB();
SB();
asm volatile("s_waitcnt vmcnt(14)\n\ts_barrier" ::: "memory");
SB(); M16ZA(R[0][0], AF(0), hb[0][0]); SB();
afl[3] = TR(lbB1, 4096);
SB(); M16ZA(R[0][1], AF(0), hb[1][0]); SB();
afh[3] = TR(lbB1, 6144);
SB(); M16ZA(R[0][2], AF(0), hb[2][0]); SB();
lbase = wdb + 295936;
SB(); M16ZA(R[0][3], AF(0), hb[3][0]); SB();
SB(); M16ZA(R[1][0], AF(1), hb[0][0]); SB();
afl[0] = TR(lbB0, 8192);
SB(); M16ZA(R[1][1], AF(1), hb[1][0]); SB();
afh[0] = TR(lbB0, 10240);
SB(); M16ZA(R[1][2], AF(1), hb[2][0]); SB();
SETM0(wv2k + 65536);
SB(); M16ZA(R[1][3], AF(1), hb[3][0]); SB();
GLDS(lvoffA, lbase, 0);
SB(); M16A(R[0][0], AF(2), hb[0][1]); SB();
afl[1] = TR(lbB1, 8192);
SB(); M16A(R[0][1], AF(2), hb[1][1]); SB();
afh[1] = TR(lbB1, 10240);
SB(); M16A(R[0][2], AF(2), hb[2][1]); SB();
{ unsigned u = pk_bf16(P[0][0][0], P[0][0][1]); PIN(u); hh[0] = u; }
{ unsigned u = pk_bf16(P[0][0][2], P[0][0][3]); PIN(u); hh[1] = u; }
SB(); M16A(R[0][3], AF(2), hb[3][1]); SB();
{ unsigned u = pk_bf16(P[1][0][0], P[1][0][1]); PIN(u); hh[2] = u; }
{ unsigned u = pk_bf16(P[1][0][2], P[1][0][3]); PIN(u); hh[3] = u; }
SB(); M16A(R[1][0], AF(3), hb[0][1]); SB();
afl[2] = TR(lbB0, 12288);
SB(); M16A(R[1][1], AF(3), hb[1][1]); SB();
afh[2] = TR(lbB0, 14336);
SB(); M16A(R[1][2], AF(3), hb[2][1]); SB();
GLDS(lvoffB, lbase, 1024);
SB(); M16A(R[1][3], AF(3), hb[3][1]); SB();
SB();
asm volatile("s_waitcnt vmcnt(13)\n\ts_barrier" ::: "memory");
SB(); M16A(R[0][0], AF(0), hb[0][2]); SB();
afl[3] = TR(lbB1, 12288);
SB(); M16A(R[0][1], AF(0), hb[1][2]); SB();
afh[3] = TR(lbB1, 14336);
SB(); M16A(R[0][2], AF(0), hb[2][2]); SB();
lbase = a.wd; lvoffA = 0; lvoffB = 0;
SB(); M16A(R[0][3], AF(0), hb[3][2]); SB();
PST(pvo, hh, pbase0 + 129024);
SB(); M16A(R[1][0], AF(1), hb[0][2]); SB();
afl[0] = TR(lbB0, 16384);
SB(); M16A(R[1][1], AF(1), hb[1][2]); SB();
afh[0] = TR(lbB0, 18432);
SB(); M16A(R[1][2], AF(1), hb[2][2]); SB();
SETM0(wv2k + 0);
{ unsigned u = pk_bf16(P[0][1][0], P[0][1][1]); PIN(u); hh[0] = u; }
SB(); M16A(R[1][3], AF(1), hb[3][2]); SB();
GLDS(lvoffA, lbase, 0);
SB(); M16A(R[0][0], AF(2), hb[0][3]); SB();
afl[1] = TR(lbB1, 16384);
SB(); M16A(R[0][1], AF(2), hb[1][3]); SB();
afh[1] = TR(lbB1, 18432);
SB(); M16A(R[0][2], AF(2), hb[2][3]); SB();
{ unsigned u = pk_bf16(P[0][1][2], P[0][1][3]); PIN(u); hh[1] = u; }
{ unsigned u = pk_bf16(P[1][1][0], P[1][1][1]); PIN(u); hh[2] = u; }
SB(); M16A(R[0][3], AF(2), hb[3][3]); SB();
{ unsigned u = pk_bf16(P[1][1][2], P[1][1][3]); PIN(u); hh[3] = u; }
SB(); M16A(R[1][0], AF(3), hb[0][3]); SB();
afl[2] = TR(lbB0, 20480);
SB(); M16A(R[1][1], AF(3), hb[1][3]); SB();
afh[2] = TR(lbB0, 22528);
SB(); M16A(R[1][2], AF(3), hb[2][3]); SB();
GLDS(lvoffB, lbase, 1024);
SB(); M16A(R[1][3], AF(3), hb[3][3]); SB();
SB();
asm volatile("s_waitcnt vmcnt(13)\n\ts_barrier" ::: "memory");
SB(); M16A(R[0][0], AF(0), hb[0][4]); SB();
afl[3] = TR(lbB1, 20480);
SB(); M16A(R[0][1], AF(0), hb[1][4]); SB();
afh[3] = TR(lbB1, 22528);
SB(); M16A(R[0][2], AF(0), hb[2][4]); SB();
SB(); M16A(R[0][3], AF(0), hb[3][4]); SB();
PST(pvo, hh, pbase0 + 294912);
SB(); M16A(R[1][0], AF(1), hb[0][4]); SB();
afl[0] = TR(lbB0, 24576);
SB(); M16A(R[1][1], AF(1), hb[1][4]); SB();
afh[0] = TR(lbB0, 26624);
SB(); M16A(R[1][2], AF(1), hb[2][4]); SB();
SETM0(wv2k + 8192);
{ unsigned u = pk_bf16(P[0][2][0], P[0][2][1]); PIN(u); hh[0] = u; }
SB(); M16A(R[1][3], AF(1), hb[3][4]); SB();
GLDS(lvoffA, lbase, 0);
SB(); M16A(R[0][0], AF(2), hb[0][5]); SB();
afl[1] = TR(lbB1, 24576);
SB(); M16A(R[0][1], AF(2), hb[1][5]); SB();
afh[1] = TR(lbB1, 26624);
SB(); M16A(R[0][2], AF(2), hb[2][5]); SB();
{ unsigned u = pk_bf16(P[0][2][2], P[0][2][3]); PIN(u); hh[1] = u; }
{ unsigned u = pk_bf16(P[1][2][0], P[1][2][1]); PIN(u); hh[2] = u; }
SB(); M16A(R[0][3], AF(2), hb[3][5]); SB();
{ unsigned u = pk_bf16(P[1][2][2], P[1][2][3]); PIN(u); hh[3] = u; }
SB(); M16A(R[1][0], AF(3), hb[0][5]); SB();
afl[2] = TR(lbB0, 28672);
SB(); M16A(R[1][1], AF(3), hb[1][5]); SB();
afh[2] = TR(lbB0, 30720);
SB(); M16A(R[1][2], AF(3), hb[2][5]); SB();
GLDS(lvoffB, lbase, 1024);
SB(); M16A(R[1][3], AF(3), hb[3][5]); SB();
SB();
asm volatile("s_waitcnt vmcnt(14)\n\ts_barrier" ::: "memory");
SB(); M16A(R[0][0], AF(0), hb[0][6]); SB();
afl[3] = TR(lbB1, 28672);
SB(); M16A(R[0][1], AF(0), hb[1][6]); SB();
afh[3] = TR(lbB1, 30720);
SB(); M16A(R[0][2], AF(0), hb[2][6]); SB();
SB(); M16A(R[0][3], AF(0), hb[3][6]); SB();
PST(pvo, hh, pbase0 + 460800);
SB(); M16A(R[1][0], AF(1), hb[0][6]); SB();
afl[0] = TR(lbB0, 32768);
SB(); M16A(R[1][1], AF(1), hb[1][6]); SB();
afh[0] = TR(lbB0, 34816);
SB(); M16A(R[1][2], AF(1), hb[2][6]); SB();
SETM0(wv2k + 16384);
{ unsigned u = pk_bf16(P[0][3][0], P[0][3][1]); PIN(u); hh[0] = u; }
SB(); M16A(R[1][3], AF(1), hb[3][6]); SB();
GLDS(lvoffA, lbase, 0);
SB(); M16A(R[0][0], AF(2), hb[0][7]); SB();
afl[1] = TR(lbB1, 32768);
SB(); M16A(R[0][1], AF(2), hb[1][7]); SB();
afh[1] = TR(lbB1, 34816);
SB(); M16A(R[0][2], AF(2), hb[2][7]); SB();
{ unsigned u = pk_bf16(P[0][3][2], P[0][3][3]); PIN(u); hh[1] = u; }
{ unsigned u = pk_bf16(P[1][3][0], P[1][3][1]); PIN(u); hh[2] = u; }
SB(); M16A(R[0][3], AF(2), hb[3][7]); SB();
{ unsigned u = pk_bf16(P[1][3][2], P[1][3][3]); PIN(u); hh[3] = u; }
SB(); M16A(R[1][0], AF(3), hb[0][7]); SB();
afl[2] = TR(lbB0, 36864);
SB(); M16A(R[1][1], AF(3), hb[1][7]); SB();
afh[2] = TR(lbB0, 38912);
SB(); M16A(R[1][2], AF(3), hb[2][7]); SB();
GLDS(lvoffB, lbase, 1024);
SB(); M16A(R[1][3], AF(3), hb[3][7]); SB();
SB();
asm volatile("s_waitcnt vmcnt(15)\n\ts_barrier" ::: "memory");
SB(); M16A(R[0][0], AF(0), hb[0][8]); SB();
afl[3] = TR(lbB1, 36864);
SB(); M16A(R[0][1], AF(0), hb[1][8]); SB();
afh[3] = TR(lbB1, 38912);
SB(); M16A(R[0][2], AF(0), hb[2][8]); SB();
SB(); M16A(R[0][3], AF(0), hb[3][8]); SB();
PST(pvo, hh, pbase0 + 626688);
SB(); M16A(R[1][0], AF(1), hb[0][8]); SB();
afl[0] = TR(lbB0, 40960);
SB(); M16A(R[1][1], AF(1), hb[1][8]); SB();
afh[0] = TR(lbB0, 43008);
SB(); M16A(R[1][2], AF(1), hb[2][8]); SB();
SETM0(wv2k + 24576);
SB(); M16A(R[1][3], AF(1), hb[3][8]); SB();
GLDS(lvoffA, lbase, 0);
SB(); M16A(R[0][0], AF(2), hb[0][9]); SB();
afl[1] = TR(lbB1, 40960);
SB(); M16A(R[0][1], AF(2), hb[1][9]); SB();
afh[1] = TR(lbB1, 43008);
SB(); M16A(R[0][2], AF(2), hb[2][9]); SB();
SB(); M16A(R[0][3], AF(2), hb[3][9]); SB();
SB(); M16A(R[1][0], AF(3), hb[0][9]); SB();
afl[2] = TR(lbB0, 45056);
SB(); M16A(R[1][1], AF(3), hb[1][9]); SB();
afh[2] = TR(lbB0, 47104);
SB(); M16A(R[1][2], AF(3), hb[2][9]); SB();
GLDS(lvoffB, lbase, 1024);
SB(); M16A(R[1][3], AF(3), hb[3][9]); SB();
SB();
asm volatile("s_waitcnt vmcnt(16)\n\ts_barrier" ::: "memory");
SB(); M16A(R[0][0], AF(0), hb[0][10]); SB();
afl[3] = TR(lbB1, 45056);
SB(); M16A(R[0][1], AF(0), hb[1][10]); SB();
afh[3] = TR(lbB1, 47104);
SB(); M16A(R[0][2], AF(0), hb[2][10]); SB();
SB(); M16A(R[0][3], AF(0), hb[3][10]); SB();
SB(); M16A(R[1][0], AF(1), hb[0][10]); SB();
afl[0] = TR(lbB0, 49152);
SB(); M16A(R[1][1], AF(1), hb[1][10]); SB();
afh[0] = TR(lbB0, 51200);
SB(); M16A(R[1][2], AF(1), hb[2][10]); SB();
SETM0(wv2k + 32768);
SB(); M16A(R[1][3], AF(1), hb[3][10]); SB();
GLDS(lvoffA, lbase, 0);
SB(); M16A(R[0][0], AF(2), hb[0][11]); SB();
afl[1] = TR(lbB1, 49152);
SB(); M16A(R[0][1], AF(2), hb[1][11]); SB();
afh[1] = TR(lbB1, 51200);
SB(); M16A(R[0][2], AF(2), hb[2][11]); SB();
SB(); M16A(R[0][3], AF(2), hb[3][11]); SB();
SB(); M16A(R[1][0], AF(3), hb[0][11]); SB();
afl[2] = TR(lbB0, 53248);
SB(); M16A(R[1][1], AF(3), hb[1][11]); SB();
afh[2] = TR(lbB0, 55296);
SB(); M16A(R[1][2], AF(3), hb[2][11]); SB();
GLDS(lvoffB, lbase, 1024);
SB(); M16A(R[1][3], AF(3), hb[3][11]); SB();
SB();
asm volatile("s_waitcnt vmcnt(4)\n\ts_barrier" ::: "memory");
SIGNAL_ADD(fP + 32, 1, true);
SB(); M16A(R[0][0], AF(0), hb[0][12]); SB();
afl[3] = TR(lbB1, 53248);
SB(); M16A(R[0][1], AF(0), hb[1][12]); SB();
afh[3] = TR(lbB1, 55296);
SB(); M16A(R[0][2], AF(0), hb[2][12]); SB();
SB(); M16A(R[0][3], AF(0), hb[3][12]); SB();
SB(); M16A(R[1][0], AF(1), hb[0][12]); SB();
afl[0] = TR(lbB0, 57344);
SB(); M16A(R[1][1], AF(1), hb[1][12]); SB();
afh[0] = TR(lbB0, 59392);
SB(); M16A(R[1][2], AF(1), hb[2][12]); SB();
SETM0(wv2k + 40960);
SB(); M16A(R[1][3], AF(1), hb[3][12]); SB();
GLDS(lvoffA, lbase, 0);
SB(); M16A(R[0][0], AF(2), hb[0][13]); SB();
afl[1] = TR(lbB1, 57344);
SB(); M16A(R[0][1], AF(2), hb[1][13]); SB();
afh[1] = TR(lbB1, 59392);
SB(); M16A(R[0][2], AF(2), hb[2][13]); SB();
SB(); M16A(R[0][3], AF(2), hb[3][13]); SB();
SB(); M16A(R[1][0], AF(3), hb[0][13]); SB();
afl[2] = TR(lbB0, 61440);
SB(); M16A(R[1][1], AF(3), hb[1][13]); SB();
afh[2] = TR(lbB0, 63488);
SB(); M16A(R[1][2], AF(3), hb[2][13]); SB();
GLDS(lvoffB, lbase, 1024);
SB(); M16A(R[1][3], AF(3), hb[3][13]); SB();
SB();
asm volatile("s_waitcnt vmcnt(16)\n\ts_barrier" ::: "memory");
SB(); M16A(R[0][0], AF(0), hb[0][14]); SB();
afl[3] = TR(lbB1, 61440);
SB(); M16A(R[0][1], AF(0), hb[1][14]); SB();
afh[3] = TR(lbB1, 63488);
SB(); M16A(R[0][2], AF(0), hb[2][14]); SB();
SB(); M16A(R[0][3], AF(0), hb[3][14]); SB();
SB(); M16A(R[1][0], AF(1), hb[0][14]); SB();
afl[0] = TR(lbB0, 65536);
SB(); M16A(R[1][1], AF(1), hb[1][14]); SB();
afh[0] = TR(lbB0, 67584);
SB(); M16A(R[1][2], AF(1), hb[2][14]); SB();
SETM0(wv2k + 49152);
SB(); M16A(R[1][3], AF(1), hb[3][14]); SB();
GLDS(lvoffA, lbase, 0);
SB(); M16V(R[0][0], AF(2), hb[0][15]); SB();
afl[1] = TR(lbB1, 65536);
SB(); M16V(R[0][1], AF(2), hb[1][15]); SB();
afh[1] = TR(lbB1, 67584);
SB(); M16V(R[0][2], AF(2), hb[2][15]); SB();
SB(); M16V(R[0][3], AF(2), hb[3][15]); SB();
SB(); M16V(R[1][0], AF(3), hb[0][15]); SB();
afl[2] = TR(lbB0, 69632);
SB(); M16V(R[1][1], AF(3), hb[1][15]); SB();
afh[2] = TR(lbB0, 71680);
SB(); M16V(R[1][2], AF(3), hb[2][15]); SB();
GLDS(lvoffB, lbase, 1024);
SB(); M16V(R[1][3], AF(3), hb[3][15]); SB();
SB();
asm volatile("s_waitcnt vmcnt(15)\n\ts_barrier" ::: "memory");
SB(); M16V(R[0][0], AF(0), hb[0][16]); SB();
afl[3] = TR(lbB1, 69632);
SB(); M16V(R[0][1], AF(0), hb[1][16]); SB();
afh[3] = TR(lbB1, 71680);
SB(); M16V(R[0][2], AF(0), hb[2][16]); SB();
SB(); M16V(R[0][3], AF(0), hb[3][16]); SB();
SB(); M16V(R[1][0], AF(1), hb[0][16]); SB();
afl[0] = TR(lbB0, 0);
SB(); M16V(R[1][1], AF(1), hb[1][16]); SB();
afh[0] = TR(lbB0, 2048);
SB(); M16V(R[1][2], AF(1), hb[2][16]); SB();
SETM0(wv2k + 57344);
SB(); M16V(R[1][3], AF(1), hb[3][16]); SB();
GLDS(lvoffA, lbase, 0);
SB(); M16V(R[0][0], AF(2), hb[0][17]); SB();
afl[1] = TR(lbB1, 0);
SB(); M16V(R[0][1], AF(2), hb[1][17]); SB();
afh[1] = TR(lbB1, 2048);
SB(); M16V(R[0][2], AF(2), hb[2][17]); SB();
SB(); M16V(R[0][3], AF(2), hb[3][17]); SB();
SB(); M16V(R[1][0], AF(3), hb[0][17]); SB();
afl[2] = TR(lbB0, 4096);
SB(); M16V(R[1][1], AF(3), hb[1][17]); SB();
afh[2] = TR(lbB0, 6144);
SB(); M16V(R[1][2], AF(3), hb[2][17]); SB();
GLDS(lvoffB, lbase, 1024);
SB(); M16V(R[1][3], AF(3), hb[3][17]); SB();
STAMP(10); JIT(10);
  const char* rbase = (a.ws + WS_PART) + ((size_t)(((m * 8 + (s >> 1)) * 18 + (s & 1)) * NSL)) * 1024;
  const int rdp0 = (wave & 1) + 4 * (wave >> 1);
  const int rdp1 = wave == 0 ? 2 : (wave == 3 ? 3 : 5 + wave);
  const unsigned ln3 = lane_id();
  const unsigned xoff = (unsigned)((m * 128 + (s >> 1) * 16 + (ln3 & 15)) * 1152 + ((s & 1) * 32 + 4 * (ln3 >> 4)) * 2);
  u32x2 x0[2], x1[2], x2[2];
  unsigned z0, z1, z2;
  #define LDX(dst, dp, q) asm volatile("global_load_dwordx2 %0, %1, %2" : "=v"(dst) : "v"(xoff), "s"(a.x + (dp) * 128 + (q) * 32) : "memory")
  LDW1(z0, zvo, fP); LDW1(z1, zvo, fP + 32); LDW1(z2, zvo, fP + 64);
  LDX(x0[0], rdp0, 0); LDX(x0[1], rdp0, 1); LDX(x1[0], rdp1, 0); LDX(x1[1], rdp1, 1); LDX(x2[0], 8, 0); LDX(x2[1], 8, 1);

MNOP();
{ unsigned u = pk_bf16(R[0][0][0], R[0][0][1]); PIN(u); hh[0] = u; }
{ unsigned u = pk_bf16(R[0][0][2], R[0][0][3]); PIN(u); hh[1] = u; }
{ unsigned u = pk_bf16(R[1][0][0], R[1][0][1]); PIN(u); hh[2] = u; }
{ unsigned u = pk_bf16(R[1][0][2], R[1][0][3]); PIN(u); hh[3] = u; }
PST(pvo, hh, pbase0 + 147456);
{ unsigned u = pk_bf16(R[0][1][0], R[0][1][1]); PIN(u); hh[0] = u; }
{ unsigned u = pk_bf16(R[0][1][2], R[0][1][3]); PIN(u); hh[1] = u; }
{ unsigned u = pk_bf16(R[1][1][0], R[1][1][1]); PIN(u); hh[2] = u; }
{ unsigned u = pk_bf16(R[1][1][2], R[1][1][3]); PIN(u); hh[3] = u; }
PST(pvo, hh, pbase0 + 313344);
{ unsigned u = pk_bf16(R[0][2][0], R[0][2][1]); PIN(u); hh[0] = u; }
{ unsigned u = pk_bf16(R[0][2][2], R[0][2][3]); PIN(u); hh[1] = u; }
{ unsigned u = pk_bf16(R[1][2][0], R[1][2][1]); PIN(u); hh[2] = u; }
{ unsigned u = pk_bf16(R[1][2][2], R[1][2][3]); PIN(u); hh[3] = u; }
PST(pvo, hh, pbase0 + 479232);
{ unsigned u = pk_bf16(R[0][3][0], R[0][3][1]); PIN(u); hh[0] = u; }
{ unsigned u = pk_bf16(R[0][3][2], R[0][3][3]); PIN(u); hh[1] = u; }
{ unsigned u = pk_bf16(R[1][3][0], R[1][3][1]); PIN(u); hh[2] = u; }
{ unsigned u = pk_bf16(R[1][3][2], R[1][3][3]); PIN(u); hh[3] = u; }
PST(pvo, hh, pbase0 + 645120);
  {
    asm volatile("s_waitcnt vmcnt(0)\n\ts_barrier" : "+v"(z0), "+v"(z1), "+v"(z2), "+v"(x0[0]), "+v"(x0[1]), "+v"(x1[0]), "+v"(x1[1]), "+v"(x2[0]), "+v"(x2[1]) :: "memory");
    SIGNAL_ADD(fP + 64, 1, true);
    STAMP(12);
    const __amdgpu_buffer_rsrc_t rrs = __builtin_amdgcn_make_buffer_rsrc((void*)rbase, 0, 18 * NSL * 1024, 0x00020000);
    auto load_unit = [&](u32x4 (&v)[NSL], int dp) {
      #pragma unroll
      for (int i = 0; i < NSL; ++i) v[i] = __builtin_amdgcn_raw_buffer_load_b128(rrs, pvo + i * 1024, dp * (2 * NSL * 1024), 16);
    };
    auto finish_unit = [&](u32x4 (&v)[NSL], const u32x2 (&xv)[2], int dp) {
      float acc[8];
      #pragma unroll
      for (int i = 0; i < 8; ++i) acc[i] = 0.f;
      #pragma unroll
      for (int sl = 0; sl < 8; ++sl)
        #pragma unroll
        for (int i = 0; i < 4; ++i) {
          unsigned u = v[sl][i];
          acc[2 * i] += bflo(u);
          acc[2 * i + 1] += bfhi(u);
        }
      #pragma unroll
      for (int q = 0; q < 2; ++q) {
        float r[4];
        #pragma unroll
        for (int i = 0; i < 4; ++i) {
          float d = rb(acc[4 * q + i]);
          float gg = rb(d * __builtin_amdgcn_rcpf(1.0f + __builtin_amdgcn_exp2f(-1.4426950408889634f * d)));
          unsigned xu = xv[q][i >> 1], su = v[8][2 * q + (i >> 1)];
          float xx = (i & 1) ? bfhi(xu) : bflo(xu);
          float ss = (i & 1) ? bfhi(su) : bflo(su);
          r[i] = xx + rb(ss * gg);
        }
        u32x2 o; o[0] = pk_bf16(r[0], r[1]); o[1] = pk_bf16(r[2], r[3]);
        *(u32x2*)(a.out + xoff + dp * 128 + q * 32) = o;
      }
    };
    u32x4 rv[NSL], rw[NSL], ru[NSL];
    CHECK_GE(z0, fP, 25); load_unit(rv, rdp0);
    if (wave < 3) { CHECK_GE(z1, fP + 32, 16); load_unit(rw, rdp1); }
    STAMP(13);
    const bool last2 = (wave == 1 || wave == 3);
    const int rdp2 = wave == 1 ? 8 : 3;
    z2 += 1;
    const bool rdy2 = __builtin_amdgcn_readfirstlane(z2) >= 16u;
    if (last2 && rdy2) load_unit(ru, rdp2);
    finish_unit(rv, x0, rdp0);
    if (wave < 3) finish_unit(rw, x1, rdp1);
    STAMP(14);
    if (last2 && !rdy2) { z2 = 0; CHECK_GE0(z2, fP + 64, 16); load_unit(ru, rdp2); }
    STAMP(15);
    if (last2) finish_unit(ru, wave == 1 ? x2 : x1, rdp2);
  }

asm volatile(".Lk1_tail1:");
  asm volatile("s_waitcnt vmcnt(0)" ::: "memory");
  STAMP(11);

}

__global__ __launch_bounds__(256, 1) void k1(KArgs a) {
  __shared__ __attribute__((aligned(16))) char smem[LDS_BYTES];
  asm volatile("" :: "v"((AS3 char*)smem) : "memory");
  const char* code_base; unsigned code_len;
  asm volatile("s_getpc_b64 %0\n.Lk1_pc:\n\ts_mov_b32 %1, .Lk1_tail0-.Lk1_pc-4\n\ts_max_i32 %1, %1, .Lk1_tail1-.Lk1_pc-4\n\ts_max_i32 %1, %1, 0" : "=s"(code_base), "=s"(code_len) :: "scc");
  const int bid = blockIdx.x;
  const int j = bid & 7, idx = bid >> 3;
  const int m = idx >> 1;
  const int wave = __builtin_amdgcn_readfirstlane(threadIdx.x >> 6);
  if ((idx & 1) == 0) body0(a, m, j, 1, j, 1, m & 7, 2 * j + (m >> 3), wave, code_base, code_len);
  else body1(a, m, j, j < 4 ? 3 : (j < 7 ? 2 : 1), 8 + (j < 4 ? 3 * j : 12 + 2 * (j - 4)), j == 7 ? 1 : 0, 8, m, wave, code_base, code_len);
}

extern "C" void kernel_launch(void* const* d_in, const int* in_sizes, int n_in,
                              void* d_out, int out_size, void* d_ws, size_t ws_size,
                              hipStream_t stream) {
  char* ws=(char*)d_ws;
  KArgs a;
  a.ws=ws; a.x=(const char*)d_in[0]; a.out=(char*)d_out;
  a.wqkv=(const char*)d_in[1]; a.wo=(const char*)d_in[2];
  a.wg=(const char*)d_in[4]; a.wu=(const char*)d_in[5]; a.wd=(const char*)d_in[6];
  K0Args ka;
  ka.x=a.x; ka.xn=ws+WS_XN; ka.flags=(unsigned*)(ws+WS_FLAGS); ka.wqkv=a.wqkv; ka.wg=a.wg; ka.wu=a.wu; ka.wo=a.wo;
  ka.sink=(unsigned*)(ws+WS_SINK); ka.ts=(unsigned long long*)(ws+WS_TS);
  k0<<<256,192,0,stream>>>(ka);
  k1<<<256,256,0,stream>>>(a);
}
```
